```python
import torch

def _softmax(x, axis=-1):
    return torch.softmax(x, dim=axis)

def _gelu(x):
    return torch.nn.functional.gelu(x, approximate='tanh')

def _silu(x):
    return torch.nn.functional.silu(x)

def _sigmoid(x):
    return torch.sigmoid(x)

def _layernorm(x, axis=-1, eps=1e-05):
    m = x.mean(dim=axis, keepdim=True)
    v = x.var(dim=axis, keepdim=True, unbiased=False)
    return (x - m) / torch.sqrt(v + eps)

def _scatter_add(tgt, idx, src, axis=0):
    return tgt.clone().index_add_(axis, idx, src)

def _segment_sum(src, seg, n):
    out = torch.zeros((n,) + src.shape[1:], dtype=src.dtype)
    return out.index_add_(0, seg, src)

def _pad(x, pw):
    flat = tuple((v for pair in reversed(pw) for v in pair))
    return torch.nn.functional.pad(x, flat)
kernel_name = 'standalone__conformer_family_branchformer__speech'

def setup_inputs():

    _w = lambda a: (lambda t: t.to(torch.bfloat16) if t.is_floating_point() else t)(torch.as_tensor(a))
    B, T, D, K = (2, 4000, 144, 31)
    s = float(D) ** (-0.5)
    return {'x': _w((lambda *s: torch.randn(*s, dtype=torch.bfloat16) * 0.05)(B, T, D)), 'w_q': _w((lambda *s: torch.randn(*s, dtype=torch.bfloat16) * 1.6)(D, D) * s), 'w_k': _w((lambda *s: torch.randn(*s, dtype=torch.bfloat16) * 1.6)(D, D) * s), 'w_v': _w((lambda *s: torch.randn(*s, dtype=torch.bfloat16) * 0.8)(D, D) * s), 'w_ao': _w((lambda *s: torch.randn(*s, dtype=torch.bfloat16) * 0.8)(D, D) * s), 'w_up': _w((lambda *s: torch.randn(*s, dtype=torch.bfloat16) * 1.6)(D, 2 * D) * s), 'dw': _w((lambda *s: torch.randn(*s, dtype=torch.bfloat16) * 1.6)(K, D) * (1.0 / K)), 'w_dn': _w((lambda *s: torch.randn(*s, dtype=torch.bfloat16) * 0.8)(D, D) * s), 'w_merge': _w((lambda *s: torch.randn(*s, dtype=torch.bfloat16) * 0.8)(2 * D, D) * s)}

def reference(x, w_q, w_k, w_v, w_ao, w_up, dw, w_dn, w_merge):
    B, T, D = x.shape
    K = dw.shape[0]
    xa = _layernorm(x, axis=-1)
    q = torch.einsum('btd,de->bte', xa, w_q)
    k = torch.einsum('btd,de->bte', xa, w_k)
    v = torch.einsum('btd,de->bte', xa, w_v)
    logits = torch.einsum('btd,bsd->bts', q, k) * float(D) ** (-0.5)
    attn = _softmax(logits, axis=-1)
    ya = torch.einsum('bts,bsd->btd', attn, v)
    ya = torch.einsum('btd,de->bte', ya, w_ao)
    xc = _layernorm(x, axis=-1)
    h = _gelu(torch.einsum('btd,de->bte', xc, w_up))
    u = h[..., :D].clone()
    g = _layernorm(h[..., D:], axis=-1)
    pad = K // 2
    gp = _pad(g, ((0, 0), (pad, pad), (0, 0)))
    conv = torch.zeros_like(g)
    for i in range(K):
        conv = conv + gp[:, i:i + T, :] * dw[i][None, None, :]
    yc = torch.einsum('btd,de->bte', u * conv, w_dn)
    merged = torch.concatenate([ya, yc], axis=-1)
    out = torch.einsum('btf,fd->btd', merged, w_merge)
    return x + out
```

```cpp
#include <hip/hip_runtime.h>
#include <hip/hip_bf16.h>
typedef __hip_bfloat16 bf16;
constexpr int B=2,T=4000,D=144,KC=31;
__device__ inline float bf(const bf16 x){return __bfloat162float(x);}
__global__ __launch_bounds__(320) void k_pre(const bf16* x,const bf16* wq,const bf16* wk,const bf16* wv,const bf16* wup,float* q,float* k,float* v,float* u,float* g){
  __shared__ float xa[D]; __shared__ float h[2*D]; __shared__ float red[4];
  int t=blockIdx.x; int tid=threadIdx.x;
  if(tid==0){float m=0;for(int d=0;d<D;d++)m+=bf(x[t*D+d]);m/=D;float vv=0;for(int d=0;d<D;d++){float e=bf(x[t*D+d])-m;vv+=e*e;}vv/=D;red[0]=m;red[1]=1.f/sqrtf(vv+1e-5f);}
  __syncthreads();
  if(tid<D)xa[tid]=(bf(x[t*D+tid])-red[0])*red[1];
  __syncthreads();
  if(tid<D){float a=0,b=0,c=0;for(int d=0;d<D;d++){a+=xa[d]*bf(wq[d*D+tid]);b+=xa[d]*bf(wk[d*D+tid]);c+=xa[d]*bf(wv[d*D+tid]);}q[t*D+tid]=a;k[t*D+tid]=b;v[t*D+tid]=c;}
  if(tid<2*D){float a=0;for(int d=0;d<D;d++)a+=xa[d]*bf(wup[d*2*D+tid]);float th=tanhf(0.7978845608028654f*(a+0.044715f*a*a*a));h[tid]=0.5f*a*(1.f+th);}
  __syncthreads();
  if(tid==0){float m=0;for(int d=0;d<D;d++)m+=h[D+d];m/=D;float vv=0;for(int d=0;d<D;d++){float e=h[D+d]-m;vv+=e*e;}vv/=D;red[2]=m;red[3]=1.f/sqrtf(vv+1e-5f);}
  __syncthreads();
  if(tid<D){u[t*D+tid]=h[tid];g[t*D+tid]=(h[D+tid]-red[2])*red[3];}
}
__global__ __launch_bounds__(256) void k_attn(const float* q,const float* k,const float* v,float* ya){
  __shared__ float s[T]; __shared__ float qs[D]; __shared__ float red[256];
  int t=blockIdx.x; int b=t/T; int tid=threadIdx.x;
  if(tid<D)qs[tid]=q[t*D+tid];
  __syncthreads();
  float mx=-1e30f;
  for(int j=tid;j<T;j+=256){float a=0;const float* kr=k+(size_t)(b*T+j)*D;for(int d=0;d<D;d++)a+=qs[d]*kr[d];a*=0.08333333333f;s[j]=a;mx=fmaxf(mx,a);}
  red[tid]=mx;__syncthreads();
  for(int o=128;o>0;o>>=1){if(tid<o)red[tid]=fmaxf(red[tid],red[tid+o]);__syncthreads();}
  mx=red[0];__syncthreads();
  float sum=0;for(int j=tid;j<T;j+=256){float e=__expf(s[j]-mx);s[j]=e;sum+=e;}
  red[tid]=sum;__syncthreads();
  for(int o=128;o>0;o>>=1){if(tid<o)red[tid]+=red[tid+o];__syncthreads();}
  sum=red[0];
  if(tid<D){float a=0;for(int j=0;j<T;j++)a+=s[j]*v[(size_t)(b*T+j)*D+tid];ya[t*D+tid]=a/sum;}
}
__global__ __launch_bounds__(192) void k_post(const bf16* x,const float* ya,const float* u,const float* g,const bf16* wao,const bf16* dw,const bf16* wdn,const bf16* wm,bf16* out){
  __shared__ float a[D],c[D],m[2*D];
  int t=blockIdx.x; int b=t/T; int tt=t%T; int tid=threadIdx.x;
  if(tid<D){a[tid]=ya[t*D+tid];float cv=0;for(int i=0;i<KC;i++){int s=tt+i-15;if(s>=0&&s<T)cv+=g[(size_t)(b*T+s)*D+tid]*bf(dw[i*D+tid]);}c[tid]=cv*u[t*D+tid];}
  __syncthreads();
  if(tid<D){float p=0,r=0;for(int d=0;d<D;d++){p+=a[d]*bf(wao[d*D+tid]);r+=c[d]*bf(wdn[d*D+tid]);}m[tid]=p;m[D+tid]=r;}
  __syncthreads();
  if(tid<D){float p=0;for(int d=0;d<2*D;d++)p+=m[d]*bf(wm[d*D+tid]);out[t*D+tid]=__float2bfloat16(bf(x[t*D+tid])+p);}
}
extern "C" void kernel_launch(void* const* d_in,const int* in_sizes,int n_in,void* d_out,int out_size,void* d_ws,size_t ws_size,hipStream_t stream){
  const bf16* x=(const bf16*)d_in[0];const bf16* wq=(const bf16*)d_in[1];const bf16* wk=(const bf16*)d_in[2];const bf16* wv=(const bf16*)d_in[3];
  const bf16* wao=(const bf16*)d_in[4];const bf16* wup=(const bf16*)d_in[5];const bf16* dw=(const bf16*)d_in[6];const bf16* wdn=(const bf16*)d_in[7];const bf16* wm=(const bf16*)d_in[8];
  float* ws=(float*)d_ws; size_t n=(size_t)B*T*D;
  float *q=ws,*k=ws+n,*v=ws+2*n,*u=ws+3*n,*g=ws+4*n,*ya=ws+5*n;
  k_pre<<<B*T,320,0,stream>>>(x,wq,wk,wv,wup,q,k,v,u,g);
  k_attn<<<B*T,256,0,stream>>>(q,k,v,ya);
  k_post<<<B*T,192,0,stream>>>(x,ya,u,g,wao,dw,wdn,wm,(bf16*)d_out);
}
```

```cpp
#include <hip/hip_runtime.h>
#include <hip/hip_bf16.h>

#define MAIN_ASM \
  "s_and_b32 s36, s2, 0xf3\n\t" \
  "s_cmpk_eq_i32 s36, 0xf3\n\t" \
  "s_cbranch_scc1 MFOLD%=\n\t" \
  "s_lshl_b32 s98, s2, 4\n\t" \
  "s_and_b32 s98, s98, 64\n\t" \
  "s_load_dwordx16 s[4:19], s[0:1], s98\n\t" \
  "v_lshrrev_b32_e32 v213, 6, v0\n\t" \
  "v_and_b32_e32 v0, 63, v0\n\t" \
  "v_readfirstlane_b32 s3, v213\n\t" \
  "v_lshlrev_b32_e32 v59, 4, v0\n\t" \
  "v_lshlrev_b32_e32 v218, 7, v0\n\t" \
  "s_lshr_b32 s98, s2, 1\n\t" \
  "s_and_b32 s98, s98, 4\n\t" \
  "s_add_u32 s98, s98, s3\n\t" \
  "s_mul_i32 s98, s98, 192\n\t" \
  "s_load_dwordx16 s[20:35], s[0:1], 0x80\n\t" \
  "s_load_dwordx8 s[36:43], s[0:1], 0xc0\n\t" \
  "s_add_u32 s0, s0, s98\n\t" \
  "s_addc_u32 s1, s1, 0\n\t" \
  "s_load_dwordx16 s[44:59], s[0:1], 0xe0\n\t" \
  "s_load_dwordx16 s[60:75], s[0:1], 0x120\n\t" \
  "s_load_dwordx16 s[76:91], s[0:1], 0x160\n\t" \
  "s_getpc_b64 s[92:93]\n\t" \
  "CODE_A%=:\n\t" \
  "s_mov_b32 s94, CODE_END%=-CODE_A%=\n\t" \
  "s_mov_b32 s95, 0x20000\n\t" \
  "s_bfe_u32 s98, s2, 0x20003\n\t" \
  "s_add_u32 s98, s98, 1\n\t" \
  "s_lshl_b32 s98, s98, 13\n\t" \
  "s_mul_i32 s98, s98, s3\n\t" \
  "s_cmp_lt_u32 s3, 2\n\t" \
  "s_cselect_b32 s98, s98, 1879048192\n\t" \
  "buffer_load_dword v215, v218, s[92:95], s98 offen\n\t" \
  "s_lshr_b32 s97, s2, 4\n\t" \
  "s_lshl_b32 s97, s97, 2\n\t" \
  "s_and_b32 s98, s2, 3\n\t" \
  "s_or_b32 s97, s97, s98\n\t" \
  "s_lshl2_add_u32 s96, s97, s3\n\t" \
  "s_mul_i32 s96, s96, 4608\n\t" \
  "s_mul_i32 s99, s3, 4608\n\t" \
  "s_add_u32 m0, s99, 12288\n\t" \
  "s_waitcnt lgkmcnt(0)\n\t" \
  "buffer_load_dwordx4 v59, s[4:7], s96 offen offset:0 lds\n\t" \
  "buffer_load_dwordx4 v59, s[4:7], s96 offen offset:1024 lds\n\t" \
  "buffer_load_dwordx4 v59, s[4:7], s96 offen offset:2048 lds\n\t" \
  "buffer_load_dwordx4 v59, s[4:7], s96 offen offset:3072 lds\n\t" \
  "buffer_load_dwordx4 v59, s[4:7], s96 offen offset:3584 lds\n\t" \
  "s_bfe_u32 s99, s2, 0x20003\n\t" \
  "s_lshl2_add_u32 s99, s99, s3\n\t" \
  "s_lshl_b32 s99, s99, 13\n\t" \
  "s_bitcmp1_b32 s99, 16\n\t" \
  "s_cselect_b64 s[92:93], s[24:25], s[20:21]\n\t" \
  "s_and_b32 s98, s99, 65535\n\t" \
  "s_bitcmp1_b32 s2, 5\n\t" \
  "s_cselect_b64 s[92:93], s[92:93], s[28:29]\n\t" \
  "s_cselect_b32 s94, s26, s30\n\t" \
  "s_cselect_b32 s99, s98, s99\n\t" \
  "buffer_load_dword v217, v218, s[92:95], s99 offen\n\t" \
  "s_branch MAIN%=\n\t" \
  "MFOLD%=:\n\t" \
  "s_load_dwordx8 s[36:43], s[0:1], 0x80\n\t" \
  "s_load_dwordx4 s[44:47], s[0:1], 0x30\n\t" \
  "v_lshrrev_b32_e32 v65, 6, v0\n\t" \
  "v_and_b32_e32 v64, 63, v0\n\t" \
  "v_readfirstlane_b32 s48, v65\n\t" \
  "v_lshlrev_b32_e32 v65, 4, v64\n\t" \
  "v_lshlrev_b32_e32 v66, 3, v64\n\t" \
  "s_bfe_u32 s49, s2, 0x10003\n\t" \
  "s_bfe_u32 s50, s2, 0x10002\n\t" \
  "s_mul_i32 s50, s50, 18432\n\t" \
  "s_mul_i32 s54, s49, 18432\n\t" \
  "s_mul_i32 s51, s48, 5120\n\t" \
  "s_min_u32 s51, s51, 13312\n\t" \
  "s_add_u32 s52, s51, s54\n\t" \
  "s_add_u32 m0, s51, 23552\n\t" \
  "s_add_u32 s53, s52, 0\n\t" \
  "s_waitcnt lgkmcnt(0)\n\t" \
  "buffer_load_dwordx4 v65, s[36:39], s53 offen offset:0 lds\n\t" \
  "buffer_load_dwordx4 v65, s[36:39], s53 offen offset:1024 lds\n\t" \
  "buffer_load_dwordx4 v65, s[36:39], s53 offen offset:2048 lds\n\t" \
  "buffer_load_dwordx4 v65, s[36:39], s53 offen offset:3072 lds\n\t" \
  "s_add_u32 m0, s51, 27648\n\t" \
  "s_add_u32 s53, s52, 4096\n\t" \
  "buffer_load_dwordx4 v65, s[36:39], s53 offen offset:0 lds\n\t" \
  "s_mul_i32 s51, s48, 6144\n\t" \
  "s_min_u32 s51, s51, 16896\n\t" \
  "s_add_u32 s52, s51, s50\n\t" \
  "s_add_u32 m0, s51, 0\n\t" \
  "s_add_u32 s53, s52, 0\n\t" \
  "buffer_load_dwordx4 v65, s[40:43], s53 offen offset:0 lds\n\t" \
  "buffer_load_dwordx4 v65, s[40:43], s53 offen offset:1024 lds\n\t" \
  "buffer_load_dwordx4 v65, s[40:43], s53 offen offset:2048 lds\n\t" \
  "buffer_load_dwordx4 v65, s[40:43], s53 offen offset:3072 lds\n\t" \
  "s_add_u32 m0, s51, 4096\n\t" \
  "s_add_u32 s53, s52, 4096\n\t" \
  "buffer_load_dwordx4 v65, s[40:43], s53 offen offset:0 lds\n\t" \
  "buffer_load_dwordx4 v65, s[40:43], s53 offen offset:1024 lds\n\t" \
  "s_mul_i32 s51, s48, 1024\n\t" \
  "s_min_u32 s51, s51, 2560\n\t" \
  "s_add_u32 s52, s51, 36864\n\t" \
  "s_add_u32 m0, s51, 41984\n\t" \
  "s_add_u32 s53, s52, 0\n\t" \
  "buffer_load_dwordx4 v65, s[36:39], s53 offen offset:0 lds\n\t" \
  "buffer_load_dwordx4 v65, s[36:39], s53 offen offset:1024 lds\n\t" \
  "buffer_load_dwordx2 v[70:71], off, s[44:47], 0 sc1\n\t" \
  "s_getpc_b64 s[60:61]\n\t" \
  "HPC%=:\n\t" \
  "s_add_u32 s60, s60, LEAVE%=-HPC%=\n\t" \
  "s_addc_u32 s61, s61, 0\n\t" \
  "s_mov_b32 s62, BF_HELPER_END-LEAVE%=\n\t" \
  "s_mov_b32 s63, 0x20000\n\t" \
  "v_lshlrev_b32_e32 v138, 6, v0\n\t" \
  "buffer_load_dword v138, v138, s[60:63], 0 offen\n\t" \
  "v_and_b32_e32 v67, 15, v64\n\t" \
  "v_mul_u32_u24_e32 v67, 288, v67\n\t" \
  "v_lshrrev_b32_e32 v69, 4, v64\n\t" \
  "v_lshl_add_u32 v68, v69, 3, v67\n\t" \
  "v_lshl_add_u32 v67, v69, 4, v67\n\t" \
  "s_mul_i32 s51, s48, 4608\n\t" \
  "v_add_u32_e32 v132, s51, v67\n\t" \
  "v_add_u32_e32 v133, s51, v68\n\t" \
  "s_cmp_eq_u32 s50, 0\n\t" \
  "s_cselect_b32 s55, 3, s48\n\t" \
  "s_add_u32 s51, s51, s50\n\t" \
  "s_add_u32 s51, s51, 9600512\n\t" \
  "s_add_u32 s52, s50, 9618944\n\t" \
  "s_lshl_b32 s53, s49, 11\n\t" \
  "s_bfe_u32 s50, s2, 0x20002\n\t" \
  "s_lshl_b32 s50, s50, 7\n\t" \
  "s_add_u32 s50, s50, 102400\n\t" \
  "s_cmp_eq_u32 s48, 0\n\t" \
  "s_cselect_b32 s50, s50, 1879048192\n\t" \
  "v_add_u32_e32 v134, s53, v65\n\t" \
  "v_add_u32_e32 v135, 4096, v66\n\t" \
  "v_lshrrev_b32_e32 v136, 3, v64\n\t" \
  "v_and_b32_e32 v139, 112, v65\n\t" \
  "v_lshl_add_u32 v136, v136, 9, v139\n\t" \
  "s_waitcnt vmcnt(2)\n\t" \
  "s_barrier\n\t" \
  "ds_read_b128 a[0:3], v132 offset:0\n\t" \
  "ds_read_b128 a[4:7], v132 offset:64\n\t" \
  "ds_read_b128 a[8:11], v132 offset:128\n\t" \
  "ds_read_b128 a[12:15], v132 offset:192\n\t" \
  "ds_read_b64 a[16:17], v133 offset:256\n\t" \
  "ds_read_b128 a[36:39], v67 offset:23552\n\t" \
  "ds_read_b128 a[54:57], v67 offset:28160\n\t" \
  "ds_read_b128 a[40:43], v67 offset:23616\n\t" \
  "ds_read_b128 a[58:61], v67 offset:28224\n\t" \
  "ds_read_b128 a[44:47], v67 offset:23680\n\t" \
  "ds_read_b128 a[62:65], v67 offset:28288\n\t" \
  "ds_read_b128 a[48:51], v67 offset:23744\n\t" \
  "ds_read_b128 a[66:69], v67 offset:28352\n\t" \
  "ds_read_b64 a[52:53], v68 offset:23808\n\t" \
  "ds_read_b64 a[70:71], v68 offset:28416\n\t" \
  "s_waitcnt lgkmcnt(9)\n\t" \
  "v_mfma_f32_16x16x32_bf16 v[72:75], a[36:39], a[0:3], 0\n\t" \
  "ds_read_b128 a[72:75], v67 offset:32768\n\t" \
  "s_waitcnt lgkmcnt(9)\n\t" \
  "v_mfma_f32_16x16x32_bf16 v[76:79], a[54:57], a[0:3], 0\n\t" \
  "ds_read_b128 a[90:93], v67 offset:37376\n\t" \
  "s_waitcnt lgkmcnt(9)\n\t" \
  "v_mfma_f32_16x16x32_bf16 v[72:75], a[40:43], a[4:7], v[72:75]\n\t" \
  "ds_read_b128 a[108:111], v67 offset:41984\n\t" \
  "s_waitcnt lgkmcnt(8)\n\t" \
  "v_mfma_f32_16x16x32_bf16 v[76:79], a[58:61], a[4:7], v[76:79]\n\t" \
  "ds_read_b128 a[76:79], v67 offset:32832\n\t" \
  "v_mfma_f32_16x16x32_bf16 v[72:75], a[44:47], a[8:11], v[72:75]\n\t" \
  "ds_read_b128 a[94:97], v67 offset:37440\n\t" \
  "s_waitcnt lgkmcnt(5)\n\t" \
  "v_mfma_f32_16x16x32_bf16 v[76:79], a[62:65], a[8:11], v[76:79]\n\t" \
  "ds_read_b128 a[112:115], v67 offset:42048\n\t" \
  "v_mfma_f32_16x16x32_bf16 v[72:75], a[48:51], a[12:15], v[72:75]\n\t" \
  "ds_read_b128 a[80:83], v67 offset:32896\n\t" \
  "v_mfma_f32_16x16x32_bf16 v[76:79], a[66:69], a[12:15], v[76:79]\n\t" \
  "ds_read_b128 a[98:101], v67 offset:37504\n\t" \
  "v_mfma_f32_16x16x16_bf16 v[72:75], a[52:53], a[16:17], v[72:75]\n\t" \
  "ds_read_b128 a[116:119], v67 offset:42112\n\t" \
  "v_mfma_f32_16x16x16_bf16 v[76:79], a[70:71], a[16:17], v[76:79]\n\t" \
  "ds_read_b128 a[84:87], v67 offset:32960\n\t" \
  "s_waitcnt lgkmcnt(7)\n\t" \
  "v_mfma_f32_16x16x32_bf16 v[80:83], a[72:75], a[0:3], 0\n\t" \
  "ds_read_b128 a[102:105], v67 offset:37568\n\t" \
  "v_mfma_f32_16x16x32_bf16 v[84:87], a[90:93], a[0:3], 0\n\t" \
  "ds_read_b128 a[120:123], v67 offset:42176\n\t" \
  "v_mfma_f32_16x16x32_bf16 v[88:91], a[108:111], a[0:3], 0\n\t" \
  "ds_read_b64 a[88:89], v68 offset:33024\n\t" \
  "s_waitcnt lgkmcnt(7)\n\t" \
  "v_mfma_f32_16x16x32_bf16 v[80:83], a[76:79], a[4:7], v[80:83]\n\t" \
  "ds_read_b64 a[106:107], v68 offset:37632\n\t" \
  "v_cvt_pk_bf16_f32 v112, v72, v73\n\t" \
  "v_cvt_pk_bf16_f32 v113, v74, v75\n\t" \
  "v_mfma_f32_16x16x32_bf16 v[84:87], a[94:97], a[4:7], v[84:87]\n\t" \
  "ds_read_b64 a[124:125], v68 offset:42240\n\t" \
  "v_cvt_pk_bf16_f32 v114, v76, v77\n\t" \
  "v_cvt_pk_bf16_f32 v115, v78, v79\n\t" \
  "v_mfma_f32_16x16x32_bf16 v[88:91], a[112:115], a[4:7], v[88:91]\n\t" \
  "s_waitcnt lgkmcnt(4)\n\t" \
  "v_mfma_f32_16x16x32_bf16 v[80:83], a[80:83], a[8:11], v[80:83]\n\t" \
  "buffer_store_dwordx4 v[112:115], v134, s[44:47], s51 offen offset:0 sc1\n\t" \
  "v_mfma_f32_16x16x32_bf16 v[84:87], a[98:101], a[8:11], v[84:87]\n\t" \
  "ds_read_b128 a[18:21], v67 offset:18432\n\t" \
  "v_mfma_f32_16x16x32_bf16 v[88:91], a[116:119], a[8:11], v[88:91]\n\t" \
  "ds_read_b128 a[22:25], v67 offset:18496\n\t" \
  "v_mfma_f32_16x16x32_bf16 v[80:83], a[84:87], a[12:15], v[80:83]\n\t" \
  "ds_read_b128 a[26:29], v67 offset:18560\n\t" \
  "v_mfma_f32_16x16x32_bf16 v[84:87], a[102:105], a[12:15], v[84:87]\n\t" \
  "ds_read_b128 a[30:33], v67 offset:18624\n\t" \
  "s_waitcnt lgkmcnt(4)\n\t" \
  "v_mfma_f32_16x16x32_bf16 v[88:91], a[120:123], a[12:15], v[88:91]\n\t" \
  "ds_read_b64 a[34:35], v68 offset:18688\n\t" \
  "v_mfma_f32_16x16x16_bf16 v[80:83], a[88:89], a[16:17], v[80:83]\n\t" \
  "v_mfma_f32_16x16x16_bf16 v[84:87], a[106:107], a[16:17], v[84:87]\n\t" \
  "v_mfma_f32_16x16x16_bf16 v[88:91], a[124:125], a[16:17], v[88:91]\n\t" \
  "s_cmp_eq_u32 s55, 0\n\t" \
  "s_cbranch_scc0 MF0%=\n\t" \
  "s_waitcnt lgkmcnt(4)\n\t" \
  "v_mfma_f32_16x16x32_bf16 v[92:95], a[36:39], a[18:21], 0\n\t" \
  "v_mfma_f32_16x16x32_bf16 v[96:99], a[54:57], a[18:21], 0\n\t" \
  "s_waitcnt lgkmcnt(3)\n\t" \
  "v_mfma_f32_16x16x32_bf16 v[92:95], a[40:43], a[22:25], v[92:95]\n\t" \
  "v_mfma_f32_16x16x32_bf16 v[96:99], a[58:61], a[22:25], v[96:99]\n\t" \
  "s_waitcnt lgkmcnt(1)\n\t" \
  "v_mfma_f32_16x16x32_bf16 v[92:95], a[44:47], a[26:29], v[92:95]\n\t" \
  "v_mfma_f32_16x16x32_bf16 v[96:99], a[62:65], a[26:29], v[96:99]\n\t" \
  "v_mfma_f32_16x16x32_bf16 v[92:95], a[48:51], a[30:33], v[92:95]\n\t" \
  "v_mfma_f32_16x16x32_bf16 v[96:99], a[66:69], a[30:33], v[96:99]\n\t" \
  "s_waitcnt lgkmcnt(0)\n\t" \
  "v_mfma_f32_16x16x16_bf16 v[92:95], a[52:53], a[34:35], v[92:95]\n\t" \
  "v_mfma_f32_16x16x16_bf16 v[96:99], a[70:71], a[34:35], v[96:99]\n\t" \
  "s_nop 3\n\t" \
  "v_cvt_pk_bf16_f32 v122, v92, v93\n\t" \
  "v_cvt_pk_bf16_f32 v123, v94, v95\n\t" \
  "s_nop 1\n\t" \
  "v_cvt_pk_bf16_f32 v124, v96, v97\n\t" \
  "v_cvt_pk_bf16_f32 v125, v98, v99\n\t" \
  "buffer_store_dwordx4 v[122:125], v134, s[44:47], s52 offen offset:0 sc1\n\t" \
  "MF0%=:\n\t" \
  "s_cmp_eq_u32 s55, 1\n\t" \
  "s_cbranch_scc0 MF1%=\n\t" \
  "s_waitcnt lgkmcnt(0)\n\t" \
  "v_mfma_f32_16x16x32_bf16 v[100:103], a[72:75], a[18:21], 0\n\t" \
  "v_mfma_f32_16x16x32_bf16 v[104:107], a[90:93], a[18:21], 0\n\t" \
  "v_mfma_f32_16x16x32_bf16 v[100:103], a[76:79], a[22:25], v[100:103]\n\t" \
  "v_mfma_f32_16x16x32_bf16 v[104:107], a[94:97], a[22:25], v[104:107]\n\t" \
  "v_mfma_f32_16x16x32_bf16 v[100:103], a[80:83], a[26:29], v[100:103]\n\t" \
  "v_mfma_f32_16x16x32_bf16 v[104:107], a[98:101], a[26:29], v[104:107]\n\t" \
  "v_mfma_f32_16x16x32_bf16 v[100:103], a[84:87], a[30:33], v[100:103]\n\t" \
  "v_mfma_f32_16x16x32_bf16 v[104:107], a[102:105], a[30:33], v[104:107]\n\t" \
  "v_mfma_f32_16x16x16_bf16 v[100:103], a[88:89], a[34:35], v[100:103]\n\t" \
  "v_mfma_f32_16x16x16_bf16 v[104:107], a[106:107], a[34:35], v[104:107]\n\t" \
  "s_nop 3\n\t" \
  "v_cvt_pk_bf16_f32 v126, v100, v101\n\t" \
  "v_cvt_pk_bf16_f32 v127, v102, v103\n\t" \
  "s_nop 1\n\t" \
  "v_cvt_pk_bf16_f32 v128, v104, v105\n\t" \
  "v_cvt_pk_bf16_f32 v129, v106, v107\n\t" \
  "buffer_store_dwordx4 v[126:129], v134, s[44:47], s52 offen offset:1024 sc1\n\t" \
  "MF1%=:\n\t" \
  "s_cmp_eq_u32 s55, 2\n\t" \
  "s_cbranch_scc0 MF2%=\n\t" \
  "s_waitcnt lgkmcnt(0)\n\t" \
  "v_mfma_f32_16x16x32_bf16 v[108:111], a[108:111], a[18:21], 0\n\t" \
  "v_mfma_f32_16x16x32_bf16 v[108:111], a[112:115], a[22:25], v[108:111]\n\t" \
  "v_mfma_f32_16x16x32_bf16 v[108:111], a[116:119], a[26:29], v[108:111]\n\t" \
  "v_mfma_f32_16x16x32_bf16 v[108:111], a[120:123], a[30:33], v[108:111]\n\t" \
  "s_nop 5\n\t" \
  "v_mfma_f32_16x16x16_bf16 v[108:111], a[124:125], a[34:35], v[108:111]\n\t" \
  "s_nop 7\n\t" \
  "v_cvt_pk_bf16_f32 v130, v108, v109\n\t" \
  "v_cvt_pk_bf16_f32 v131, v110, v111\n\t" \
  "buffer_store_dwordx2 v[130:131], v135, s[44:47], s52 offen sc1\n\t" \
  "MF2%=:\n\t" \
  "v_cvt_pk_bf16_f32 v116, v80, v81\n\t" \
  "v_cvt_pk_bf16_f32 v117, v82, v83\n\t" \
  "v_cvt_pk_bf16_f32 v118, v84, v85\n\t" \
  "v_cvt_pk_bf16_f32 v119, v86, v87\n\t" \
  "v_cvt_pk_bf16_f32 v120, v88, v89\n\t" \
  "v_cvt_pk_bf16_f32 v121, v90, v91\n\t" \
  "buffer_store_dwordx4 v[116:119], v134, s[44:47], s51 offen offset:1024 sc1\n\t" \
  "buffer_store_dwordx2 v[120:121], v135, s[44:47], s51 offen sc1\n\t" \
  "s_waitcnt vmcnt(4)\n\t" \
  "v_readfirstlane_b32 s56, v70\n\t" \
  "v_readfirstlane_b32 s57, v71\n\t" \
  "s_add_u32 s56, s56, 1\n\t" \
  "s_addc_u32 s57, s57, 0\n\t" \
  "v_mov_b32_e32 v140, s56\n\t" \
  "v_mov_b32_e32 v141, s57\n\t" \
  "v_mov_b32_e32 v142, s56\n\t" \
  "v_mov_b32_e32 v143, s57\n\t" \
  "s_waitcnt vmcnt(0)\n\t" \
  "s_barrier\n\t" \
  "buffer_store_dwordx4 v[140:143], v136, s[44:47], s50 offen sc1\n\t" \
  "s_branch LEAVE%=\n\t" \
  "MAIN%=:\n\t" \
  "v_and_b32_e32 v208, 15, v0\n\t" \
  "v_lshrrev_b32_e32 v209, 4, v0\n\t" \
  "v_lshrrev_b32_e32 v210, 2, v0\n\t" \
  "v_and_b32_e32 v211, 3, v0\n\t" \
  "v_lshlrev_b32_e32 v63, 3, v0\n\t" \
  "v_mul_u32_u24_e32 v213, 288, v208\n\t" \
  "v_lshl_add_u32 v3, v209, 3, v213\n\t" \
  "v_lshl_add_u32 v2, v209, 4, v213\n\t" \
  "v_bfe_u32 v212, v0, 4, 1\n\t" \
  "v_mul_u32_u24_e32 v214, 192, v212\n\t" \
  "v_xor_b32_e32 v17, v214, v59\n\t" \
  "v_mul_u32_u24_e32 v26, 576, v210\n\t" \
  "v_lshl_add_u32 v26, v211, 3, v26\n\t" \
  "v_lshl_add_u32 v26, v212, 5, v26\n\t" \
  "v_add_u32_e32 v26, 36864, v26\n\t" \
  "v_xor_b32_e32 v28, 32, v26\n\t" \
  "v_add_u32_e32 v27, 46080, v26\n\t" \
  "v_add_u32_e32 v29, 46080, v28\n\t" \
  "v_lshrrev_b32_e32 v214, 5, v0\n\t" \
  "v_lshlrev_b32_e32 v214, 4, v214\n\t" \
  "v_lshl_add_u32 v214, v208, 6, v214\n\t" \
  "v_lshl_add_u32 v250, v212, 10, v214\n\t" \
  "v_lshl_add_u32 v251, v212, 3, v214\n\t" \
  "buffer_load_dwordx2 v[30:31], off, s[16:19], 0 sc1\n\t" \
  "s_add_u32 m0, s50, 36864\n\t" \
  "s_add_u32 s99, s50, 0\n\t" \
  "buffer_load_dwordx4 v59, s[28:31], s99 offen offset:0 lds\n\t" \
  "buffer_load_dwordx4 v59, s[28:31], s99 offen offset:1024 lds\n\t" \
  "v_xor_b32_e32 v226, 32, v59\n\t" \
  "v_cmp_gt_u32_e32 vcc, 16, v0\n\t" \
  "v_add_u32_e32 v57, 4096, v59\n\t" \
  "v_add_u32_e32 v58, 8192, v59\n\t" \
  "v_cndmask_b32_e32 v227, v226, v59, vcc\n\t" \
  "v_cmp_gt_u32_e32 vcc, 48, v0\n\t" \
  "v_lshl_add_u32 v230, s89, 6, v0\n\t" \
  "v_add_u32_e32 v231, 256, v230\n\t" \
  "v_cndmask_b32_e32 v229, v226, v59, vcc\n\t" \
  "v_cmp_gt_u32_e32 vcc, 32, v0\n\t" \
  "v_add_u32_e32 v216, s72, v3\n\t" \
  "v_mov_b32_e32 v20, s43\n\t" \
  "v_cndmask_b32_e32 v228, v59, v226, vcc\n\t" \
  "v_mul_u32_u24_e32 v230, 3641, v230\n\t" \
  "v_lshrrev_b32_e32 v230, 14, v230\n\t" \
  "v_and_b32_e32 v230, 32, v230\n\t" \
  "v_xor_b32_e32 v230, v230, v59\n\t" \
  "v_mul_u32_u24_e32 v231, 3641, v231\n\t" \
  "v_lshrrev_b32_e32 v231, 14, v231\n\t" \
  "v_and_b32_e32 v231, 32, v231\n\t" \
  "v_xor_b32_e32 v231, v231, v59\n\t" \
  "s_bfe_u32 s3, s2, 0x10002\n\t" \
  "s_bfe_u32 s94, s2, 0x10003\n\t" \
  "s_lshl_b32 s1, s97, 1\n\t" \
  "s_add_u32 s1, s1, s94\n\t" \
  "s_lshl_b32 s95, s3, 7\n\t" \
  "s_add_u32 s98, s95, s1\n\t" \
  "s_lshl_b32 s98, s98, 7\n\t" \
  "v_min_u32_e32 v212, 2, v211\n\t" \
  "v_add_u32_e32 v212, s1, v212\n\t" \
  "v_max_u32_e32 v212, 1, v212\n\t" \
  "v_min_u32_e32 v212, 125, v212\n\t" \
  "v_add_u32_e32 v212, s95, v212\n\t" \
  "v_lshlrev_b32_e32 v212, 7, v212\n\t" \
  "buffer_load_dwordx4 v227, s[28:31], s99 offen offset:2048 lds\n\t" \
  "v_bfe_u32 v214, v0, 2, 2\n\t" \
  "v_lshl_add_u32 v212, v214, 3, v212\n\t" \
  "v_add_u32_e32 v212, 69504, v212\n\t" \
  "v_lshlrev_b32_e32 v214, 3, v208\n\t" \
  "v_add_u32_e32 v214, 1024, v214\n\t" \
  "v_cndmask_b32_e32 v32, v214, v212, vcc\n\t" \
  "v_add_u32_e32 v22, s75, v26\n\t" \
  "v_add_u32_e32 v23, s75, v27\n\t" \
  "v_add_u32_e32 v24, s75, v28\n\t" \
  "v_add_u32_e32 v25, s75, v29\n\t" \
  "v_lshrrev_b32_e32 v212, 1, v211\n\t" \
  "v_mul_u32_u24_e32 v214, 12, v212\n\t" \
  "v_xor_b32_e32 v214, v214, v210\n\t" \
  "v_lshlrev_b32_e32 v214, 4, v214\n\t" \
  "v_lshl_add_u32 v18, v212, 8, v214\n\t" \
  "v_and_b32_e32 v214, 1, v211\n\t" \
  "v_lshl_add_u32 v18, v214, 3, v18\n\t" \
  "v_lshrrev_b32_e32 v214, 5, v0\n\t" \
  "v_mul_u32_u24_e32 v214, 28, v214\n\t" \
  "v_xor_b32_e32 v214, v214, v208\n\t" \
  "buffer_load_dwordx4 v226, s[28:31], s99 offen offset:3072 lds\n\t" \
  "v_bfe_u32 v19, v0, 4, 1\n\t" \
  "v_lshlrev_b32_e32 v19, 3, v19\n\t" \
  "v_lshl_add_u32 v19, v214, 4, v19\n\t" \
  "v_add_u32_e32 v245, 119808, v59\n\t" \
  "v_add_u32_e32 v246, 123904, v63\n\t" \
  "v_add_u32_e32 v250, s65, v250\n\t" \
  "v_add_u32_e32 v251, s66, v251\n\t" \
  "s_add_u32 s94, s87, 110592\n\t" \
  "v_add_u32_e32 v244, s94, v59\n\t" \
  "v_add_u32_e32 v248, s86, v59\n\t" \
  "v_add_u32_e32 v247, s73, v59\n\t" \
  "v_lshl_add_u32 v249, v209, 5, s74\n\t" \
  "s_lshl_b32 s94, s67, 1\n\t" \
  "v_min_u32_e32 v212, 29, v0\n\t" \
  "v_add_u32_e32 v212, s94, v212\n\t" \
  "v_min_u32_e32 v214, 9, v208\n\t" \
  "v_add_u32_e32 v214, 240, v214\n\t" \
  "v_cndmask_b32_e32 v212, v214, v212, vcc\n\t" \
  "s_lshl_b32 s95, s95, 8\n\t" \
  "s_add_u32 s95, s95, 4096\n\t" \
  "s_add_u32 m0, s50, 40960\n\t" \
  "s_add_u32 s99, s50, 4096\n\t" \
  "buffer_load_dwordx4 v228, s[28:31], s99 offen offset:0 lds\n\t" \
  "v_lshl_add_u32 v252, v212, 7, s95\n\t" \
  "s_lshl_b32 s73, s2, 1\n\t" \
  "s_sub_u32 s73, s73, s67\n\t" \
  "s_and_b32 s73, s73, 7\n\t" \
  "s_add_u32 s94, s73, s67\n\t" \
  "s_lshl_b32 s94, s94, 8\n\t" \
  "s_add_u32 s94, s94, s95\n\t" \
  "v_lshl_add_u32 v253, v211, 7, s94\n\t" \
  "s_lshl_b32 s94, s2, 6\n\t" \
  "s_and_b32 s94, s94, 3584\n\t" \
  "s_add_u32 s94, s94, 102400\n\t" \
  "v_lshl_add_u32 v255, v211, 7, s94\n\t" \
  "s_mul_i32 s95, s3, 126\n\t" \
  "s_add_u32 s0, s95, s67\n\t" \
  "s_mul_i32 s0, s0, 9216\n\t" \
  "s_add_u32 s0, s0, 131072\n\t" \
  "s_mov_b32 s72, s0\n\t" \
  "s_mul_i32 s73, s73, 9216\n\t" \
  "s_add_u32 s0, s0, s73\n\t" \
  "s_add_u32 s73, s72, 138240\n\t" \
  "buffer_load_dwordx4 v59, s[28:31], s99 offen offset:1024 lds\n\t" \
  "s_add_u32 s97, s95, s1\n\t" \
  "s_mul_i32 s97, s97, 9216\n\t" \
  "s_add_u32 s97, s97, 131072\n\t" \
  "s_lshl4_add_u32 s94, s44, s98\n\t" \
  "s_add_u32 s94, s94, s98\n\t" \
  "s_add_u32 s94, s94, 4096\n\t" \
  "s_cmp_eq_u32 s46, 1\n\t" \
  "s_cselect_b32 s97, s97, 1879048192\n\t" \
  "s_cselect_b32 s94, s94, 1879048192\n\t" \
  "v_lshlrev_b32_e32 v1, 26, v209\n\t" \
  "v_lshl_add_u32 v1, v208, 3, v1\n\t" \
  "v_add_u32_e32 v1, s94, v1\n\t" \
  "v_mov_b32_e32 v35, 2147483632\n\t" \
  "s_lshl3_add_u32 s95, s89, s98\n\t" \
  "s_add_u32 s95, s95, 69632\n\t" \
  "v_writelane_b32 v35, s95, 0\n\t" \
  "s_mul_i32 s67, s1, 9216\n\t" \
  "v_add_u32_e32 v33, s67, v59\n\t" \
  "v_add_u32_e32 v48, s52, v17\n\t" \
  "v_add_u32_e32 v51, s52, v18\n\t" \
  "buffer_load_dwordx4 v229, s[28:31], s99 offen offset:2048 lds\n\t" \
  "v_add_u32_e32 v54, s52, v19\n\t" \
  "v_add_u32_e32 v49, s55, v17\n\t" \
  "v_add_u32_e32 v52, s55, v18\n\t" \
  "v_add_u32_e32 v55, s55, v19\n\t" \
  "v_add_u32_e32 v50, s58, v17\n\t" \
  "v_add_u32_e32 v53, s58, v18\n\t" \
  "v_add_u32_e32 v56, s58, v19\n\t" \
  "v_add_u32_e32 v34, s55, v59\n\t" \
  "v_add_u32_e32 v12, s61, v59\n\t" \
  "v_add_u32_e32 v13, s62, v59\n\t" \
  "v_add_u32_e32 v14, s63, v59\n\t" \
  "v_add_u32_e32 v15, s64, v59\n\t" \
  "s_waitcnt vmcnt(9)\n\t" \
  "ds_read_b64 v[64:65], v216 offset:0\n\t" \
  "ds_read_b64 v[66:67], v216 offset:32\n\t" \
  "ds_read_b64 v[68:69], v216 offset:64\n\t" \
  "ds_read_b64 v[70:71], v216 offset:96\n\t" \
  "ds_read_b64 v[72:73], v216 offset:128\n\t" \
  "ds_read_b64 v[74:75], v216 offset:160\n\t" \
  "ds_read_b64 v[76:77], v216 offset:192\n\t" \
  "buffer_load_dwordx4 v226, s[28:31], s99 offen offset:3072 lds\n\t" \
  "ds_read_b64 v[78:79], v216 offset:224\n\t" \
  "ds_read_b64 v[80:81], v216 offset:256\n\t" \
  "s_waitcnt lgkmcnt(8)\n\t" \
  "v_dot2_f32_bf16 v154, v64, s42, 0\n\t" \
  "v_dot2_f32_bf16 v156, v64, v64, 0\n\t" \
  "v_dot2_f32_bf16 v155, v65, s42, 0\n\t" \
  "v_dot2_f32_bf16 v157, v65, v65, 0\n\t" \
  "s_waitcnt lgkmcnt(7)\n\t" \
  "v_dot2_f32_bf16 v154, v66, s42, v154\n\t" \
  "s_add_u32 m0, s50, 45056\n\t" \
  "s_add_u32 s99, s50, 8192\n\t" \
  "buffer_load_dwordx4 v226, s[28:31], s99 offen offset:0 lds\n\t" \
  "v_dot2_f32_bf16 v156, v66, v66, v156\n\t" \
  "v_dot2_f32_bf16 v155, v67, s42, v155\n\t" \
  "v_dot2_f32_bf16 v157, v67, v67, v157\n\t" \
  "s_waitcnt lgkmcnt(1)\n\t" \
  "v_dot2_f32_bf16 v154, v68, s42, v154\n\t" \
  "v_dot2_f32_bf16 v156, v68, v68, v156\n\t" \
  "v_dot2_f32_bf16 v155, v69, s42, v155\n\t" \
  "v_dot2_f32_bf16 v157, v69, v69, v157\n\t" \
  "buffer_load_dwordx4 v59, s[28:31], s99 offen offset:1024 lds\n\t" \
  "v_dot2_f32_bf16 v154, v70, s42, v154\n\t" \
  "v_dot2_f32_bf16 v156, v70, v70, v156\n\t" \
  "v_dot2_f32_bf16 v155, v71, s42, v155\n\t" \
  "v_dot2_f32_bf16 v157, v71, v71, v157\n\t" \
  "v_dot2_f32_bf16 v154, v72, s42, v154\n\t" \
  "v_dot2_f32_bf16 v156, v72, v72, v156\n\t" \
  "v_dot2_f32_bf16 v155, v73, s42, v155\n\t" \
  "buffer_load_dwordx4 v59, s[28:31], s99 offen offset:2048 lds\n\t" \
  "v_dot2_f32_bf16 v157, v73, v73, v157\n\t" \
  "v_dot2_f32_bf16 v154, v74, s42, v154\n\t" \
  "v_dot2_f32_bf16 v156, v74, v74, v156\n\t" \
  "v_dot2_f32_bf16 v155, v75, s42, v155\n\t" \
  "v_dot2_f32_bf16 v157, v75, v75, v157\n\t" \
  "v_dot2_f32_bf16 v154, v76, s42, v154\n\t" \
  "v_dot2_f32_bf16 v156, v76, v76, v156\n\t" \
  "buffer_load_dwordx4 v227, s[28:31], s99 offen offset:3072 lds\n\t" \
  "v_dot2_f32_bf16 v155, v77, s42, v155\n\t" \
  "v_dot2_f32_bf16 v157, v77, v77, v157\n\t" \
  "v_dot2_f32_bf16 v154, v78, s42, v154\n\t" \
  "v_dot2_f32_bf16 v156, v78, v78, v156\n\t" \
  "v_dot2_f32_bf16 v155, v79, s42, v155\n\t" \
  "v_dot2_f32_bf16 v157, v79, v79, v157\n\t" \
  "s_waitcnt lgkmcnt(0)\n\t" \
  "v_dot2_f32_bf16 v154, v80, s42, v154\n\t" \
  "s_add_u32 m0, s50, 49152\n\t" \
  "s_add_u32 s99, s50, 12288\n\t" \
  "buffer_load_dwordx4 v226, s[28:31], s99 offen offset:0 lds\n\t" \
  "v_dot2_f32_bf16 v156, v80, v80, v156\n\t" \
  "v_dot2_f32_bf16 v155, v81, s42, v155\n\t" \
  "v_dot2_f32_bf16 v157, v81, v81, v157\n\t" \
  "v_lshlrev_b32_e32 v82, 16, v64\n\t" \
  "v_and_b32_e32 v83, 0xffff0000, v64\n\t" \
  "v_lshlrev_b32_e32 v84, 16, v65\n\t" \
  "v_and_b32_e32 v85, 0xffff0000, v65\n\t" \
  "buffer_load_dwordx4 v228, s[28:31], s99 offen offset:1024 lds\n\t" \
  "v_add_f32_e32 v154, v154, v155\n\t" \
  "v_lshlrev_b32_e32 v86, 16, v66\n\t" \
  "v_and_b32_e32 v87, 0xffff0000, v66\n\t" \
  "v_add_f32_e32 v156, v156, v157\n\t" \
  "v_lshlrev_b32_e32 v88, 16, v67\n\t" \
  "v_and_b32_e32 v89, 0xffff0000, v67\n\t" \
  "v_permlane32_swap_b32_e32 v154, v156\n\t" \
  "buffer_load_dwordx4 v59, s[28:31], s99 offen offset:2048 lds\n\t" \
  "v_lshlrev_b32_e32 v90, 16, v68\n\t" \
  "v_and_b32_e32 v91, 0xffff0000, v68\n\t" \
  "v_add_f32_e32 v154, v154, v156\n\t" \
  "v_lshlrev_b32_e32 v92, 16, v69\n\t" \
  "v_and_b32_e32 v93, 0xffff0000, v69\n\t" \
  "v_mov_b32_e32 v158, v154\n\t" \
  "v_lshlrev_b32_e32 v94, 16, v70\n\t" \
  "buffer_load_dwordx4 v229, s[28:31], s99 offen offset:3072 lds\n\t" \
  "v_and_b32_e32 v95, 0xffff0000, v70\n\t" \
  "v_permlane16_swap_b32_e32 v154, v158\n\t" \
  "v_lshlrev_b32_e32 v96, 16, v71\n\t" \
  "v_and_b32_e32 v97, 0xffff0000, v71\n\t" \
  "v_add_f32_e32 v154, v154, v158\n\t" \
  "v_lshlrev_b32_e32 v98, 16, v72\n\t" \
  "v_and_b32_e32 v99, 0xffff0000, v72\n\t" \
  "s_add_u32 m0, s50, 53248\n\t" \
  "s_add_u32 s99, s50, 16384\n\t" \
  "buffer_load_dwordx4 v226, s[28:31], s99 offen offset:0 lds\n\t" \
  "v_mul_f32_e32 v154, 0x3be38e39, v154\n\t" \
  "v_lshlrev_b32_e32 v100, 16, v73\n\t" \
  "v_and_b32_e32 v101, 0xffff0000, v73\n\t" \
  "v_mov_b32_e32 v156, v154\n\t" \
  "v_lshlrev_b32_e32 v102, 16, v74\n\t" \
  "v_and_b32_e32 v103, 0xffff0000, v74\n\t" \
  "v_permlane32_swap_b32_e32 v154, v156\n\t" \
  "buffer_load_dwordx4 v226, s[28:31], s99 offen offset:1024 lds\n\t" \
  "v_lshlrev_b32_e32 v104, 16, v75\n\t" \
  "v_and_b32_e32 v105, 0xffff0000, v75\n\t" \
  "v_fma_f32 v156, -v154, v154, v156\n\t" \
  "v_lshlrev_b32_e32 v106, 16, v76\n\t" \
  "v_and_b32_e32 v107, 0xffff0000, v76\n\t" \
  "v_max_f32_e32 v156, 0, v156\n\t" \
  "v_lshlrev_b32_e32 v108, 16, v77\n\t" \
  "s_add_u32 m0, s51, 110592\n\t" \
  "s_add_u32 s99, s51, 73728\n\t" \
  "buffer_load_dwordx4 v230, s[28:31], s99 offen lds\n\t" \
  "v_and_b32_e32 v109, 0xffff0000, v77\n\t" \
  "v_add_f32_e32 v156, 0x3727c5ac, v156\n\t" \
  "v_lshlrev_b32_e32 v110, 16, v78\n\t" \
  "v_and_b32_e32 v111, 0xffff0000, v78\n\t" \
  "v_rsq_f32_e32 v160, v156\n\t" \
  "v_lshlrev_b32_e32 v112, 16, v79\n\t" \
  "s_add_u32 m0, s51, 114688\n\t" \
  "s_add_u32 s99, s51, 77824\n\t" \
  "buffer_load_dwordx4 v231, s[28:31], s99 offen lds\n\t" \
  "v_and_b32_e32 v113, 0xffff0000, v79\n\t" \
  "v_mul_f32_e32 v162, v154, v160\n\t" \
  "v_lshlrev_b32_e32 v114, 16, v80\n\t" \
  "v_and_b32_e32 v115, 0xffff0000, v80\n\t" \
  "v_sub_f32_e32 v162, 0x3f000000, v162\n\t" \
  "v_lshlrev_b32_e32 v116, 16, v81\n\t" \
  "v_and_b32_e32 v117, 0xffff0000, v81\n\t" \
  "s_mov_b32 m0, 118784\n\t" \
  "s_mov_b32 s99, 81920\n\t" \
  "buffer_load_dwordx4 v226, s[28:31], s99 offen lds\n\t" \
  "v_pk_fma_f32 v[82:83], v[82:83], v[160:161], v[162:163] op_sel_hi:[1,0,0]\n\t" \
  "v_pk_fma_f32 v[84:85], v[84:85], v[160:161], v[162:163] op_sel_hi:[1,0,0]\n\t" \
  "v_pk_fma_f32 v[86:87], v[86:87], v[160:161], v[162:163] op_sel_hi:[1,0,0]\n\t" \
  "v_pk_fma_f32 v[88:89], v[88:89], v[160:161], v[162:163] op_sel_hi:[1,0,0]\n\t" \
  "v_pk_fma_f32 v[90:91], v[90:91], v[160:161], v[162:163] op_sel_hi:[1,0,0]\n\t" \
  "v_pk_fma_f32 v[92:93], v[92:93], v[160:161], v[162:163] op_sel_hi:[1,0,0]\n\t" \
  "v_pk_fma_f32 v[94:95], v[94:95], v[160:161], v[162:163] op_sel_hi:[1,0,0]\n\t" \
  "v_pk_fma_f32 v[96:97], v[96:97], v[160:161], v[162:163] op_sel_hi:[1,0,0]\n\t" \
  "v_pk_fma_f32 v[98:99], v[98:99], v[160:161], v[162:163] op_sel_hi:[1,0,0]\n\t" \
  "v_pk_fma_f32 v[100:101], v[100:101], v[160:161], v[162:163] op_sel_hi:[1,0,0]\n\t" \
  "v_pk_fma_f32 v[102:103], v[102:103], v[160:161], v[162:163] op_sel_hi:[1,0,0]\n\t" \
  "v_pk_fma_f32 v[104:105], v[104:105], v[160:161], v[162:163] op_sel_hi:[1,0,0]\n\t" \
  "v_pk_fma_f32 v[106:107], v[106:107], v[160:161], v[162:163] op_sel_hi:[1,0,0]\n\t" \
  "v_pk_fma_f32 v[108:109], v[108:109], v[160:161], v[162:163] op_sel_hi:[1,0,0]\n\t" \
  "v_pk_fma_f32 v[110:111], v[110:111], v[160:161], v[162:163] op_sel_hi:[1,0,0]\n\t" \
  "v_pk_fma_f32 v[112:113], v[112:113], v[160:161], v[162:163] op_sel_hi:[1,0,0]\n\t" \
  "v_pk_fma_f32 v[114:115], v[114:115], v[160:161], v[162:163] op_sel_hi:[1,0,0]\n\t" \
  "v_pk_fma_f32 v[116:117], v[116:117], v[160:161], v[162:163] op_sel_hi:[1,0,0]\n\t" \
  "s_waitcnt vmcnt(21)\n\t" \
  "v_readfirstlane_b32 s92, v30\n\t" \
  "v_readfirstlane_b32 s93, v31\n\t" \
  "s_lshl_b32 s94, s91, 10\n\t" \
  "s_add_u32 s94, s94, s97\n\t" \
  "s_add_u32 s95, s94, 4096\n\t" \
  "v_cvt_pk_bf16_f32 v118, v82, v83\n\t" \
  "v_cvt_pk_bf16_f32 v119, v84, v85\n\t" \
  "v_cvt_pk_bf16_f32 v120, v86, v87\n\t" \
  "v_cvt_pk_bf16_f32 v121, v88, v89\n\t" \
  "buffer_store_dwordx4 v[118:121], v17, s[16:19], s94 offen offset:0 sc1\n\t" \
  "v_cvt_pk_bf16_f32 v122, v90, v91\n\t" \
  "v_cvt_pk_bf16_f32 v123, v92, v93\n\t" \
  "v_cvt_pk_bf16_f32 v124, v94, v95\n\t" \
  "v_cvt_pk_bf16_f32 v125, v96, v97\n\t" \
  "buffer_store_dwordx4 v[122:125], v17, s[16:19], s94 offen offset:2048 sc1\n\t" \
  "v_cvt_pk_bf16_f32 v126, v98, v99\n\t" \
  "v_cvt_pk_bf16_f32 v127, v100, v101\n\t" \
  "v_cvt_pk_bf16_f32 v128, v102, v103\n\t" \
  "v_cvt_pk_bf16_f32 v129, v104, v105\n\t" \
  "buffer_store_dwordx4 v[126:129], v17, s[16:19], s95 offen offset:0 sc1\n\t" \
  "v_cvt_pk_bf16_f32 v130, v106, v107\n\t" \
  "v_cvt_pk_bf16_f32 v131, v108, v109\n\t" \
  "v_cvt_pk_bf16_f32 v132, v110, v111\n\t" \
  "v_cvt_pk_bf16_f32 v133, v112, v113\n\t" \
  "buffer_store_dwordx4 v[130:133], v17, s[16:19], s95 offen offset:2048 sc1\n\t" \
  "s_lshl_b32 s94, s91, 9\n\t" \
  "s_add_u32 s94, s94, s97\n\t" \
  "s_add_u32 s94, s94, 8192\n\t" \
  "v_cvt_pk_bf16_f32 v134, v114, v115\n\t" \
  "v_cvt_pk_bf16_f32 v135, v116, v117\n\t" \
  "buffer_store_dwordx2 v[134:135], v19, s[16:19], s94 offen sc1\n\t" \
  "s_add_u32 s92, s92, 1\n\t" \
  "s_addc_u32 s93, s93, 0\n\t" \
  "v_pk_add_f32 v[166:167], v[82:83], s[36:37]\n\t" \
  "v_pk_add_f32 v[168:169], v[84:85], s[36:37]\n\t" \
  "v_cvt_pk_bf16_f32 v136, v166, v167\n\t" \
  "v_pk_add_f32 v[170:171], v[86:87], s[36:37]\n\t" \
  "v_cvt_pk_bf16_f32 v137, v168, v169\n\t" \
  "v_pk_add_f32 v[172:173], v[88:89], s[36:37]\n\t" \
  "v_cvt_pk_bf16_f32 v138, v170, v171\n\t" \
  "v_pk_add_f32 v[174:175], v[90:91], s[36:37]\n\t" \
  "v_cvt_pk_bf16_f32 v139, v172, v173\n\t" \
  "v_pk_add_f32 v[176:177], v[92:93], s[36:37]\n\t" \
  "v_cvt_pk_bf16_f32 v140, v174, v175\n\t" \
  "v_pk_add_f32 v[178:179], v[94:95], s[36:37]\n\t" \
  "v_cvt_pk_bf16_f32 v141, v176, v177\n\t" \
  "v_pk_add_f32 v[180:181], v[96:97], s[36:37]\n\t" \
  "v_cvt_pk_bf16_f32 v142, v178, v179\n\t" \
  "v_pk_add_f32 v[166:167], v[98:99], s[36:37]\n\t" \
  "v_cvt_pk_bf16_f32 v143, v180, v181\n\t" \
  "v_pk_add_f32 v[168:169], v[100:101], s[36:37]\n\t" \
  "v_cvt_pk_bf16_f32 v144, v166, v167\n\t" \
  "v_pk_add_f32 v[170:171], v[102:103], s[36:37]\n\t" \
  "v_cvt_pk_bf16_f32 v145, v168, v169\n\t" \
  "v_pk_add_f32 v[172:173], v[104:105], s[36:37]\n\t" \
  "v_cvt_pk_bf16_f32 v146, v170, v171\n\t" \
  "v_pk_add_f32 v[174:175], v[106:107], s[36:37]\n\t" \
  "v_cvt_pk_bf16_f32 v147, v172, v173\n\t" \
  "v_pk_add_f32 v[176:177], v[108:109], s[36:37]\n\t" \
  "v_cvt_pk_bf16_f32 v148, v174, v175\n\t" \
  "v_pk_add_f32 v[178:179], v[110:111], s[36:37]\n\t" \
  "v_cvt_pk_bf16_f32 v149, v176, v177\n\t" \
  "v_pk_add_f32 v[180:181], v[112:113], s[36:37]\n\t" \
  "v_cvt_pk_bf16_f32 v150, v178, v179\n\t" \
  "v_pk_add_f32 v[166:167], v[114:115], s[36:37]\n\t" \
  "v_cvt_pk_bf16_f32 v151, v180, v181\n\t" \
  "v_pk_add_f32 v[168:169], v[116:117], s[36:37]\n\t" \
  "v_cvt_pk_bf16_f32 v152, v166, v167\n\t" \
  "v_cvt_pk_bf16_f32 v153, v168, v169\n\t" \
  "s_cmp_eq_u32 s46, 0\n\t" \
  "s_cbranch_scc1 NP2%=\n\t" \
  "ds_write_b128 v247, v[136:139] offset:0\n\t" \
  "ds_write_b128 v247, v[140:143] offset:1024\n\t" \
  "ds_write_b128 v247, v[144:147] offset:2048\n\t" \
  "ds_write_b128 v247, v[148:151] offset:3072\n\t" \
  "ds_write_b64 v247, v[152:153] offset:4096\n\t" \
  "NP2%=:\n\t" \
  "v_mov_b32_e32 v30, s92\n\t" \
  "v_mov_b32_e32 v31, s93\n\t" \
  "s_waitcnt vmcnt(5)\n\t" \
  "s_waitcnt lgkmcnt(0)\n\t" \
  "s_barrier\n\t" \
  "ds_read_b128 v[190:193], v247 offset:0\n\t" \
  "ds_read_b128 v[194:197], v247 offset:1024\n\t" \
  "ds_read_b128 v[198:201], v247 offset:2048\n\t" \
  "ds_read_b128 v[202:205], v247 offset:3072\n\t" \
  "ds_read_b64 v[206:207], v247 offset:4096\n\t" \
  "ds_read_b64_tr_b16 a[0:1], v24 offset:256\n\t" \
  "ds_read_b64_tr_b16 a[2:3], v24 offset:9472\n\t" \
  "ds_read_b64_tr_b16 a[18:19], v22 offset:320\n\t" \
  "ds_read_b64_tr_b16 a[20:21], v22 offset:9536\n\t" \
  "ds_read_b64_tr_b16 a[4:5], v24 offset:18688\n\t" \
  "ds_read_b64_tr_b16 a[6:7], v24 offset:27904\n\t" \
  "ds_read_b64_tr_b16 a[22:23], v22 offset:18752\n\t" \
  "ds_read_b64_tr_b16 a[24:25], v22 offset:27968\n\t" \
  "ds_read_b64_tr_b16 a[8:9], v24 offset:37120\n\t" \
  "ds_read_b64_tr_b16 a[10:11], v25 offset:256\n\t" \
  "ds_read_b64_tr_b16 a[26:27], v22 offset:37184\n\t" \
  "ds_read_b64_tr_b16 a[28:29], v23 offset:320\n\t" \
  "s_waitcnt lgkmcnt(10)\n\t" \
  "v_mfma_f32_16x16x32_bf16 v[154:157], v[190:193], a[0:3], 0\n\t" \
  "ds_read_b64_tr_b16 a[12:13], v25 offset:9472\n\t" \
  "ds_read_b64_tr_b16 a[14:15], v25 offset:18688\n\t" \
  "s_waitcnt lgkmcnt(10)\n\t" \
  "v_mfma_f32_16x16x32_bf16 v[158:161], v[190:193], a[18:21], 0\n\t" \
  "ds_read_b64_tr_b16 a[30:31], v23 offset:9536\n\t" \
  "ds_read_b64_tr_b16 a[32:33], v23 offset:18752\n\t" \
  "s_waitcnt lgkmcnt(10)\n\t" \
  "v_mfma_f32_16x16x32_bf16 v[154:157], v[194:197], a[4:7], v[154:157]\n\t" \
  "ds_read_b64_tr_b16 a[16:17], v25 offset:27904\n\t" \
  "ds_read_b64_tr_b16 a[34:35], v23 offset:27968\n\t" \
  "s_waitcnt lgkmcnt(8)\n\t" \
  "v_mfma_f32_16x16x32_bf16 v[158:161], v[194:197], a[22:25], v[158:161]\n\t" \
  "ds_read_b64_tr_b16 a[36:37], v24 offset:320\n\t" \
  "ds_read_b64_tr_b16 a[38:39], v24 offset:9536\n\t" \
  "v_mfma_f32_16x16x32_bf16 v[154:157], v[198:201], a[8:11], v[154:157]\n\t" \
  "ds_read_b64_tr_b16 a[54:55], v22 offset:384\n\t" \
  "ds_read_b64_tr_b16 a[56:57], v22 offset:9600\n\t" \
  "s_waitcnt lgkmcnt(8)\n\t" \
  "v_mfma_f32_16x16x32_bf16 v[158:161], v[198:201], a[26:29], v[158:161]\n\t" \
  "ds_read_b64_tr_b16 a[40:41], v24 offset:18752\n\t" \
  "ds_read_b64_tr_b16 a[42:43], v24 offset:27968\n\t" \
  "v_mfma_f32_16x16x32_bf16 v[154:157], v[202:205], a[12:15], v[154:157]\n\t" \
  "ds_read_b64_tr_b16 a[58:59], v22 offset:18816\n\t" \
  "ds_read_b64_tr_b16 a[60:61], v22 offset:28032\n\t" \
  "s_waitcnt lgkmcnt(8)\n\t" \
  "v_mfma_f32_16x16x32_bf16 v[158:161], v[202:205], a[30:33], v[158:161]\n\t" \
  "ds_read_b64_tr_b16 a[44:45], v24 offset:37184\n\t" \
  "ds_read_b64_tr_b16 a[46:47], v25 offset:320\n\t" \
  "v_mfma_f32_16x16x16_bf16 v[154:157], v[206:207], a[16:17], v[154:157]\n\t" \
  "ds_read_b64_tr_b16 a[62:63], v22 offset:37248\n\t" \
  "ds_read_b64_tr_b16 a[64:65], v23 offset:384\n\t" \
  "v_mfma_f32_16x16x16_bf16 v[158:161], v[206:207], a[34:35], v[158:161]\n\t" \
  "ds_read_b64_tr_b16 a[48:49], v25 offset:9536\n\t" \
  "ds_read_b64_tr_b16 a[50:51], v25 offset:18752\n\t" \
  "s_waitcnt lgkmcnt(9)\n\t" \
  "v_mfma_f32_16x16x32_bf16 v[162:165], v[190:193], a[36:39], 0\n\t" \
  "ds_read_b64_tr_b16 a[66:67], v23 offset:9600\n\t" \
  "ds_read_b64_tr_b16 a[68:69], v23 offset:18816\n\t" \
  "v_mfma_f32_16x16x32_bf16 v[166:169], v[190:193], a[54:57], 0\n\t" \
  "ds_read_b64_tr_b16 a[52:53], v25 offset:27968\n\t" \
  "ds_read_b64_tr_b16 a[70:71], v23 offset:28032\n\t" \
  "s_waitcnt lgkmcnt(9)\n\t" \
  "v_mfma_f32_16x16x32_bf16 v[162:165], v[194:197], a[40:43], v[162:165]\n\t" \
  "ds_read_b64_tr_b16 a[72:73], v28 offset:512\n\t" \
  "ds_read_b64_tr_b16 a[74:75], v28 offset:9728\n\t" \
  "v_mfma_f32_16x16x32_bf16 v[166:169], v[194:197], a[58:61], v[166:169]\n\t" \
  "ds_read_b64_tr_b16 a[90:91], v22 offset:0\n\t" \
  "ds_read_b64_tr_b16 a[92:93], v22 offset:9216\n\t" \
  "s_waitcnt lgkmcnt(8)\n\t" \
  "v_mfma_f32_16x16x32_bf16 v[162:165], v[198:201], a[44:47], v[162:165]\n\t" \
  "ds_read_b64_tr_b16 a[76:77], v28 offset:18944\n\t" \
  "ds_read_b64_tr_b16 a[78:79], v28 offset:28160\n\t" \
  "v_mfma_f32_16x16x32_bf16 v[166:169], v[198:201], a[62:65], v[166:169]\n\t" \
  "ds_read_b64_tr_b16 a[94:95], v22 offset:18432\n\t" \
  "ds_read_b64_tr_b16 a[96:97], v22 offset:27648\n\t" \
  "v_mfma_f32_16x16x32_bf16 v[162:165], v[202:205], a[48:51], v[162:165]\n\t" \
  "ds_read_b64_tr_b16 a[80:81], v28 offset:37376\n\t" \
  "ds_read_b64_tr_b16 a[82:83], v29 offset:512\n\t" \
  "s_waitcnt lgkmcnt(9)\n\t" \
  "v_mfma_f32_16x16x32_bf16 v[166:169], v[202:205], a[66:69], v[166:169]\n\t" \
  "ds_read_b64_tr_b16 a[98:99], v22 offset:36864\n\t" \
  "ds_read_b64_tr_b16 a[100:101], v23 offset:0\n\t" \
  "v_mfma_f32_16x16x16_bf16 v[162:165], v[206:207], a[52:53], v[162:165]\n\t" \
  "ds_read_b64_tr_b16 a[84:85], v29 offset:9728\n\t" \
  "ds_read_b64_tr_b16 a[86:87], v29 offset:18944\n\t" \
  "v_mfma_f32_16x16x16_bf16 v[166:169], v[206:207], a[70:71], v[166:169]\n\t" \
  "ds_read_b64_tr_b16 a[102:103], v23 offset:9216\n\t" \
  "ds_read_b64_tr_b16 a[104:105], v23 offset:18432\n\t" \
  "s_waitcnt lgkmcnt(9)\n\t" \
  "v_mfma_f32_16x16x32_bf16 v[170:173], v[190:193], a[72:75], 0\n\t" \
  "ds_read_b64_tr_b16 a[88:89], v29 offset:28160\n\t" \
  "ds_read_b64_tr_b16 a[106:107], v23 offset:27648\n\t" \
  "v_mfma_f32_16x16x32_bf16 v[208:211], a[90:93], v[190:193], 0\n\t" \
  "ds_read_b64_tr_b16 a[108:109], v24 offset:0\n\t" \
  "ds_read_b64_tr_b16 a[110:111], v24 offset:9216\n\t" \
  "v_mfma_f32_16x16x32_bf16 v[170:173], v[194:197], a[76:79], v[170:173]\n\t" \
  "ds_read_b64_tr_b16 a[126:127], v22 offset:64\n\t" \
  "ds_read_b64_tr_b16 a[128:129], v22 offset:9280\n\t" \
  "s_waitcnt lgkmcnt(9)\n\t" \
  "v_mfma_f32_16x16x32_bf16 v[208:211], a[94:97], v[194:197], v[208:211]\n\t" \
  "ds_read_b64_tr_b16 a[112:113], v24 offset:18432\n\t" \
  "ds_read_b64_tr_b16 a[114:115], v24 offset:27648\n\t" \
  "v_mfma_f32_16x16x32_bf16 v[170:173], v[198:201], a[80:83], v[170:173]\n\t" \
  "ds_read_b64_tr_b16 a[130:131], v22 offset:18496\n\t" \
  "ds_read_b64_tr_b16 a[132:133], v22 offset:27712\n\t" \
  "v_mfma_f32_16x16x32_bf16 v[208:211], a[98:101], v[198:201], v[208:211]\n\t" \
  "ds_read_b64_tr_b16 a[116:117], v24 offset:36864\n\t" \
  "ds_read_b64_tr_b16 a[118:119], v25 offset:0\n\t" \
  "s_waitcnt lgkmcnt(9)\n\t" \
  "v_mfma_f32_16x16x32_bf16 v[170:173], v[202:205], a[84:87], v[170:173]\n\t" \
  "ds_read_b64_tr_b16 a[134:135], v22 offset:36928\n\t" \
  "ds_read_b64_tr_b16 a[136:137], v23 offset:64\n\t" \
  "v_mfma_f32_16x16x32_bf16 v[208:211], a[102:105], v[202:205], v[208:211]\n\t" \
  "ds_read_b64_tr_b16 a[120:121], v25 offset:9216\n\t" \
  "ds_read_b64_tr_b16 a[122:123], v25 offset:18432\n\t" \
  "v_mfma_f32_16x16x16_bf16 v[170:173], v[206:207], a[88:89], v[170:173]\n\t" \
  "ds_read_b64_tr_b16 a[138:139], v23 offset:9280\n\t" \
  "ds_read_b64_tr_b16 a[140:141], v23 offset:18496\n\t" \
  "v_mfma_f32_16x16x16_bf16 v[208:211], a[106:107], v[206:207], v[208:211]\n\t" \
  "s_waitcnt vmcnt(0)\n\t" \
  "buffer_store_dwordx2 v[30:31], v1, s[16:19], 0 offen sc1\n\t" \
  "ds_read_b64_tr_b16 a[124:125], v25 offset:27648\n\t" \
  "ds_read_b64_tr_b16 a[142:143], v23 offset:27712\n\t" \
  "s_waitcnt lgkmcnt(8)\n\t" \
  "v_mfma_f32_16x16x32_bf16 v[212:215], a[108:111], v[190:193], 0\n\t" \
  "ds_read_b64_tr_b16 a[144:145], v24 offset:64\n\t" \
  "ds_read_b64_tr_b16 a[146:147], v24 offset:9280\n\t" \
  "v_mfma_f32_16x16x32_bf16 v[216:219], a[126:129], v[190:193], 0\n\t" \
  "ds_read_b64_tr_b16 a[162:163], v26 offset:256\n\t" \
  "ds_read_b64_tr_b16 a[164:165], v26 offset:9472\n\t" \
  "v_mfma_f32_16x16x32_bf16 v[212:215], a[112:115], v[194:197], v[212:215]\n\t" \
  "ds_read_b64_tr_b16 a[148:149], v24 offset:18496\n\t" \
  "ds_read_b64_tr_b16 a[150:151], v24 offset:27712\n\t" \
  "v_mfma_f32_16x16x32_bf16 v[216:219], a[130:133], v[194:197], v[216:219]\n\t" \
  "ds_read_b64_tr_b16 a[166:167], v26 offset:18688\n\t" \
  "ds_read_b64_tr_b16 a[168:169], v26 offset:27904\n\t" \
  "v_mfma_f32_16x16x32_bf16 v[212:215], a[116:119], v[198:201], v[212:215]\n\t" \
  "ds_read_b64_tr_b16 a[152:153], v24 offset:36928\n\t" \
  "ds_read_b64_tr_b16 a[154:155], v25 offset:64\n\t" \
  "s_waitcnt lgkmcnt(10)\n\t" \
  "v_mfma_f32_16x16x32_bf16 v[216:219], a[134:137], v[198:201], v[216:219]\n\t" \
  "ds_read_b64_tr_b16 a[170:171], v26 offset:37120\n\t" \
  "ds_read_b64_tr_b16 a[172:173], v27 offset:256\n\t" \
  "v_mfma_f32_16x16x32_bf16 v[212:215], a[120:123], v[202:205], v[212:215]\n\t" \
  "ds_read_b64_tr_b16 a[156:157], v25 offset:9280\n\t" \
  "ds_read_b64_tr_b16 a[158:159], v25 offset:18496\n\t" \
  "v_mfma_f32_16x16x32_bf16 v[216:219], a[138:141], v[202:205], v[216:219]\n\t" \
  "ds_read_b64_tr_b16 a[174:175], v27 offset:9472\n\t" \
  "ds_read_b64_tr_b16 a[176:177], v27 offset:18688\n\t" \
  "v_mfma_f32_16x16x16_bf16 v[212:215], a[124:125], v[206:207], v[212:215]\n\t" \
  "ds_read_b64_tr_b16 a[160:161], v25 offset:27712\n\t" \
  "ds_read_b64_tr_b16 a[178:179], v27 offset:27904\n\t" \
  "v_mfma_f32_16x16x16_bf16 v[216:219], a[142:143], v[206:207], v[216:219]\n\t" \
  "s_waitcnt lgkmcnt(8)\n\t" \
  "v_mfma_f32_16x16x32_bf16 v[220:223], a[144:147], v[190:193], 0\n\t" \
  "v_mfma_f32_16x16x32_bf16 v[224:227], a[162:165], v[190:193], 0\n\t" \
  "v_mfma_f32_16x16x32_bf16 v[220:223], a[148:151], v[194:197], v[220:223]\n\t" \
  "v_mfma_f32_16x16x32_bf16 v[224:227], a[166:169], v[194:197], v[224:227]\n\t" \
  "v_mfma_f32_16x16x32_bf16 v[220:223], a[152:155], v[198:201], v[220:223]\n\t" \
  "s_waitcnt lgkmcnt(5)\n\t" \
  "v_mfma_f32_16x16x32_bf16 v[224:227], a[170:173], v[198:201], v[224:227]\n\t" \
  "s_waitcnt lgkmcnt(4)\n\t" \
  "v_mfma_f32_16x16x32_bf16 v[220:223], a[156:159], v[202:205], v[220:223]\n\t" \
  "s_waitcnt lgkmcnt(2)\n\t" \
  "v_mfma_f32_16x16x32_bf16 v[224:227], a[174:177], v[202:205], v[224:227]\n\t" \
  "s_waitcnt lgkmcnt(1)\n\t" \
  "v_mfma_f32_16x16x16_bf16 v[220:223], a[160:161], v[206:207], v[220:223]\n\t" \
  "s_waitcnt lgkmcnt(0)\n\t" \
  "v_mfma_f32_16x16x16_bf16 v[224:227], a[178:179], v[206:207], v[224:227]\n\t" \
  "v_pk_mul_f32 v[174:175], v[154:155], v[154:155]\n\t" \
  "v_pk_mul_f32 v[176:177], v[156:157], v[156:157]\n\t" \
  "v_pk_fma_f32 v[174:175], v[174:175], s[32:33], v[20:21] op_sel_hi:[1,1,0]\n\t" \
  "v_pk_fma_f32 v[176:177], v[176:177], s[32:33], v[20:21] op_sel_hi:[1,1,0]\n\t" \
  "v_pk_mul_f32 v[174:175], v[174:175], v[154:155]\n\t" \
  "v_pk_mul_f32 v[176:177], v[176:177], v[156:157]\n\t" \
  "v_exp_f32_e32 v174, v174\n\t" \
  "v_exp_f32_e32 v175, v175\n\t" \
  "v_exp_f32_e32 v176, v176\n\t" \
  "v_exp_f32_e32 v177, v177\n\t" \
  "v_pk_add_f32 v[174:175], v[174:175], s[34:35]\n\t" \
  "v_pk_add_f32 v[176:177], v[176:177], s[34:35]\n\t" \
  "v_rcp_f32_e32 v174, v174\n\t" \
  "v_rcp_f32_e32 v175, v175\n\t" \
  "v_rcp_f32_e32 v176, v176\n\t" \
  "v_rcp_f32_e32 v177, v177\n\t" \
  "v_pk_mul_f32 v[154:155], v[154:155], v[174:175]\n\t" \
  "v_pk_mul_f32 v[156:157], v[156:157], v[176:177]\n\t" \
  "v_pk_mul_f32 v[178:179], v[158:159], v[158:159]\n\t" \
  "v_pk_mul_f32 v[180:181], v[160:161], v[160:161]\n\t" \
  "v_pk_fma_f32 v[178:179], v[178:179], s[32:33], v[20:21] op_sel_hi:[1,1,0]\n\t" \
  "v_pk_fma_f32 v[180:181], v[180:181], s[32:33], v[20:21] op_sel_hi:[1,1,0]\n\t" \
  "v_pk_mul_f32 v[178:179], v[178:179], v[158:159]\n\t" \
  "v_pk_mul_f32 v[180:181], v[180:181], v[160:161]\n\t" \
  "v_exp_f32_e32 v178, v178\n\t" \
  "v_exp_f32_e32 v179, v179\n\t" \
  "v_exp_f32_e32 v180, v180\n\t" \
  "v_exp_f32_e32 v181, v181\n\t" \
  "v_pk_add_f32 v[178:179], v[178:179], s[34:35]\n\t" \
  "v_pk_add_f32 v[180:181], v[180:181], s[34:35]\n\t" \
  "s_nop 0\n\t" \
  "buffer_load_dwordx2 v[46:47], v255, s[16:19], 0 offen sc1\n\t" \
  "v_rcp_f32_e32 v178, v178\n\t" \
  "v_rcp_f32_e32 v179, v179\n\t" \
  "v_rcp_f32_e32 v180, v180\n\t" \
  "v_rcp_f32_e32 v181, v181\n\t" \
  "v_pk_mul_f32 v[158:159], v[158:159], v[178:179]\n\t" \
  "v_pk_mul_f32 v[160:161], v[160:161], v[180:181]\n\t" \
  "v_pk_mul_f32 v[174:175], v[162:163], v[162:163]\n\t" \
  "v_pk_mul_f32 v[176:177], v[164:165], v[164:165]\n\t" \
  "v_pk_fma_f32 v[174:175], v[174:175], s[32:33], v[20:21] op_sel_hi:[1,1,0]\n\t" \
  "v_pk_fma_f32 v[176:177], v[176:177], s[32:33], v[20:21] op_sel_hi:[1,1,0]\n\t" \
  "v_pk_mul_f32 v[174:175], v[174:175], v[162:163]\n\t" \
  "v_pk_mul_f32 v[176:177], v[176:177], v[164:165]\n\t" \
  "v_exp_f32_e32 v174, v174\n\t" \
  "v_exp_f32_e32 v175, v175\n\t" \
  "v_exp_f32_e32 v176, v176\n\t" \
  "v_exp_f32_e32 v177, v177\n\t" \
  "v_pk_add_f32 v[174:175], v[174:175], s[34:35]\n\t" \
  "v_pk_add_f32 v[176:177], v[176:177], s[34:35]\n\t" \
  "v_rcp_f32_e32 v174, v174\n\t" \
  "v_rcp_f32_e32 v175, v175\n\t" \
  "v_rcp_f32_e32 v176, v176\n\t" \
  "v_rcp_f32_e32 v177, v177\n\t" \
  "v_pk_mul_f32 v[162:163], v[162:163], v[174:175]\n\t" \
  "v_pk_mul_f32 v[164:165], v[164:165], v[176:177]\n\t" \
  "v_pk_mul_f32 v[178:179], v[166:167], v[166:167]\n\t" \
  "v_pk_mul_f32 v[180:181], v[168:169], v[168:169]\n\t" \
  "v_pk_fma_f32 v[178:179], v[178:179], s[32:33], v[20:21] op_sel_hi:[1,1,0]\n\t" \
  "v_pk_fma_f32 v[180:181], v[180:181], s[32:33], v[20:21] op_sel_hi:[1,1,0]\n\t" \
  "v_pk_mul_f32 v[178:179], v[178:179], v[166:167]\n\t" \
  "v_pk_mul_f32 v[180:181], v[180:181], v[168:169]\n\t" \
  "v_exp_f32_e32 v178, v178\n\t" \
  "v_exp_f32_e32 v179, v179\n\t" \
  "v_exp_f32_e32 v180, v180\n\t" \
  "v_exp_f32_e32 v181, v181\n\t" \
  "v_pk_add_f32 v[178:179], v[178:179], s[34:35]\n\t" \
  "v_pk_add_f32 v[180:181], v[180:181], s[34:35]\n\t" \
  "v_rcp_f32_e32 v178, v178\n\t" \
  "v_rcp_f32_e32 v179, v179\n\t" \
  "v_rcp_f32_e32 v180, v180\n\t" \
  "v_rcp_f32_e32 v181, v181\n\t" \
  "v_pk_mul_f32 v[166:167], v[166:167], v[178:179]\n\t" \
  "v_pk_mul_f32 v[168:169], v[168:169], v[180:181]\n\t" \
  "v_pk_mul_f32 v[174:175], v[170:171], v[170:171]\n\t" \
  "v_pk_mul_f32 v[176:177], v[172:173], v[172:173]\n\t" \
  "v_pk_fma_f32 v[174:175], v[174:175], s[32:33], v[20:21] op_sel_hi:[1,1,0]\n\t" \
  "v_pk_fma_f32 v[176:177], v[176:177], s[32:33], v[20:21] op_sel_hi:[1,1,0]\n\t" \
  "v_pk_mul_f32 v[174:175], v[174:175], v[170:171]\n\t" \
  "v_pk_mul_f32 v[176:177], v[176:177], v[172:173]\n\t" \
  "v_exp_f32_e32 v174, v174\n\t" \
  "v_exp_f32_e32 v175, v175\n\t" \
  "v_exp_f32_e32 v176, v176\n\t" \
  "v_exp_f32_e32 v177, v177\n\t" \
  "v_pk_add_f32 v[174:175], v[174:175], s[34:35]\n\t" \
  "v_pk_add_f32 v[176:177], v[176:177], s[34:35]\n\t" \
  "v_rcp_f32_e32 v174, v174\n\t" \
  "v_rcp_f32_e32 v175, v175\n\t" \
  "v_rcp_f32_e32 v176, v176\n\t" \
  "v_rcp_f32_e32 v177, v177\n\t" \
  "v_pk_mul_f32 v[170:171], v[170:171], v[174:175]\n\t" \
  "v_pk_mul_f32 v[172:173], v[172:173], v[176:177]\n\t" \
  "v_pk_add_f32 v[182:183], v[154:155], v[158:159]\n\t" \
  "v_pk_mul_f32 v[186:187], v[154:155], v[154:155]\n\t" \
  "v_pk_add_f32 v[184:185], v[156:157], v[160:161]\n\t" \
  "v_pk_mul_f32 v[188:189], v[156:157], v[156:157]\n\t" \
  "v_pk_fma_f32 v[186:187], v[158:159], v[158:159], v[186:187]\n\t" \
  "v_pk_fma_f32 v[188:189], v[160:161], v[160:161], v[188:189]\n\t" \
  "v_pk_add_f32 v[182:183], v[182:183], v[162:163]\n\t" \
  "v_pk_fma_f32 v[186:187], v[162:163], v[162:163], v[186:187]\n\t" \
  "v_pk_add_f32 v[184:185], v[184:185], v[164:165]\n\t" \
  "v_pk_fma_f32 v[188:189], v[164:165], v[164:165], v[188:189]\n\t" \
  "v_pk_add_f32 v[182:183], v[182:183], v[166:167]\n\t" \
  "v_pk_fma_f32 v[186:187], v[166:167], v[166:167], v[186:187]\n\t" \
  "v_pk_add_f32 v[184:185], v[184:185], v[168:169]\n\t" \
  "v_pk_fma_f32 v[188:189], v[168:169], v[168:169], v[188:189]\n\t" \
  "s_cmp_eq_u32 s45, 1\n\t" \
  "s_cbranch_scc1 NE1%=\n\t" \
  "v_pk_add_f32 v[182:183], v[182:183], v[170:171]\n\t" \
  "v_pk_fma_f32 v[186:187], v[170:171], v[170:171], v[186:187]\n\t" \
  "v_pk_add_f32 v[184:185], v[184:185], v[172:173]\n\t" \
  "v_pk_fma_f32 v[188:189], v[172:173], v[172:173], v[188:189]\n\t" \
  "NE1%=:\n\t" \
  "v_add_f32_dpp v182, v182, v182 row_ror:8 row_mask:0xf bank_mask:0xf\n\t" \
  "v_add_f32_dpp v183, v183, v183 row_ror:8 row_mask:0xf bank_mask:0xf\n\t" \
  "v_add_f32_dpp v184, v184, v184 row_ror:8 row_mask:0xf bank_mask:0xf\n\t" \
  "v_add_f32_dpp v185, v185, v185 row_ror:8 row_mask:0xf bank_mask:0xf\n\t" \
  "v_add_f32_dpp v186, v186, v186 row_ror:8 row_mask:0xf bank_mask:0xf\n\t" \
  "v_add_f32_dpp v187, v187, v187 row_ror:8 row_mask:0xf bank_mask:0xf\n\t" \
  "v_add_f32_dpp v188, v188, v188 row_ror:8 row_mask:0xf bank_mask:0xf\n\t" \
  "v_add_f32_dpp v189, v189, v189 row_ror:8 row_mask:0xf bank_mask:0xf\n\t" \
  "v_add_f32_dpp v182, v182, v182 row_ror:4 row_mask:0xf bank_mask:0xf\n\t" \
  "v_add_f32_dpp v183, v183, v183 row_ror:4 row_mask:0xf bank_mask:0xf\n\t" \
  "v_add_f32_dpp v184, v184, v184 row_ror:4 row_mask:0xf bank_mask:0xf\n\t" \
  "v_add_f32_dpp v185, v185, v185 row_ror:4 row_mask:0xf bank_mask:0xf\n\t" \
  "v_add_f32_dpp v186, v186, v186 row_ror:4 row_mask:0xf bank_mask:0xf\n\t" \
  "v_add_f32_dpp v187, v187, v187 row_ror:4 row_mask:0xf bank_mask:0xf\n\t" \
  "v_add_f32_dpp v188, v188, v188 row_ror:4 row_mask:0xf bank_mask:0xf\n\t" \
  "v_add_f32_dpp v189, v189, v189 row_ror:4 row_mask:0xf bank_mask:0xf\n\t" \
  "v_add_f32_dpp v182, v182, v182 row_ror:2 row_mask:0xf bank_mask:0xf\n\t" \
  "v_add_f32_dpp v183, v183, v183 row_ror:2 row_mask:0xf bank_mask:0xf\n\t" \
  "v_add_f32_dpp v184, v184, v184 row_ror:2 row_mask:0xf bank_mask:0xf\n\t" \
  "v_add_f32_dpp v185, v185, v185 row_ror:2 row_mask:0xf bank_mask:0xf\n\t" \
  "v_add_f32_dpp v186, v186, v186 row_ror:2 row_mask:0xf bank_mask:0xf\n\t" \
  "v_add_f32_dpp v187, v187, v187 row_ror:2 row_mask:0xf bank_mask:0xf\n\t" \
  "v_add_f32_dpp v188, v188, v188 row_ror:2 row_mask:0xf bank_mask:0xf\n\t" \
  "v_add_f32_dpp v189, v189, v189 row_ror:2 row_mask:0xf bank_mask:0xf\n\t" \
  "v_add_f32_dpp v182, v182, v182 row_ror:1 row_mask:0xf bank_mask:0xf\n\t" \
  "v_add_f32_dpp v183, v183, v183 row_ror:1 row_mask:0xf bank_mask:0xf\n\t" \
  "v_add_f32_dpp v184, v184, v184 row_ror:1 row_mask:0xf bank_mask:0xf\n\t" \
  "v_add_f32_dpp v185, v185, v185 row_ror:1 row_mask:0xf bank_mask:0xf\n\t" \
  "v_add_f32_dpp v186, v186, v186 row_ror:1 row_mask:0xf bank_mask:0xf\n\t" \
  "v_add_f32_dpp v187, v187, v187 row_ror:1 row_mask:0xf bank_mask:0xf\n\t" \
  "v_add_f32_dpp v188, v188, v188 row_ror:1 row_mask:0xf bank_mask:0xf\n\t" \
  "v_add_f32_dpp v189, v189, v189 row_ror:1 row_mask:0xf bank_mask:0xf\n\t" \
  "s_mov_b32 exec_lo, 0x00010001\n\t" \
  "s_mov_b32 exec_hi, 0x00010001\n\t" \
  "ds_write_b128 v249, v[182:185]\n\t" \
  "ds_write_b128 v249, v[186:189] offset:16\n\t" \
  "s_mov_b64 exec, -1\n\t" \
  "s_waitcnt vmcnt(0)\n\t" \
  "v_cmp_ne_u64_e32 vcc, s[92:93], v[46:47]\n\t" \
  "v_xor_b32_e32 v249, 256, v249\n\t" \
  "s_cbranch_vccnz POLLM%=\n\t" \
  "BACKM%=:\n\t" \
  "buffer_load_dwordx2 v[60:61], v253, s[16:19], 0 offen sc1\n\t" \
  "s_add_u32 m0, s49, 119808\n\t" \
  "s_add_u32 s99, s49, 9600512\n\t" \
  "buffer_load_dwordx4 v59, s[16:19], s99 offen offset:0 sc1 lds\n\t" \
  "v_pk_mul_f32 v[174:175], v[208:209], v[208:209]\n\t" \
  "v_pk_mul_f32 v[176:177], v[210:211], v[210:211]\n\t" \
  "v_pk_fma_f32 v[174:175], v[174:175], s[32:33], v[20:21] op_sel_hi:[1,1,0]\n\t" \
  "v_pk_fma_f32 v[176:177], v[176:177], s[32:33], v[20:21] op_sel_hi:[1,1,0]\n\t" \
  "v_pk_mul_f32 v[174:175], v[174:175], v[208:209]\n\t" \
  "v_pk_mul_f32 v[176:177], v[176:177], v[210:211]\n\t" \
  "v_exp_f32_e32 v174, v174\n\t" \
  "buffer_load_dwordx4 v59, s[16:19], s99 offen offset:1024 sc1 lds\n\t" \
  "v_exp_f32_e32 v175, v175\n\t" \
  "v_exp_f32_e32 v176, v176\n\t" \
  "v_exp_f32_e32 v177, v177\n\t" \
  "v_pk_add_f32 v[174:175], v[174:175], s[34:35]\n\t" \
  "buffer_load_dwordx4 v59, s[16:19], s99 offen offset:2048 sc1 lds\n\t" \
  "v_pk_add_f32 v[176:177], v[176:177], s[34:35]\n\t" \
  "v_rcp_f32_e32 v174, v174\n\t" \
  "v_rcp_f32_e32 v175, v175\n\t" \
  "v_rcp_f32_e32 v176, v176\n\t" \
  "buffer_load_dwordx4 v59, s[16:19], s99 offen offset:3072 sc1 lds\n\t" \
  "v_rcp_f32_e32 v177, v177\n\t" \
  "v_pk_mul_f32 v[208:209], v[208:209], v[174:175]\n\t" \
  "v_pk_mul_f32 v[210:211], v[210:211], v[176:177]\n\t" \
  "v_pk_mul_f32 v[178:179], v[212:213], v[212:213]\n\t" \
  "v_pk_mul_f32 v[180:181], v[214:215], v[214:215]\n\t" \
  "v_pk_fma_f32 v[178:179], v[178:179], s[32:33], v[20:21] op_sel_hi:[1,1,0]\n\t" \
  "s_add_u32 m0, s49, 123904\n\t" \
  "s_add_u32 s99, s49, 9604608\n\t" \
  "buffer_load_dwordx4 v59, s[16:19], s99 offen offset:0 sc1 lds\n\t" \
  "v_pk_fma_f32 v[180:181], v[180:181], s[32:33], v[20:21] op_sel_hi:[1,1,0]\n\t" \
  "v_pk_mul_f32 v[178:179], v[178:179], v[212:213]\n\t" \
  "v_pk_mul_f32 v[180:181], v[180:181], v[214:215]\n\t" \
  "v_exp_f32_e32 v178, v178\n\t" \
  "v_exp_f32_e32 v179, v179\n\t" \
  "buffer_load_dwordx4 v59, s[16:19], s99 offen offset:1024 sc1 lds\n\t" \
  "v_exp_f32_e32 v180, v180\n\t" \
  "v_exp_f32_e32 v181, v181\n\t" \
  "v_pk_add_f32 v[178:179], v[178:179], s[34:35]\n\t" \
  "v_pk_add_f32 v[180:181], v[180:181], s[34:35]\n\t" \
  "v_rcp_f32_e32 v178, v178\n\t" \
  "buffer_load_dwordx4 v59, s[16:19], s99 offen offset:2048 sc1 lds\n\t" \
  "v_rcp_f32_e32 v179, v179\n\t" \
  "v_rcp_f32_e32 v180, v180\n\t" \
  "v_rcp_f32_e32 v181, v181\n\t" \
  "v_pk_mul_f32 v[212:213], v[212:213], v[178:179]\n\t" \
  "buffer_load_dwordx4 v59, s[16:19], s99 offen offset:3072 sc1 lds\n\t" \
  "v_pk_mul_f32 v[214:215], v[214:215], v[180:181]\n\t" \
  "v_pk_mul_f32 v[174:175], v[216:217], v[216:217]\n\t" \
  "v_pk_mul_f32 v[176:177], v[218:219], v[218:219]\n\t" \
  "v_pk_fma_f32 v[174:175], v[174:175], s[32:33], v[20:21] op_sel_hi:[1,1,0]\n\t" \
  "v_pk_fma_f32 v[176:177], v[176:177], s[32:33], v[20:21] op_sel_hi:[1,1,0]\n\t" \
  "v_pk_mul_f32 v[174:175], v[174:175], v[216:217]\n\t" \
  "v_pk_mul_f32 v[176:177], v[176:177], v[218:219]\n\t" \
  "s_add_u32 m0, s49, 128000\n\t" \
  "s_add_u32 s99, s49, 9608704\n\t" \
  "buffer_load_dwordx4 v59, s[16:19], s99 offen offset:0 sc1 lds\n\t" \
  "v_exp_f32_e32 v174, v174\n\t" \
  "v_exp_f32_e32 v175, v175\n\t" \
  "v_exp_f32_e32 v176, v176\n\t" \
  "v_exp_f32_e32 v177, v177\n\t" \
  "buffer_load_dwordx4 v59, s[16:19], s99 offen offset:1024 sc1 lds\n\t" \
  "v_pk_add_f32 v[174:175], v[174:175], s[34:35]\n\t" \
  "v_pk_add_f32 v[176:177], v[176:177], s[34:35]\n\t" \
  "v_rcp_f32_e32 v174, v174\n\t" \
  "v_rcp_f32_e32 v175, v175\n\t" \
  "v_rcp_f32_e32 v176, v176\n\t" \
  "buffer_load_dwordx4 v59, s[16:19], s99 offen offset:2048 sc1 lds\n\t" \
  "v_rcp_f32_e32 v177, v177\n\t" \
  "v_pk_mul_f32 v[216:217], v[216:217], v[174:175]\n\t" \
  "v_pk_mul_f32 v[218:219], v[218:219], v[176:177]\n\t" \
  "s_waitcnt lgkmcnt(0)\n\t" \
  "s_barrier\n\t" \
  "s_waitcnt vmcnt(11)\n\t" \
  "v_cmp_ne_u64_e32 vcc, s[92:93], v[60:61]\n\t" \
  "ds_read_b128 v[190:193], v249\n\t" \
  "ds_read_b128 v[194:197], v249 offset:16\n\t" \
  "s_cbranch_vccnz POLLA%=\n\t" \
  "BACKA%=:\n\t" \
  "s_add_u32 m0, s58, 0\n\t" \
  "s_nop 0\n\t" \
  "buffer_load_dwordx4 v59, s[16:19], s0 offen offset:0 sc1 lds\n\t" \
  "v_pk_mul_f32 v[178:179], v[220:221], v[220:221]\n\t" \
  "v_pk_mul_f32 v[180:181], v[222:223], v[222:223]\n\t" \
  "v_pk_fma_f32 v[178:179], v[178:179], s[32:33], v[20:21] op_sel_hi:[1,1,0]\n\t" \
  "v_pk_fma_f32 v[180:181], v[180:181], s[32:33], v[20:21] op_sel_hi:[1,1,0]\n\t" \
  "v_pk_mul_f32 v[178:179], v[178:179], v[220:221]\n\t" \
  "v_pk_mul_f32 v[180:181], v[180:181], v[222:223]\n\t" \
  "v_exp_f32_e32 v178, v178\n\t" \
  "v_exp_f32_e32 v179, v179\n\t" \
  "buffer_load_dwordx4 v59, s[16:19], s0 offen offset:1024 sc1 lds\n\t" \
  "v_exp_f32_e32 v180, v180\n\t" \
  "v_exp_f32_e32 v181, v181\n\t" \
  "v_pk_add_f32 v[178:179], v[178:179], s[34:35]\n\t" \
  "v_pk_add_f32 v[180:181], v[180:181], s[34:35]\n\t" \
  "v_rcp_f32_e32 v178, v178\n\t" \
  "v_rcp_f32_e32 v179, v179\n\t" \
  "buffer_load_dwordx4 v59, s[16:19], s0 offen offset:2048 sc1 lds\n\t" \
  "v_rcp_f32_e32 v180, v180\n\t" \
  "v_rcp_f32_e32 v181, v181\n\t" \
  "v_pk_mul_f32 v[220:221], v[220:221], v[178:179]\n\t" \
  "v_pk_mul_f32 v[222:223], v[222:223], v[180:181]\n\t" \
  "s_waitcnt lgkmcnt(0)\n\t" \
  "v_pk_add_f32 v[182:183], v[182:183], v[190:191]\n\t" \
  "v_pk_add_f32 v[184:185], v[184:185], v[192:193]\n\t" \
  "v_pk_add_f32 v[186:187], v[186:187], v[194:195]\n\t" \
  "v_pk_add_f32 v[188:189], v[188:189], v[196:197]\n\t" \
  "buffer_load_dwordx4 v59, s[16:19], s0 offen offset:3072 sc1 lds\n\t" \
  "v_mov_b32_e32 v202, 0x3be38e39\n\t" \
  "v_pk_mul_f32 v[182:183], v[182:183], v[202:203] op_sel_hi:[1,0]\n\t" \
  "v_pk_mul_f32 v[184:185], v[184:185], v[202:203] op_sel_hi:[1,0]\n\t" \
  "v_pk_mul_f32 v[186:187], v[186:187], v[202:203] op_sel_hi:[1,0]\n\t" \
  "v_pk_mul_f32 v[188:189], v[188:189], v[202:203] op_sel_hi:[1,0]\n\t" \
  "v_pk_fma_f32 v[186:187], v[182:183], v[182:183], v[186:187] neg_lo:[1,0,0] neg_hi:[1,0,0]\n\t" \
  "v_pk_fma_f32 v[188:189], v[184:185], v[184:185], v[188:189] neg_lo:[1,0,0] neg_hi:[1,0,0]\n\t" \
  "v_max_f32_e32 v186, 0, v186\n\t" \
  "v_max_f32_e32 v187, 0, v187\n\t" \
  "v_max_f32_e32 v188, 0, v188\n\t" \
  "s_add_u32 m0, s58, 4096\n\t" \
  "s_nop 0\n\t" \
  "buffer_load_dwordx4 v57, s[16:19], s0 offen offset:0 sc1 lds\n\t" \
  "v_max_f32_e32 v189, 0, v189\n\t" \
  "v_add_f32_e32 v186, 0x3727c5ac, v186\n\t" \
  "v_add_f32_e32 v187, 0x3727c5ac, v187\n\t" \
  "v_add_f32_e32 v188, 0x3727c5ac, v188\n\t" \
  "v_add_f32_e32 v189, 0x3727c5ac, v189\n\t" \
  "v_rsq_f32_e32 v186, v186\n\t" \
  "v_rsq_f32_e32 v187, v187\n\t" \
  "v_rsq_f32_e32 v188, v188\n\t" \
  "buffer_load_dwordx4 v57, s[16:19], s0 offen offset:1024 sc1 lds\n\t" \
  "v_rsq_f32_e32 v189, v189\n\t" \
  "v_pk_mul_f32 v[182:183], v[182:183], v[186:187] neg_lo:[1,0] neg_hi:[1,0]\n\t" \
  "v_pk_mul_f32 v[184:185], v[184:185], v[188:189] neg_lo:[1,0] neg_hi:[1,0]\n\t" \
  "v_pk_fma_f32 v[154:155], v[154:155], v[186:187], v[182:183]\n\t" \
  "v_pk_fma_f32 v[156:157], v[156:157], v[188:189], v[184:185]\n\t" \
  "v_pk_fma_f32 v[158:159], v[158:159], v[186:187], v[182:183]\n\t" \
  "v_pk_fma_f32 v[160:161], v[160:161], v[188:189], v[184:185]\n\t" \
  "v_pk_fma_f32 v[162:163], v[162:163], v[186:187], v[182:183]\n\t" \
  "v_pk_fma_f32 v[164:165], v[164:165], v[188:189], v[184:185]\n\t" \
  "buffer_load_dwordx4 v57, s[16:19], s0 offen offset:2048 sc1 lds\n\t" \
  "v_pk_fma_f32 v[166:167], v[166:167], v[186:187], v[182:183]\n\t" \
  "v_pk_fma_f32 v[168:169], v[168:169], v[188:189], v[184:185]\n\t" \
  "v_pk_fma_f32 v[170:171], v[170:171], v[186:187], v[182:183]\n\t" \
  "v_pk_fma_f32 v[172:173], v[172:173], v[188:189], v[184:185]\n\t" \
  "v_cvt_pk_bf16_f32 v190, v154, v155\n\t" \
  "v_cvt_pk_bf16_f32 v191, v156, v157\n\t" \
  "v_cvt_pk_bf16_f32 v192, v158, v159\n\t" \
  "v_cvt_pk_bf16_f32 v193, v160, v161\n\t" \
  "v_cvt_pk_bf16_f32 v194, v162, v163\n\t" \
  "v_cvt_pk_bf16_f32 v195, v164, v165\n\t" \
  "buffer_load_dwordx4 v57, s[16:19], s0 offen offset:3072 sc1 lds\n\t" \
  "v_cvt_pk_bf16_f32 v196, v166, v167\n\t" \
  "v_cvt_pk_bf16_f32 v197, v168, v169\n\t" \
  "v_cvt_pk_bf16_f32 v198, v170, v171\n\t" \
  "v_cvt_pk_bf16_f32 v199, v172, v173\n\t" \
  "v_permlane16_swap_b32_e32 v190, v192\n\t" \
  "v_permlane16_swap_b32_e32 v191, v193\n\t" \
  "v_permlane16_swap_b32_e32 v194, v196\n\t" \
  "v_permlane16_swap_b32_e32 v195, v197\n\t" \
  "ds_write_b128 v250, v[190:193] offset:0\n\t" \
  "ds_write_b128 v250, v[194:197] offset:2048\n\t" \
  "s_add_u32 m0, s58, 8192\n\t" \
  "s_nop 0\n\t" \
  "buffer_load_dwordx4 v58, s[16:19], s0 offen offset:0 sc1 lds\n\t" \
  "ds_write_b64 v251, v[198:199]\n\t" \
  "v_pk_mul_f32 v[174:175], v[224:225], v[224:225]\n\t" \
  "v_pk_mul_f32 v[176:177], v[226:227], v[226:227]\n\t" \
  "v_pk_fma_f32 v[174:175], v[174:175], s[32:33], v[20:21] op_sel_hi:[1,1,0]\n\t" \
  "v_pk_fma_f32 v[176:177], v[176:177], s[32:33], v[20:21] op_sel_hi:[1,1,0]\n\t" \
  "v_pk_mul_f32 v[174:175], v[174:175], v[224:225]\n\t" \
  "v_pk_mul_f32 v[176:177], v[176:177], v[226:227]\n\t" \
  "v_exp_f32_e32 v174, v174\n\t" \
  "v_exp_f32_e32 v175, v175\n\t" \
  "v_exp_f32_e32 v176, v176\n\t" \
  "v_exp_f32_e32 v177, v177\n\t" \
  "v_pk_add_f32 v[174:175], v[174:175], s[34:35]\n\t" \
  "v_pk_add_f32 v[176:177], v[176:177], s[34:35]\n\t" \
  "v_rcp_f32_e32 v174, v174\n\t" \
  "v_rcp_f32_e32 v175, v175\n\t" \
  "v_rcp_f32_e32 v176, v176\n\t" \
  "v_rcp_f32_e32 v177, v177\n\t" \
  "v_pk_mul_f32 v[224:225], v[224:225], v[174:175]\n\t" \
  "v_pk_mul_f32 v[226:227], v[226:227], v[176:177]\n\t" \
  "v_cvt_pk_bf16_f32 v36, v208, v209\n\t" \
  "v_cvt_pk_bf16_f32 v37, v210, v211\n\t" \
  "v_cvt_pk_bf16_f32 v38, v212, v213\n\t" \
  "v_cvt_pk_bf16_f32 v39, v214, v215\n\t" \
  "v_cvt_pk_bf16_f32 v40, v216, v217\n\t" \
  "v_cvt_pk_bf16_f32 v41, v218, v219\n\t" \
  "v_cvt_pk_bf16_f32 v42, v220, v221\n\t" \
  "v_cvt_pk_bf16_f32 v43, v222, v223\n\t" \
  "v_cvt_pk_bf16_f32 v44, v224, v225\n\t" \
  "v_cvt_pk_bf16_f32 v45, v226, v227\n\t" \
  "s_waitcnt vmcnt(9)\n\t" \
  "s_waitcnt lgkmcnt(0)\n\t" \
  "s_barrier\n\t" \
  "ds_read_b128 a[94:97], v245 offset:0\n\t" \
  "ds_read_b128 a[112:115], v245 offset:4608\n\t" \
  "ds_read_b128 a[98:101], v245 offset:1024\n\t" \
  "ds_read_b128 a[116:119], v245 offset:5632\n\t" \
  "ds_read_b128 a[102:105], v245 offset:2048\n\t" \
  "ds_read_b128 a[120:123], v245 offset:6656\n\t" \
  "ds_read_b128 a[106:109], v245 offset:3072\n\t" \
  "ds_read_b128 a[124:127], v245 offset:7680\n\t" \
  "ds_read_b64 a[110:111], v246 offset:0\n\t" \
  "ds_read_b64 a[128:129], v246 offset:4608\n\t" \
  "ds_read_b128 a[130:133], v245 offset:9216\n\t" \
  "ds_read_b128 a[148:151], v245 offset:13824\n\t" \
  "s_waitcnt lgkmcnt(11)\n\t" \
  "v_mfma_f32_16x16x32_bf16 v[208:211], a[94:97], v[136:139], 0\n\t" \
  "ds_read_b128 a[134:137], v245 offset:10240\n\t" \
  "s_add_u32 m0, s52, 0\n\t" \
  "s_add_u32 s96, s0, 9216\n\t" \
  "buffer_load_dwordx4 v59, s[16:19], s96 offen offset:0 sc1 lds\n\t" \
  "s_waitcnt lgkmcnt(9)\n\t" \
  "v_mfma_f32_16x16x32_bf16 v[212:215], a[112:115], v[136:139], 0\n\t" \
  "ds_read_b128 a[152:155], v245 offset:14848\n\t" \
  "v_mfma_f32_16x16x32_bf16 v[208:211], a[98:101], v[140:143], v[208:211]\n\t" \
  "ds_read_b128 a[138:141], v245 offset:11264\n\t" \
  "v_mfma_f32_16x16x32_bf16 v[212:215], a[116:119], v[140:143], v[212:215]\n\t" \
  "ds_read_b128 a[156:159], v245 offset:15872\n\t" \
  "buffer_load_dwordx4 v59, s[16:19], s96 offen offset:1024 sc1 lds\n\t" \
  "s_waitcnt lgkmcnt(4)\n\t" \
  "v_mfma_f32_16x16x32_bf16 v[208:211], a[102:105], v[144:147], v[208:211]\n\t" \
  "ds_read_b128 a[142:145], v245 offset:12288\n\t" \
  "v_mfma_f32_16x16x32_bf16 v[212:215], a[120:123], v[144:147], v[212:215]\n\t" \
  "ds_read_b128 a[160:163], v245 offset:16896\n\t" \
  "v_mfma_f32_16x16x32_bf16 v[208:211], a[106:109], v[148:151], v[208:211]\n\t" \
  "ds_read_b64 a[146:147], v246 offset:9216\n\t" \
  "v_mfma_f32_16x16x32_bf16 v[212:215], a[124:127], v[148:151], v[212:215]\n\t" \
  "ds_read_b64 a[164:165], v246 offset:13824\n\t" \
  "buffer_load_dwordx4 v59, s[16:19], s96 offen offset:2048 sc1 lds\n\t" \
  "v_mfma_f32_16x16x16_bf16 v[208:211], a[110:111], v[152:153], v[208:211]\n\t" \
  "ds_read_b128 a[166:169], v245 offset:18432\n\t" \
  "v_mfma_f32_16x16x16_bf16 v[212:215], a[128:129], v[152:153], v[212:215]\n\t" \
  "ds_read_b128 a[184:187], v245 offset:23040\n\t" \
  "v_mfma_f32_16x16x32_bf16 v[216:219], a[130:133], v[136:139], 0\n\t" \
  "ds_read_b128 a[170:173], v245 offset:19456\n\t" \
  "v_mfma_f32_16x16x32_bf16 v[220:223], a[148:151], v[136:139], 0\n\t" \
  "ds_read_b128 a[188:191], v245 offset:24064\n\t" \
  "buffer_load_dwordx4 v59, s[16:19], s96 offen offset:3072 sc1 lds\n\t" \
  "s_waitcnt lgkmcnt(6)\n\t" \
  "v_mfma_f32_16x16x32_bf16 v[216:219], a[134:137], v[140:143], v[216:219]\n\t" \
  "ds_read_b128 a[174:177], v245 offset:20480\n\t" \
  "v_mfma_f32_16x16x32_bf16 v[220:223], a[152:155], v[140:143], v[220:223]\n\t" \
  "ds_read_b128 a[192:195], v245 offset:25088\n\t" \
  "v_mfma_f32_16x16x32_bf16 v[216:219], a[138:141], v[144:147], v[216:219]\n\t" \
  "ds_read_b128 a[178:181], v245 offset:21504\n\t" \
  "v_mfma_f32_16x16x32_bf16 v[220:223], a[156:159], v[144:147], v[220:223]\n\t" \
  "ds_read_b128 a[196:199], v245 offset:26112\n\t" \
  "s_add_u32 m0, s52, 4096\n\t" \
  "s_nop 0\n\t" \
  "buffer_load_dwordx4 v57, s[16:19], s96 offen offset:0 sc1 lds\n\t" \
  "v_mfma_f32_16x16x32_bf16 v[216:219], a[142:145], v[148:151], v[216:219]\n\t" \
  "ds_read_b64 a[182:183], v246 offset:18432\n\t" \
  "v_mfma_f32_16x16x32_bf16 v[220:223], a[160:163], v[148:151], v[220:223]\n\t" \
  "ds_read_b64 a[200:201], v246 offset:23040\n\t" \
  "s_waitcnt lgkmcnt(6)\n\t" \
  "v_mfma_f32_16x16x16_bf16 v[216:219], a[146:147], v[152:153], v[216:219]\n\t" \
  "ds_read_b128 a[202:205], v245 offset:27648\n\t" \
  "buffer_load_dwordx4 v57, s[16:19], s96 offen offset:1024 sc1 lds\n\t" \
  "v_mfma_f32_16x16x16_bf16 v[220:223], a[164:165], v[152:153], v[220:223]\n\t" \
  "ds_read_b128 a[220:223], v245 offset:32256\n\t" \
  "v_mfma_f32_16x16x32_bf16 v[224:227], a[166:169], v[136:139], 0\n\t" \
  "ds_read_b128 a[238:241], v245 offset:36864\n\t" \
  "v_mfma_f32_16x16x32_bf16 v[228:231], a[184:187], v[136:139], 0\n\t" \
  "ds_read_b128 a[206:209], v245 offset:28672\n\t" \
  "v_mfma_f32_16x16x32_bf16 v[224:227], a[170:173], v[140:143], v[224:227]\n\t" \
  "ds_read_b128 a[224:227], v245 offset:33280\n\t" \
  "buffer_load_dwordx4 v57, s[16:19], s96 offen offset:2048 sc1 lds\n\t" \
  "v_mfma_f32_16x16x32_bf16 v[228:231], a[188:191], v[140:143], v[228:231]\n\t" \
  "ds_read_b128 a[242:245], v245 offset:37888\n\t" \
  "s_waitcnt lgkmcnt(6)\n\t" \
  "v_mfma_f32_16x16x32_bf16 v[224:227], a[174:177], v[144:147], v[224:227]\n\t" \
  "ds_read_b128 a[210:213], v245 offset:29696\n\t" \
  "v_mfma_f32_16x16x32_bf16 v[228:231], a[192:195], v[144:147], v[228:231]\n\t" \
  "ds_read_b128 a[228:231], v245 offset:34304\n\t" \
  "v_mfma_f32_16x16x32_bf16 v[224:227], a[178:181], v[148:151], v[224:227]\n\t" \
  "ds_read_b128 a[246:249], v245 offset:38912\n\t" \
  "buffer_load_dwordx4 v57, s[16:19], s96 offen offset:3072 sc1 lds\n\t" \
  "v_mfma_f32_16x16x32_bf16 v[228:231], a[196:199], v[148:151], v[228:231]\n\t" \
  "ds_read_b128 a[214:217], v245 offset:30720\n\t" \
  "v_mfma_f32_16x16x16_bf16 v[224:227], a[182:183], v[152:153], v[224:227]\n\t" \
  "ds_read_b128 a[232:235], v245 offset:35328\n\t" \
  "v_mfma_f32_16x16x16_bf16 v[228:231], a[200:201], v[152:153], v[228:231]\n\t" \
  "ds_read_b128 a[250:253], v245 offset:39936\n\t" \
  "s_waitcnt lgkmcnt(6)\n\t" \
  "v_mfma_f32_16x16x32_bf16 v[232:235], a[202:205], v[136:139], 0\n\t" \
  "ds_read_b64 a[218:219], v246 offset:27648\n\t" \
  "s_add_u32 m0, s52, 8192\n\t" \
  "s_nop 0\n\t" \
  "buffer_load_dwordx4 v58, s[16:19], s96 offen offset:0 sc1 lds\n\t" \
  "v_mfma_f32_16x16x32_bf16 v[236:239], a[220:223], v[136:139], 0\n\t" \
  "ds_read_b64 a[236:237], v246 offset:32256\n\t" \
  "v_mfma_f32_16x16x32_bf16 v[240:243], a[238:241], v[136:139], 0\n\t" \
  "ds_read_b64 a[254:255], v246 offset:36864\n\t" \
  "v_mfma_f32_16x16x32_bf16 v[232:235], a[206:209], v[140:143], v[232:235]\n\t" \
  "v_mfma_f32_16x16x32_bf16 v[236:239], a[224:227], v[140:143], v[236:239]\n\t" \
  "ds_read_b128 v[154:157], v244 offset:0\n\t" \
  "v_mfma_f32_16x16x32_bf16 v[240:243], a[242:245], v[140:143], v[240:243]\n\t" \
  "ds_read_b128 v[158:161], v244 offset:1024\n\t" \
  "s_waitcnt lgkmcnt(5)\n\t" \
  "v_mfma_f32_16x16x32_bf16 v[232:235], a[210:213], v[144:147], v[232:235]\n\t" \
  "ds_read_b128 v[162:165], v244 offset:2048\n\t" \
  "v_mfma_f32_16x16x32_bf16 v[236:239], a[228:231], v[144:147], v[236:239]\n\t" \
  "v_mfma_f32_16x16x32_bf16 v[240:243], a[246:249], v[144:147], v[240:243]\n\t" \
  "ds_write_b128 v34, v[36:39] offset:5120\n\t" \
  "v_mfma_f32_16x16x32_bf16 v[232:235], a[214:217], v[148:151], v[232:235]\n\t" \
  "ds_write_b128 v34, v[40:43] offset:6144\n\t" \
  "v_mfma_f32_16x16x32_bf16 v[236:239], a[232:235], v[148:151], v[236:239]\n\t" \
  "v_mfma_f32_16x16x32_bf16 v[240:243], a[250:253], v[148:151], v[240:243]\n\t" \
  "s_waitcnt lgkmcnt(5)\n\t" \
  "v_mfma_f32_16x16x16_bf16 v[232:235], a[218:219], v[152:153], v[232:235]\n\t" \
  "v_mfma_f32_16x16x16_bf16 v[236:239], a[236:237], v[152:153], v[236:239]\n\t" \
  "v_mfma_f32_16x16x16_bf16 v[240:243], a[254:255], v[152:153], v[240:243]\n\t" \
  "v_pk_mul_f32 v[200:201], v[208:209], v[82:83]\n\t" \
  "v_pk_mul_f32 v[202:203], v[210:211], v[84:85]\n\t" \
  "v_pk_fma_f32 v[200:201], v[212:213], v[86:87], v[200:201]\n\t" \
  "v_pk_fma_f32 v[202:203], v[214:215], v[88:89], v[202:203]\n\t" \
  "v_pk_fma_f32 v[200:201], v[216:217], v[90:91], v[200:201]\n\t" \
  "v_pk_fma_f32 v[202:203], v[218:219], v[92:93], v[202:203]\n\t" \
  "v_pk_fma_f32 v[200:201], v[220:221], v[94:95], v[200:201]\n\t" \
  "v_pk_fma_f32 v[202:203], v[222:223], v[96:97], v[202:203]\n\t" \
  "v_pk_fma_f32 v[200:201], v[224:225], v[98:99], v[200:201]\n\t" \
  "v_pk_fma_f32 v[202:203], v[226:227], v[100:101], v[202:203]\n\t" \
  "v_pk_fma_f32 v[200:201], v[228:229], v[102:103], v[200:201]\n\t" \
  "v_pk_fma_f32 v[202:203], v[230:231], v[104:105], v[202:203]\n\t" \
  "v_pk_fma_f32 v[200:201], v[232:233], v[106:107], v[200:201]\n\t" \
  "v_pk_fma_f32 v[202:203], v[234:235], v[108:109], v[202:203]\n\t" \
  "v_pk_fma_f32 v[200:201], v[236:237], v[110:111], v[200:201]\n\t" \
  "v_pk_fma_f32 v[202:203], v[238:239], v[112:113], v[202:203]\n\t" \
  "v_pk_fma_f32 v[200:201], v[240:241], v[114:115], v[200:201]\n\t" \
  "v_pk_fma_f32 v[202:203], v[242:243], v[116:117], v[202:203]\n\t" \
  "v_pk_add_f32 v[200:201], v[200:201], v[202:203]\n\t" \
  "v_add_f32_e32 v204, v200, v201\n\t" \
  "v_mov_b32_e32 v205, v204\n\t" \
  "s_nop 1\n\t" \
  "v_permlane32_swap_b32_e32 v204, v205\n\t" \
  "v_add_f32_e32 v204, v204, v205\n\t" \
  "v_mov_b32_e32 v205, v204\n\t" \
  "s_nop 1\n\t" \
  "v_permlane16_swap_b32_e32 v204, v205\n\t" \
  "v_add_f32_e32 v204, v204, v205\n\t" \
  "v_mul_f32_e32 v204, 0xbadadcb8, v204\n\t" \
  "s_nop 0\n\t" \
  "buffer_load_dwordx2 v[60:61], v252, s[16:19], 0 offen sc1\n\t" \
  "v_pk_fma_f32 v[208:209], v[208:209], s[38:39], v[204:205] op_sel_hi:[1,1,0]\n\t" \
  "v_pk_fma_f32 v[210:211], v[210:211], s[38:39], v[204:205] op_sel_hi:[1,1,0]\n\t" \
  "v_cvt_pk_bf16_f32 v64, v208, v209\n\t" \
  "v_pk_fma_f32 v[212:213], v[212:213], s[38:39], v[204:205] op_sel_hi:[1,1,0]\n\t" \
  "v_cvt_pk_bf16_f32 v65, v210, v211\n\t" \
  "v_pk_fma_f32 v[214:215], v[214:215], s[38:39], v[204:205] op_sel_hi:[1,1,0]\n\t" \
  "v_cvt_pk_bf16_f32 v66, v212, v213\n\t" \
  "v_pk_fma_f32 v[216:217], v[216:217], s[38:39], v[204:205] op_sel_hi:[1,1,0]\n\t" \
  "v_cvt_pk_bf16_f32 v67, v214, v215\n\t" \
  "ds_write_b128 v34, v[64:67] offset:0\n\t" \
  "v_pk_fma_f32 v[218:219], v[218:219], s[38:39], v[204:205] op_sel_hi:[1,1,0]\n\t" \
  "v_cvt_pk_bf16_f32 v68, v216, v217\n\t" \
  "v_pk_fma_f32 v[220:221], v[220:221], s[38:39], v[204:205] op_sel_hi:[1,1,0]\n\t" \
  "v_cvt_pk_bf16_f32 v69, v218, v219\n\t" \
  "v_pk_fma_f32 v[222:223], v[222:223], s[38:39], v[204:205] op_sel_hi:[1,1,0]\n\t" \
  "v_cvt_pk_bf16_f32 v70, v220, v221\n\t" \
  "v_pk_fma_f32 v[224:225], v[224:225], s[38:39], v[204:205] op_sel_hi:[1,1,0]\n\t" \
  "v_cvt_pk_bf16_f32 v71, v222, v223\n\t" \
  "ds_write_b128 v34, v[68:71] offset:1024\n\t" \
  "v_pk_fma_f32 v[226:227], v[226:227], s[38:39], v[204:205] op_sel_hi:[1,1,0]\n\t" \
  "v_cvt_pk_bf16_f32 v72, v224, v225\n\t" \
  "v_pk_fma_f32 v[228:229], v[228:229], s[38:39], v[204:205] op_sel_hi:[1,1,0]\n\t" \
  "v_cvt_pk_bf16_f32 v73, v226, v227\n\t" \
  "v_pk_fma_f32 v[230:231], v[230:231], s[38:39], v[204:205] op_sel_hi:[1,1,0]\n\t" \
  "v_cvt_pk_bf16_f32 v74, v228, v229\n\t" \
  "v_pk_fma_f32 v[232:233], v[232:233], s[38:39], v[204:205] op_sel_hi:[1,1,0]\n\t" \
  "v_cvt_pk_bf16_f32 v75, v230, v231\n\t" \
  "ds_write_b128 v34, v[72:75] offset:2048\n\t" \
  "v_pk_fma_f32 v[234:235], v[234:235], s[38:39], v[204:205] op_sel_hi:[1,1,0]\n\t" \
  "v_cvt_pk_bf16_f32 v76, v232, v233\n\t" \
  "v_pk_fma_f32 v[236:237], v[236:237], s[38:39], v[204:205] op_sel_hi:[1,1,0]\n\t" \
  "v_cvt_pk_bf16_f32 v77, v234, v235\n\t" \
  "v_pk_fma_f32 v[238:239], v[238:239], s[38:39], v[204:205] op_sel_hi:[1,1,0]\n\t" \
  "v_cvt_pk_bf16_f32 v78, v236, v237\n\t" \
  "v_pk_fma_f32 v[240:241], v[240:241], s[38:39], v[204:205] op_sel_hi:[1,1,0]\n\t" \
  "v_cvt_pk_bf16_f32 v79, v238, v239\n\t" \
  "ds_write_b128 v34, v[76:79] offset:3072\n\t" \
  "v_pk_fma_f32 v[242:243], v[242:243], s[38:39], v[204:205] op_sel_hi:[1,1,0]\n\t" \
  "v_cvt_pk_bf16_f32 v80, v240, v241\n\t" \
  "v_cvt_pk_bf16_f32 v81, v242, v243\n\t" \
  "ds_write_b64 v34, v[80:81] offset:4096\n\t" \
  "s_waitcnt vmcnt(10)\n\t" \
  "ds_read_b128 a[72:75], v50 offset:0\n\t" \
  "ds_read_b128 a[88:91], v50 offset:1024\n\t" \
  "ds_read_b128 a[76:79], v50 offset:2048\n\t" \
  "ds_read_b128 a[92:95], v50 offset:3072\n\t" \
  "ds_read_b128 a[80:83], v50 offset:4096\n\t" \
  "ds_read_b128 a[96:99], v50 offset:5120\n\t" \
  "ds_read_b128 a[84:87], v50 offset:6144\n\t" \
  "ds_read_b128 a[100:103], v50 offset:7168\n\t" \
  "ds_read2st64_b64 a[104:107], v56 offset0:16 offset1:17\n\t" \
  "s_waitcnt lgkmcnt(10)\n\t" \
  "buffer_store_dwordx4 v[154:157], v33, s[12:15], s87 offen offset:0 sc1\n\t" \
  "buffer_store_dwordx4 v[158:161], v33, s[12:15], s87 offen offset:1024 sc1\n\t" \
  "buffer_store_dwordx4 v[162:165], v33, s[12:15], s87 offen offset:2048 sc1\n\t" \
  "s_waitcnt lgkmcnt(9)\n\t" \
  "s_barrier\n\t" \
  "ds_read_b128 a[0:3], v12 offset:0\n\t" \
  "ds_read_b128 a[4:7], v12 offset:1024\n\t" \
  "ds_read_b128 a[8:11], v12 offset:2048\n\t" \
  "ds_read_b128 a[12:15], v12 offset:3072\n\t" \
  "ds_read_b64 a[16:17], v12 offset:4096\n\t" \
  "s_waitcnt lgkmcnt(4)\n\t" \
  "v_mfma_f32_16x16x32_bf16 v[80:83], a[72:75], a[0:3], 0\n\t" \
  "ds_read_b128 a[18:21], v13 offset:0\n\t" \
  "ds_read_b128 a[22:25], v13 offset:1024\n\t" \
  "s_add_u32 s99, s0, 18432\n\t" \
  "v_mfma_f32_16x16x32_bf16 v[84:87], a[88:91], a[0:3], 0\n\t" \
  "ds_read_b128 a[26:29], v13 offset:2048\n\t" \
  "ds_read_b128 a[30:33], v13 offset:3072\n\t" \
  "s_add_u32 s96, s0, 27648\n\t" \
  "s_waitcnt lgkmcnt(7)\n\t" \
  "v_mfma_f32_16x16x32_bf16 v[80:83], a[76:79], a[4:7], v[80:83]\n\t" \
  "ds_read_b64 a[34:35], v13 offset:4096\n\t" \
  "ds_read_b128 a[36:39], v14 offset:0\n\t" \
  "s_mov_b32 s97, 2\n\t" \
  "v_mfma_f32_16x16x32_bf16 v[84:87], a[92:95], a[4:7], v[84:87]\n\t" \
  "ds_read_b128 a[40:43], v14 offset:1024\n\t" \
  "s_waitcnt lgkmcnt(7)\n\t" \
  "v_mfma_f32_16x16x32_bf16 v[80:83], a[80:83], a[8:11], v[80:83]\n\t" \
  "ds_read_b128 a[44:47], v14 offset:2048\n\t" \
  "ds_read_b128 a[48:51], v14 offset:3072\n\t" \
  "v_mfma_f32_16x16x32_bf16 v[84:87], a[96:99], a[8:11], v[84:87]\n\t" \
  "ds_read_b64 a[52:53], v14 offset:4096\n\t" \
  "ds_read_b128 a[54:57], v15 offset:0\n\t" \
  "v_mfma_f32_16x16x32_bf16 v[80:83], a[84:87], a[12:15], v[80:83]\n\t" \
  "ds_read_b128 a[58:61], v15 offset:1024\n\t" \
  "v_mfma_f32_16x16x32_bf16 v[84:87], a[100:103], a[12:15], v[84:87]\n\t" \
  "ds_read_b128 a[62:65], v15 offset:2048\n\t" \
  "ds_read_b128 a[66:69], v15 offset:3072\n\t" \
  "v_mfma_f32_16x16x16_bf16 v[80:83], a[104:105], a[16:17], v[80:83]\n\t" \
  "ds_read_b64 a[70:71], v15 offset:4096\n\t" \
  "ds_read_b128 v[4:7], v248 offset:5120\n\t" \
  "v_mfma_f32_16x16x16_bf16 v[84:87], a[106:107], a[16:17], v[84:87]\n\t" \
  "ds_read_b128 v[8:11], v248 offset:6144\n\t" \
  "s_waitcnt lgkmcnt(9)\n\t" \
  "v_mfma_f32_16x16x32_bf16 v[88:91], a[72:75], a[18:21], 0\n\t" \
  "ds_read_b64_tr_b16 a[108:109], v53 offset:0\n\t" \
  "ds_read_b64_tr_b16 a[110:111], v53 offset:1024\n\t" \
  "v_mfma_f32_16x16x32_bf16 v[92:95], a[88:91], a[18:21], 0\n\t" \
  "ds_read_b64_tr_b16 a[112:113], v53 offset:512\n\t" \
  "ds_read_b64_tr_b16 a[114:115], v53 offset:1536\n\t" \
  "s_waitcnt vmcnt(3)\n\t" \
  "v_cmp_ne_u64_e32 vcc, s[92:93], v[60:61]\n\t" \
  "v_mfma_f32_16x16x32_bf16 v[88:91], a[76:79], a[22:25], v[88:91]\n\t" \
  "ds_read_b64_tr_b16 a[116:117], v53 offset:2048\n\t" \
  "ds_read_b64_tr_b16 a[118:119], v53 offset:3072\n\t" \
  "s_cbranch_vccnz POLLE%=\n\t" \
  "BACKE%=:\n\t" \
  "v_mfma_f32_16x16x32_bf16 v[92:95], a[92:95], a[22:25], v[92:95]\n\t" \
  "s_waitcnt lgkmcnt(6)\n\t" \
  "s_barrier\n\t" \
  "s_add_u32 m0, s55, 0\n\t" \
  "v_exp_f32_e32 v80, v80\n\t" \
  "v_mfma_f32_16x16x32_bf16 v[88:91], a[80:83], a[26:29], v[88:91]\n\t" \
  "buffer_load_dwordx4 v59, s[16:19], s99 offen offset:0 sc1 lds\n\t" \
  "v_exp_f32_e32 v81, v81\n\t" \
  "v_mfma_f32_16x16x32_bf16 v[92:95], a[96:99], a[26:29], v[92:95]\n\t" \
  "ds_read_b64_tr_b16 a[120:121], v53 offset:2560\n\t" \
  "v_mfma_f32_16x16x32_bf16 v[88:91], a[84:87], a[30:33], v[88:91]\n\t" \
  "ds_read_b64_tr_b16 a[122:123], v53 offset:3584\n\t" \
  "ds_read_b64_tr_b16 a[124:125], v53 offset:4096\n\t" \
  "v_mfma_f32_16x16x32_bf16 v[92:95], a[100:103], a[30:33], v[92:95]\n\t" \
  "v_exp_f32_e32 v82, v82\n\t" \
  "v_mfma_f32_16x16x16_bf16 v[88:91], a[104:105], a[34:35], v[88:91]\n\t" \
  "buffer_load_dwordx4 v59, s[16:19], s99 offen offset:1024 sc1 lds\n\t" \
  "v_exp_f32_e32 v83, v83\n\t" \
  "v_mfma_f32_16x16x16_bf16 v[92:95], a[106:107], a[34:35], v[92:95]\n\t" \
  "v_cvt_pk_bf16_f32 v76, v80, v81\n\t" \
  "v_cvt_pk_bf16_f32 v77, v82, v83\n\t" \
  "v_mfma_f32_16x16x32_bf16 v[96:99], a[72:75], a[36:39], 0\n\t" \
  "ds_read_b64_tr_b16 a[126:127], v53 offset:5120\n\t" \
  "v_exp_f32_e32 v84, v84\n\t" \
  "v_mfma_f32_16x16x32_bf16 v[100:103], a[88:91], a[36:39], 0\n\t" \
  "v_exp_f32_e32 v85, v85\n\t" \
  "v_mfma_f32_16x16x32_bf16 v[96:99], a[76:79], a[40:43], v[96:99]\n\t" \
  "buffer_load_dwordx4 v59, s[16:19], s99 offen offset:2048 sc1 lds\n\t" \
  "v_exp_f32_e32 v86, v86\n\t" \
  "v_mfma_f32_16x16x32_bf16 v[100:103], a[92:95], a[40:43], v[100:103]\n\t" \
  "v_exp_f32_e32 v87, v87\n\t" \
  "v_mfma_f32_16x16x32_bf16 v[96:99], a[80:83], a[44:47], v[96:99]\n\t" \
  "ds_read_b64_tr_b16 a[128:129], v53 offset:4608\n\t" \
  "ds_read_b64_tr_b16 a[130:131], v53 offset:5632\n\t" \
  "v_mfma_f32_16x16x32_bf16 v[100:103], a[96:99], a[44:47], v[100:103]\n\t" \
  "v_cvt_pk_bf16_f32 v78, v84, v85\n\t" \
  "v_cvt_pk_bf16_f32 v79, v86, v87\n\t" \
  "v_mfma_f32_16x16x32_bf16 v[96:99], a[84:87], a[48:51], v[96:99]\n\t" \
  "buffer_load_dwordx4 v59, s[16:19], s99 offen offset:3072 sc1 lds\n\t" \
  "v_exp_f32_e32 v88, v88\n\t" \
  "v_mfma_f32_16x16x32_bf16 v[100:103], a[100:103], a[48:51], v[100:103]\n\t" \
  "v_exp_f32_e32 v89, v89\n\t" \
  "v_mfma_f32_16x16x16_bf16 v[96:99], a[104:105], a[52:53], v[96:99]\n\t" \
  "ds_read_b64_tr_b16 a[132:133], v53 offset:6144\n\t" \
  "v_exp_f32_e32 v90, v90\n\t" \
  "v_mfma_f32_16x16x16_bf16 v[100:103], a[106:107], a[52:53], v[100:103]\n\t" \
  "s_add_u32 m0, s55, 4096\n\t" \
  "v_exp_f32_e32 v91, v91\n\t" \
  "v_mfma_f32_16x16x32_bf16 v[104:107], a[72:75], a[54:57], 0\n\t" \
  "buffer_load_dwordx4 v57, s[16:19], s99 offen offset:0 sc1 lds\n\t" \
  "v_cvt_pk_bf16_f32 v72, v88, v89\n\t" \
  "v_cvt_pk_bf16_f32 v73, v90, v91\n\t" \
  "v_mfma_f32_16x16x32_bf16 v[108:111], a[88:91], a[54:57], 0\n\t" \
  "ds_read_b64_tr_b16 a[134:135], v53 offset:7168\n\t" \
  "v_exp_f32_e32 v92, v92\n\t" \
  "v_mfma_f32_16x16x32_bf16 v[104:107], a[76:79], a[58:61], v[104:107]\n\t" \
  "v_exp_f32_e32 v93, v93\n\t" \
  "v_mfma_f32_16x16x32_bf16 v[108:111], a[92:95], a[58:61], v[108:111]\n\t" \
  "ds_read_b64_tr_b16 a[136:137], v53 offset:6656\n\t" \
  "ds_read_b64_tr_b16 a[138:139], v53 offset:7680\n\t" \
  "v_mfma_f32_16x16x32_bf16 v[104:107], a[80:83], a[62:65], v[104:107]\n\t" \
  "buffer_load_dwordx4 v57, s[16:19], s99 offen offset:1024 sc1 lds\n\t" \
  "v_exp_f32_e32 v94, v94\n\t" \
  "v_mfma_f32_16x16x32_bf16 v[108:111], a[96:99], a[62:65], v[108:111]\n\t" \
  "ds_read_b64_tr_b16 a[140:141], v53 offset:8192\n\t" \
  "v_exp_f32_e32 v95, v95\n\t" \
  "v_mfma_f32_16x16x32_bf16 v[104:107], a[84:87], a[66:69], v[104:107]\n\t" \
  "ds_read_b64_tr_b16 a[142:143], v53 offset:8704\n\t" \
  "v_cvt_pk_bf16_f32 v74, v92, v93\n\t" \
  "v_cvt_pk_bf16_f32 v75, v94, v95\n\t" \
  "v_mfma_f32_16x16x32_bf16 v[108:111], a[100:103], a[66:69], v[108:111]\n\t" \
  "v_exp_f32_e32 v96, v96\n\t" \
  "v_mfma_f32_16x16x16_bf16 v[104:107], a[104:105], a[70:71], v[104:107]\n\t" \
  "buffer_load_dwordx4 v57, s[16:19], s99 offen offset:2048 sc1 lds\n\t" \
  "v_exp_f32_e32 v97, v97\n\t" \
  "v_mfma_f32_16x16x16_bf16 v[108:111], a[106:107], a[70:71], v[108:111]\n\t" \
  "v_exp_f32_e32 v98, v98\n\t" \
  "s_waitcnt lgkmcnt(2)\n\t" \
  "v_mfma_f32_16x16x32_bf16 v[112:115], a[108:111], v[76:79], 0\n\t" \
  "v_exp_f32_e32 v99, v99\n\t" \
  "v_mfma_f32_16x16x32_bf16 v[116:119], a[112:115], v[76:79], 0\n\t" \
  "buffer_load_dwordx4 v57, s[16:19], s99 offen offset:3072 sc1 lds\n\t" \
  "v_mfma_f32_16x16x32_bf16 v[120:123], a[116:119], v[76:79], 0\n\t" \
  "v_cvt_pk_bf16_f32 v68, v96, v97\n\t" \
  "v_cvt_pk_bf16_f32 v69, v98, v99\n\t" \
  "v_mfma_f32_16x16x32_bf16 v[124:127], a[120:123], v[76:79], 0\n\t" \
  "s_add_u32 m0, s55, 8192\n\t" \
  "v_exp_f32_e32 v100, v100\n\t" \
  "v_mfma_f32_16x16x32_bf16 v[128:131], a[124:127], v[76:79], 0\n\t" \
  "buffer_load_dwordx4 v58, s[16:19], s99 offen offset:0 sc1 lds\n\t" \
  "v_exp_f32_e32 v101, v101\n\t" \
  "v_mfma_f32_16x16x32_bf16 v[132:135], a[128:131], v[76:79], 0\n\t" \
  "v_exp_f32_e32 v102, v102\n\t" \
  "v_mfma_f32_16x16x32_bf16 v[136:139], a[132:135], v[76:79], 0\n\t" \
  "v_exp_f32_e32 v103, v103\n\t" \
  "v_mfma_f32_16x16x32_bf16 v[140:143], a[136:139], v[76:79], 0\n\t" \
  "s_waitcnt lgkmcnt(0)\n\t" \
  "s_add_u32 m0, s58, 0\n\t" \
  "v_cvt_pk_bf16_f32 v70, v100, v101\n\t" \
  "v_cvt_pk_bf16_f32 v71, v102, v103\n\t" \
  "v_mfma_f32_16x16x32_bf16 v[144:147], a[140:143], v[76:79], 0\n\t" \
  "buffer_load_dwordx4 v59, s[16:19], s96 offen offset:0 sc1 lds\n\t" \
  "v_exp_f32_e32 v104, v104\n\t" \
  "v_mfma_f32_16x16x32_bf16 v[148:151], a[108:111], v[72:75], 0\n\t" \
  "v_mfma_f32_16x16x32_bf16 v[152:155], a[112:115], v[72:75], 0\n\t" \
  "v_mfma_f32_16x16x32_bf16 v[156:159], a[116:119], v[72:75], 0\n\t" \
  "v_exp_f32_e32 v105, v105\n\t" \
  "v_mfma_f32_16x16x32_bf16 v[160:163], a[120:123], v[72:75], 0\n\t" \
  "buffer_load_dwordx4 v59, s[16:19], s96 offen offset:1024 sc1 lds\n\t" \
  "v_exp_f32_e32 v106, v106\n\t" \
  "v_mfma_f32_16x16x32_bf16 v[164:167], a[124:127], v[72:75], 0\n\t" \
  "v_exp_f32_e32 v107, v107\n\t" \
  "v_mfma_f32_16x16x32_bf16 v[168:171], a[128:131], v[72:75], 0\n\t" \
  "v_cvt_pk_bf16_f32 v64, v104, v105\n\t" \
  "v_cvt_pk_bf16_f32 v65, v106, v107\n\t" \
  "v_mfma_f32_16x16x32_bf16 v[172:175], a[132:135], v[72:75], 0\n\t" \
  "v_exp_f32_e32 v108, v108\n\t" \
  "v_mfma_f32_16x16x32_bf16 v[176:179], a[136:139], v[72:75], 0\n\t" \
  "buffer_load_dwordx4 v59, s[16:19], s96 offen offset:2048 sc1 lds\n\t" \
  "v_exp_f32_e32 v109, v109\n\t" \
  "v_mfma_f32_16x16x32_bf16 v[180:183], a[140:143], v[72:75], 0\n\t" \
  "v_exp_f32_e32 v110, v110\n\t" \
  "v_mfma_f32_16x16x32_bf16 v[184:187], a[108:111], v[68:71], 0\n\t" \
  "v_mfma_f32_16x16x32_bf16 v[188:191], a[112:115], v[68:71], 0\n\t" \
  "v_exp_f32_e32 v111, v111\n\t" \
  "v_mfma_f32_16x16x32_bf16 v[192:195], a[116:119], v[68:71], 0\n\t" \
  "buffer_load_dwordx4 v59, s[16:19], s96 offen offset:3072 sc1 lds\n\t" \
  "v_cvt_pk_bf16_f32 v66, v108, v109\n\t" \
  "v_cvt_pk_bf16_f32 v67, v110, v111\n\t" \
  "v_mfma_f32_16x16x32_bf16 v[196:199], a[120:123], v[68:71], 0\n\t" \
  "ds_read_b128 a[72:75], v48 offset:0\n\t" \
  "v_mfma_f32_16x16x32_bf16 v[200:203], a[124:127], v[68:71], 0\n\t" \
  "ds_read_b128 a[88:91], v48 offset:1024\n\t" \
  "v_mfma_f32_16x16x32_bf16 v[204:207], a[128:131], v[68:71], 0\n\t" \
  "s_add_u32 m0, s58, 4096\n\t" \
  "ds_read_b128 a[76:79], v48 offset:2048\n\t" \
  "v_mfma_f32_16x16x32_bf16 v[208:211], a[132:135], v[68:71], 0\n\t" \
  "buffer_load_dwordx4 v57, s[16:19], s96 offen offset:0 sc1 lds\n\t" \
  "ds_read_b128 a[92:95], v48 offset:3072\n\t" \
  "v_mfma_f32_16x16x32_bf16 v[212:215], a[136:139], v[68:71], 0\n\t" \
  "ds_read_b128 a[80:83], v48 offset:4096\n\t" \
  "v_mfma_f32_16x16x32_bf16 v[216:219], a[140:143], v[68:71], 0\n\t" \
  "v_mfma_f32_16x16x32_bf16 v[220:223], a[108:111], v[64:67], 0\n\t" \
  "ds_read_b128 a[96:99], v48 offset:5120\n\t" \
  "v_mfma_f32_16x16x32_bf16 v[224:227], a[112:115], v[64:67], 0\n\t" \
  "buffer_load_dwordx4 v57, s[16:19], s96 offen offset:1024 sc1 lds\n\t" \
  "ds_read_b64_tr_b16 a[108:109], v51 offset:0\n\t" \
  "ds_read_b128 a[84:87], v48 offset:6144\n\t" \
  "v_mfma_f32_16x16x32_bf16 v[228:231], a[116:119], v[64:67], 0\n\t" \
  "ds_read_b64_tr_b16 a[110:111], v51 offset:1024\n\t" \
  "ds_read_b128 a[100:103], v48 offset:7168\n\t" \
  "v_mfma_f32_16x16x32_bf16 v[232:235], a[120:123], v[64:67], 0\n\t" \
  "ds_read_b64_tr_b16 a[112:113], v51 offset:512\n\t" \
  "ds_read2st64_b64 a[104:107], v54 offset0:16 offset1:17\n\t" \
  "v_mfma_f32_16x16x32_bf16 v[236:239], a[124:127], v[64:67], 0\n\t" \
  "buffer_load_dwordx4 v57, s[16:19], s96 offen offset:2048 sc1 lds\n\t" \
  "ds_read_b64_tr_b16 a[114:115], v51 offset:1536\n\t" \
  "v_mfma_f32_16x16x32_bf16 v[240:243], a[128:131], v[64:67], 0\n\t" \
  "ds_read_b64_tr_b16 a[116:117], v51 offset:2048\n\t" \
  "v_mfma_f32_16x16x32_bf16 v[244:247], a[132:135], v[64:67], 0\n\t" \
  "ds_read_b64_tr_b16 a[118:119], v51 offset:3072\n\t" \
  "v_mfma_f32_16x16x32_bf16 v[248:251], a[136:139], v[64:67], 0\n\t" \
  "buffer_load_dwordx4 v57, s[16:19], s96 offen offset:3072 sc1 lds\n\t" \
  "v_mfma_f32_16x16x32_bf16 v[252:255], a[140:143], v[64:67], 0\n\t" \
  "s_waitcnt lgkmcnt(6)\n\t" \
  ".p2align 6\n\t" \
  "LOOP%=:\n\t" \
  "v_mfma_f32_16x16x32_bf16 v[80:83], a[72:75], a[0:3], 0\n\t" \
  "ds_read_b64_tr_b16 a[120:121], v51 offset:2560\n\t" \
  "v_mfma_f32_16x16x32_bf16 v[84:87], a[88:91], a[0:3], 0\n\t" \
  "ds_read_b64_tr_b16 a[122:123], v51 offset:3584\n\t" \
  "v_mfma_f32_16x16x32_bf16 v[80:83], a[76:79], a[4:7], v[80:83]\n\t" \
  "ds_read_b64_tr_b16 a[124:125], v51 offset:4096\n\t" \
  "v_mfma_f32_16x16x32_bf16 v[84:87], a[92:95], a[4:7], v[84:87]\n\t" \
  "s_waitcnt lgkmcnt(6)\n\t" \
  "ds_read_b64_tr_b16 a[126:127], v51 offset:5120\n\t" \
  "v_mfma_f32_16x16x32_bf16 v[80:83], a[80:83], a[8:11], v[80:83]\n\t" \
  "ds_read_b64_tr_b16 a[128:129], v51 offset:4608\n\t" \
  "v_mfma_f32_16x16x32_bf16 v[84:87], a[96:99], a[8:11], v[84:87]\n\t" \
  "ds_read_b64_tr_b16 a[130:131], v51 offset:5632\n\t" \
  "v_mfma_f32_16x16x32_bf16 v[80:83], a[84:87], a[12:15], v[80:83]\n\t" \
  "s_mov_b32 m0, s60\n\t" \
  "ds_read_b64_tr_b16 a[132:133], v51 offset:6144\n\t" \
  "v_mfma_f32_16x16x32_bf16 v[84:87], a[100:103], a[12:15], v[84:87]\n\t" \
  "buffer_load_dwordx4 v58, s[16:19], s96 offen offset:0 sc1 lds\n\t" \
  "v_mfma_f32_16x16x16_bf16 v[80:83], a[104:105], a[16:17], v[80:83]\n\t" \
  "s_add_u32 s96, s96, 9216\n\t" \
  "ds_read_b64_tr_b16 a[134:135], v51 offset:7168\n\t" \
  "v_mfma_f32_16x16x16_bf16 v[84:87], a[106:107], a[16:17], v[84:87]\n\t" \
  "s_cmp_eq_u32 s96, s73\n\t" \
  "ds_read_b64_tr_b16 a[136:137], v51 offset:6656\n\t" \
  "v_mfma_f32_16x16x32_bf16 v[88:91], a[72:75], a[18:21], 0\n\t" \
  "s_cselect_b32 s96, s72, s96\n\t" \
  "ds_read_b64_tr_b16 a[138:139], v51 offset:7680\n\t" \
  "v_mfma_f32_16x16x32_bf16 v[92:95], a[88:91], a[18:21], 0\n\t" \
  "ds_read_b64_tr_b16 a[140:141], v51 offset:8192\n\t" \
  "v_mfma_f32_16x16x32_bf16 v[88:91], a[76:79], a[22:25], v[88:91]\n\t" \
  "ds_read_b64_tr_b16 a[142:143], v51 offset:8704\n\t" \
  "v_mfma_f32_16x16x32_bf16 v[92:95], a[92:95], a[22:25], v[92:95]\n\t" \
  "v_exp_f32_e32 v80, v80\n\t" \
  "v_mfma_f32_16x16x32_bf16 v[88:91], a[80:83], a[26:29], v[88:91]\n\t" \
  "v_exp_f32_e32 v81, v81\n\t" \
  "v_mfma_f32_16x16x32_bf16 v[92:95], a[96:99], a[26:29], v[92:95]\n\t" \
  "s_waitcnt lgkmcnt(0)\n\t" \
  "s_mov_b32 m0, s52\n\t" \
  "v_mfma_f32_16x16x32_bf16 v[88:91], a[84:87], a[30:33], v[88:91]\n\t" \
  "buffer_load_dwordx4 v59, s[16:19], s96 offen offset:0 sc1 lds\n\t" \
  "v_mfma_f32_16x16x32_bf16 v[92:95], a[100:103], a[30:33], v[92:95]\n\t" \
  "v_exp_f32_e32 v82, v82\n\t" \
  "v_mfma_f32_16x16x16_bf16 v[88:91], a[104:105], a[34:35], v[88:91]\n\t" \
  "v_exp_f32_e32 v83, v83\n\t" \
  "v_mfma_f32_16x16x16_bf16 v[92:95], a[106:107], a[34:35], v[92:95]\n\t" \
  "v_cvt_pk_bf16_f32 v76, v80, v81\n\t" \
  "v_cvt_pk_bf16_f32 v77, v82, v83\n\t" \
  "v_mfma_f32_16x16x32_bf16 v[96:99], a[72:75], a[36:39], 0\n\t" \
  "v_exp_f32_e32 v84, v84\n\t" \
  "v_mfma_f32_16x16x32_bf16 v[100:103], a[88:91], a[36:39], 0\n\t" \
  "v_exp_f32_e32 v85, v85\n\t" \
  "v_mfma_f32_16x16x32_bf16 v[96:99], a[76:79], a[40:43], v[96:99]\n\t" \
  "v_exp_f32_e32 v86, v86\n\t" \
  "v_mfma_f32_16x16x32_bf16 v[100:103], a[92:95], a[40:43], v[100:103]\n\t" \
  "v_exp_f32_e32 v87, v87\n\t" \
  "v_mfma_f32_16x16x32_bf16 v[96:99], a[80:83], a[44:47], v[96:99]\n\t" \
  "buffer_load_dwordx4 v59, s[16:19], s96 offen offset:1024 sc1 lds\n\t" \
  "v_mfma_f32_16x16x32_bf16 v[100:103], a[96:99], a[44:47], v[100:103]\n\t" \
  "v_cvt_pk_bf16_f32 v78, v84, v85\n\t" \
  "v_cvt_pk_bf16_f32 v79, v86, v87\n\t" \
  "v_mfma_f32_16x16x32_bf16 v[96:99], a[84:87], a[48:51], v[96:99]\n\t" \
  "v_exp_f32_e32 v88, v88\n\t" \
  "v_mfma_f32_16x16x32_bf16 v[100:103], a[100:103], a[48:51], v[100:103]\n\t" \
  "v_exp_f32_e32 v89, v89\n\t" \
  "v_mfma_f32_16x16x16_bf16 v[96:99], a[104:105], a[52:53], v[96:99]\n\t" \
  "v_exp_f32_e32 v90, v90\n\t" \
  "v_mfma_f32_16x16x16_bf16 v[100:103], a[106:107], a[52:53], v[100:103]\n\t" \
  "v_exp_f32_e32 v91, v91\n\t" \
  "v_mfma_f32_16x16x32_bf16 v[104:107], a[72:75], a[54:57], 0\n\t" \
  "v_cvt_pk_bf16_f32 v72, v88, v89\n\t" \
  "v_cvt_pk_bf16_f32 v73, v90, v91\n\t" \
  "v_mfma_f32_16x16x32_bf16 v[108:111], a[88:91], a[54:57], 0\n\t" \
  "v_exp_f32_e32 v92, v92\n\t" \
  "v_mfma_f32_16x16x32_bf16 v[104:107], a[76:79], a[58:61], v[104:107]\n\t" \
  "v_exp_f32_e32 v93, v93\n\t" \
  "v_mfma_f32_16x16x32_bf16 v[108:111], a[92:95], a[58:61], v[108:111]\n\t" \
  "buffer_load_dwordx4 v59, s[16:19], s96 offen offset:2048 sc1 lds\n\t" \
  "v_mfma_f32_16x16x32_bf16 v[104:107], a[80:83], a[62:65], v[104:107]\n\t" \
  "v_exp_f32_e32 v94, v94\n\t" \
  "v_mfma_f32_16x16x32_bf16 v[108:111], a[96:99], a[62:65], v[108:111]\n\t" \
  "v_exp_f32_e32 v95, v95\n\t" \
  "v_mfma_f32_16x16x32_bf16 v[104:107], a[84:87], a[66:69], v[104:107]\n\t" \
  "v_cvt_pk_bf16_f32 v74, v92, v93\n\t" \
  "v_cvt_pk_bf16_f32 v75, v94, v95\n\t" \
  "v_mfma_f32_16x16x32_bf16 v[108:111], a[100:103], a[66:69], v[108:111]\n\t" \
  "v_exp_f32_e32 v96, v96\n\t" \
  "v_mfma_f32_16x16x16_bf16 v[104:107], a[104:105], a[70:71], v[104:107]\n\t" \
  "v_exp_f32_e32 v97, v97\n\t" \
  "v_mfma_f32_16x16x16_bf16 v[108:111], a[106:107], a[70:71], v[108:111]\n\t" \
  "v_exp_f32_e32 v98, v98\n\t" \
  "v_mfma_f32_16x16x32_bf16 v[112:115], a[108:111], v[76:79], v[112:115]\n\t" \
  "v_exp_f32_e32 v99, v99\n\t" \
  "v_mfma_f32_16x16x32_bf16 v[116:119], a[112:115], v[76:79], v[116:119]\n\t" \
  "buffer_load_dwordx4 v59, s[16:19], s96 offen offset:3072 sc1 lds\n\t" \
  "v_mfma_f32_16x16x32_bf16 v[120:123], a[116:119], v[76:79], v[120:123]\n\t" \
  "v_cvt_pk_bf16_f32 v68, v96, v97\n\t" \
  "v_cvt_pk_bf16_f32 v69, v98, v99\n\t" \
  "v_mfma_f32_16x16x32_bf16 v[124:127], a[120:123], v[76:79], v[124:127]\n\t" \
  "v_exp_f32_e32 v100, v100\n\t" \
  "v_mfma_f32_16x16x32_bf16 v[128:131], a[124:127], v[76:79], v[128:131]\n\t" \
  "v_exp_f32_e32 v101, v101\n\t" \
  "v_mfma_f32_16x16x32_bf16 v[132:135], a[128:131], v[76:79], v[132:135]\n\t" \
  "v_exp_f32_e32 v102, v102\n\t" \
  "v_mfma_f32_16x16x32_bf16 v[136:139], a[132:135], v[76:79], v[136:139]\n\t" \
  "v_exp_f32_e32 v103, v103\n\t" \
  "v_mfma_f32_16x16x32_bf16 v[140:143], a[136:139], v[76:79], v[140:143]\n\t" \
  "v_cvt_pk_bf16_f32 v70, v100, v101\n\t" \
  "v_cvt_pk_bf16_f32 v71, v102, v103\n\t" \
  "v_mfma_f32_16x16x32_bf16 v[144:147], a[140:143], v[76:79], v[144:147]\n\t" \
  "v_exp_f32_e32 v104, v104\n\t" \
  "v_mfma_f32_16x16x32_bf16 v[148:151], a[108:111], v[72:75], v[148:151]\n\t" \
  "s_mov_b32 m0, s53\n\t" \
  "s_waitcnt vmcnt(14)\n\t" \
  "v_mfma_f32_16x16x32_bf16 v[152:155], a[112:115], v[72:75], v[152:155]\n\t" \
  "buffer_load_dwordx4 v57, s[16:19], s96 offen offset:0 sc1 lds\n\t" \
  "v_mfma_f32_16x16x32_bf16 v[156:159], a[116:119], v[72:75], v[156:159]\n\t" \
  "v_exp_f32_e32 v105, v105\n\t" \
  "v_mfma_f32_16x16x32_bf16 v[160:163], a[120:123], v[72:75], v[160:163]\n\t" \
  "v_exp_f32_e32 v106, v106\n\t" \
  "v_mfma_f32_16x16x32_bf16 v[164:167], a[124:127], v[72:75], v[164:167]\n\t" \
  "v_exp_f32_e32 v107, v107\n\t" \
  "v_mfma_f32_16x16x32_bf16 v[168:171], a[128:131], v[72:75], v[168:171]\n\t" \
  "v_cvt_pk_bf16_f32 v64, v104, v105\n\t" \
  "v_cvt_pk_bf16_f32 v65, v106, v107\n\t" \
  "v_mfma_f32_16x16x32_bf16 v[172:175], a[132:135], v[72:75], v[172:175]\n\t" \
  "v_exp_f32_e32 v108, v108\n\t" \
  "v_mfma_f32_16x16x32_bf16 v[176:179], a[136:139], v[72:75], v[176:179]\n\t" \
  "v_exp_f32_e32 v109, v109\n\t" \
  "v_mfma_f32_16x16x32_bf16 v[180:183], a[140:143], v[72:75], v[180:183]\n\t" \
  "v_exp_f32_e32 v110, v110\n\t" \
  "v_mfma_f32_16x16x32_bf16 v[184:187], a[108:111], v[68:71], v[184:187]\n\t" \
  "buffer_load_dwordx4 v57, s[16:19], s96 offen offset:1024 sc1 lds\n\t" \
  "v_mfma_f32_16x16x32_bf16 v[188:191], a[112:115], v[68:71], v[188:191]\n\t" \
  "v_exp_f32_e32 v111, v111\n\t" \
  "v_mfma_f32_16x16x32_bf16 v[192:195], a[116:119], v[68:71], v[192:195]\n\t" \
  "v_cvt_pk_bf16_f32 v66, v108, v109\n\t" \
  "v_cvt_pk_bf16_f32 v67, v110, v111\n\t" \
  "v_mfma_f32_16x16x32_bf16 v[196:199], a[120:123], v[68:71], v[196:199]\n\t" \
  "ds_read_b128 a[72:75], v49 offset:0\n\t" \
  "v_mfma_f32_16x16x32_bf16 v[200:203], a[124:127], v[68:71], v[200:203]\n\t" \
  "ds_read_b128 a[88:91], v49 offset:1024\n\t" \
  "v_mfma_f32_16x16x32_bf16 v[204:207], a[128:131], v[68:71], v[204:207]\n\t" \
  "ds_read_b128 a[76:79], v49 offset:2048\n\t" \
  "v_mfma_f32_16x16x32_bf16 v[208:211], a[132:135], v[68:71], v[208:211]\n\t" \
  "ds_read_b128 a[92:95], v49 offset:3072\n\t" \
  "v_mfma_f32_16x16x32_bf16 v[212:215], a[136:139], v[68:71], v[212:215]\n\t" \
  "ds_read_b128 a[80:83], v49 offset:4096\n\t" \
  "v_mfma_f32_16x16x32_bf16 v[216:219], a[140:143], v[68:71], v[216:219]\n\t" \
  "buffer_load_dwordx4 v57, s[16:19], s96 offen offset:2048 sc1 lds\n\t" \
  "v_mfma_f32_16x16x32_bf16 v[220:223], a[108:111], v[64:67], v[220:223]\n\t" \
  "ds_read_b128 a[96:99], v49 offset:5120\n\t" \
  "v_mfma_f32_16x16x32_bf16 v[224:227], a[112:115], v[64:67], v[224:227]\n\t" \
  "ds_read_b64_tr_b16 a[108:109], v52 offset:0\n\t" \
  "ds_read_b128 a[84:87], v49 offset:6144\n\t" \
  "v_mfma_f32_16x16x32_bf16 v[228:231], a[116:119], v[64:67], v[228:231]\n\t" \
  "ds_read_b64_tr_b16 a[110:111], v52 offset:1024\n\t" \
  "ds_read_b128 a[100:103], v49 offset:7168\n\t" \
  "v_mfma_f32_16x16x32_bf16 v[232:235], a[120:123], v[64:67], v[232:235]\n\t" \
  "ds_read_b64_tr_b16 a[112:113], v52 offset:512\n\t" \
  "ds_read2st64_b64 a[104:107], v55 offset0:16 offset1:17\n\t" \
  "v_mfma_f32_16x16x32_bf16 v[236:239], a[124:127], v[64:67], v[236:239]\n\t" \
  "ds_read_b64_tr_b16 a[114:115], v52 offset:1536\n\t" \
  "v_mfma_f32_16x16x32_bf16 v[240:243], a[128:131], v[64:67], v[240:243]\n\t" \
  "ds_read_b64_tr_b16 a[116:117], v52 offset:2048\n\t" \
  "v_mfma_f32_16x16x32_bf16 v[244:247], a[132:135], v[64:67], v[244:247]\n\t" \
  "ds_read_b64_tr_b16 a[118:119], v52 offset:3072\n\t" \
  "v_mfma_f32_16x16x32_bf16 v[248:251], a[136:139], v[64:67], v[248:251]\n\t" \
  "buffer_load_dwordx4 v57, s[16:19], s96 offen offset:3072 sc1 lds\n\t" \
  "v_mfma_f32_16x16x32_bf16 v[252:255], a[140:143], v[64:67], v[252:255]\n\t" \
  "s_waitcnt lgkmcnt(11)\n\t" \
  "v_mfma_f32_16x16x32_bf16 v[80:83], a[72:75], a[0:3], 0\n\t" \
  "ds_read_b64_tr_b16 a[120:121], v52 offset:2560\n\t" \
  "v_mfma_f32_16x16x32_bf16 v[84:87], a[88:91], a[0:3], 0\n\t" \
  "ds_read_b64_tr_b16 a[122:123], v52 offset:3584\n\t" \
  "v_mfma_f32_16x16x32_bf16 v[80:83], a[76:79], a[4:7], v[80:83]\n\t" \
  "ds_read_b64_tr_b16 a[124:125], v52 offset:4096\n\t" \
  "v_mfma_f32_16x16x32_bf16 v[84:87], a[92:95], a[4:7], v[84:87]\n\t" \
  "s_waitcnt lgkmcnt(6)\n\t" \
  "ds_read_b64_tr_b16 a[126:127], v52 offset:5120\n\t" \
  "v_mfma_f32_16x16x32_bf16 v[80:83], a[80:83], a[8:11], v[80:83]\n\t" \
  "ds_read_b64_tr_b16 a[128:129], v52 offset:4608\n\t" \
  "v_mfma_f32_16x16x32_bf16 v[84:87], a[96:99], a[8:11], v[84:87]\n\t" \
  "ds_read_b64_tr_b16 a[130:131], v52 offset:5632\n\t" \
  "v_mfma_f32_16x16x32_bf16 v[80:83], a[84:87], a[12:15], v[80:83]\n\t" \
  "s_mov_b32 m0, s54\n\t" \
  "ds_read_b64_tr_b16 a[132:133], v52 offset:6144\n\t" \
  "v_mfma_f32_16x16x32_bf16 v[84:87], a[100:103], a[12:15], v[84:87]\n\t" \
  "buffer_load_dwordx4 v58, s[16:19], s96 offen offset:0 sc1 lds\n\t" \
  "v_mfma_f32_16x16x16_bf16 v[80:83], a[104:105], a[16:17], v[80:83]\n\t" \
  "s_add_u32 s96, s96, 9216\n\t" \
  "ds_read_b64_tr_b16 a[134:135], v52 offset:7168\n\t" \
  "v_mfma_f32_16x16x16_bf16 v[84:87], a[106:107], a[16:17], v[84:87]\n\t" \
  "s_cmp_eq_u32 s96, s73\n\t" \
  "ds_read_b64_tr_b16 a[136:137], v52 offset:6656\n\t" \
  "v_mfma_f32_16x16x32_bf16 v[88:91], a[72:75], a[18:21], 0\n\t" \
  "s_cselect_b32 s96, s72, s96\n\t" \
  "ds_read_b64_tr_b16 a[138:139], v52 offset:7680\n\t" \
  "v_mfma_f32_16x16x32_bf16 v[92:95], a[88:91], a[18:21], 0\n\t" \
  "ds_read_b64_tr_b16 a[140:141], v52 offset:8192\n\t" \
  "v_mfma_f32_16x16x32_bf16 v[88:91], a[76:79], a[22:25], v[88:91]\n\t" \
  "ds_read_b64_tr_b16 a[142:143], v52 offset:8704\n\t" \
  "v_mfma_f32_16x16x32_bf16 v[92:95], a[92:95], a[22:25], v[92:95]\n\t" \
  "v_exp_f32_e32 v80, v80\n\t" \
  "v_mfma_f32_16x16x32_bf16 v[88:91], a[80:83], a[26:29], v[88:91]\n\t" \
  "v_exp_f32_e32 v81, v81\n\t" \
  "v_mfma_f32_16x16x32_bf16 v[92:95], a[96:99], a[26:29], v[92:95]\n\t" \
  "s_waitcnt lgkmcnt(0)\n\t" \
  "s_mov_b32 m0, s55\n\t" \
  "v_mfma_f32_16x16x32_bf16 v[88:91], a[84:87], a[30:33], v[88:91]\n\t" \
  "buffer_load_dwordx4 v59, s[16:19], s96 offen offset:0 sc1 lds\n\t" \
  "v_mfma_f32_16x16x32_bf16 v[92:95], a[100:103], a[30:33], v[92:95]\n\t" \
  "v_exp_f32_e32 v82, v82\n\t" \
  "v_mfma_f32_16x16x16_bf16 v[88:91], a[104:105], a[34:35], v[88:91]\n\t" \
  "v_exp_f32_e32 v83, v83\n\t" \
  "v_mfma_f32_16x16x16_bf16 v[92:95], a[106:107], a[34:35], v[92:95]\n\t" \
  "v_cvt_pk_bf16_f32 v76, v80, v81\n\t" \
  "v_cvt_pk_bf16_f32 v77, v82, v83\n\t" \
  "v_mfma_f32_16x16x32_bf16 v[96:99], a[72:75], a[36:39], 0\n\t" \
  "v_exp_f32_e32 v84, v84\n\t" \
  "v_mfma_f32_16x16x32_bf16 v[100:103], a[88:91], a[36:39], 0\n\t" \
  "v_exp_f32_e32 v85, v85\n\t" \
  "v_mfma_f32_16x16x32_bf16 v[96:99], a[76:79], a[40:43], v[96:99]\n\t" \
  "v_exp_f32_e32 v86, v86\n\t" \
  "v_mfma_f32_16x16x32_bf16 v[100:103], a[92:95], a[40:43], v[100:103]\n\t" \
  "v_exp_f32_e32 v87, v87\n\t" \
  "v_mfma_f32_16x16x32_bf16 v[96:99], a[80:83], a[44:47], v[96:99]\n\t" \
  "buffer_load_dwordx4 v59, s[16:19], s96 offen offset:1024 sc1 lds\n\t" \
  "v_mfma_f32_16x16x32_bf16 v[100:103], a[96:99], a[44:47], v[100:103]\n\t" \
  "v_cvt_pk_bf16_f32 v78, v84, v85\n\t" \
  "v_cvt_pk_bf16_f32 v79, v86, v87\n\t" \
  "v_mfma_f32_16x16x32_bf16 v[96:99], a[84:87], a[48:51], v[96:99]\n\t" \
  "v_exp_f32_e32 v88, v88\n\t" \
  "v_mfma_f32_16x16x32_bf16 v[100:103], a[100:103], a[48:51], v[100:103]\n\t" \
  "v_exp_f32_e32 v89, v89\n\t" \
  "v_mfma_f32_16x16x16_bf16 v[96:99], a[104:105], a[52:53], v[96:99]\n\t" \
  "v_exp_f32_e32 v90, v90\n\t" \
  "v_mfma_f32_16x16x16_bf16 v[100:103], a[106:107], a[52:53], v[100:103]\n\t" \
  "v_exp_f32_e32 v91, v91\n\t" \
  "v_mfma_f32_16x16x32_bf16 v[104:107], a[72:75], a[54:57], 0\n\t" \
  "v_cvt_pk_bf16_f32 v72, v88, v89\n\t" \
  "v_cvt_pk_bf16_f32 v73, v90, v91\n\t" \
  "v_mfma_f32_16x16x32_bf16 v[108:111], a[88:91], a[54:57], 0\n\t" \
  "v_exp_f32_e32 v92, v92\n\t" \
  "v_mfma_f32_16x16x32_bf16 v[104:107], a[76:79], a[58:61], v[104:107]\n\t" \
  "v_exp_f32_e32 v93, v93\n\t" \
  "v_mfma_f32_16x16x32_bf16 v[108:111], a[92:95], a[58:61], v[108:111]\n\t" \
  "buffer_load_dwordx4 v59, s[16:19], s96 offen offset:2048 sc1 lds\n\t" \
  "v_mfma_f32_16x16x32_bf16 v[104:107], a[80:83], a[62:65], v[104:107]\n\t" \
  "v_exp_f32_e32 v94, v94\n\t" \
  "v_mfma_f32_16x16x32_bf16 v[108:111], a[96:99], a[62:65], v[108:111]\n\t" \
  "v_exp_f32_e32 v95, v95\n\t" \
  "v_mfma_f32_16x16x32_bf16 v[104:107], a[84:87], a[66:69], v[104:107]\n\t" \
  "v_cvt_pk_bf16_f32 v74, v92, v93\n\t" \
  "v_cvt_pk_bf16_f32 v75, v94, v95\n\t" \
  "v_mfma_f32_16x16x32_bf16 v[108:111], a[100:103], a[66:69], v[108:111]\n\t" \
  "v_exp_f32_e32 v96, v96\n\t" \
  "v_mfma_f32_16x16x16_bf16 v[104:107], a[104:105], a[70:71], v[104:107]\n\t" \
  "v_exp_f32_e32 v97, v97\n\t" \
  "v_mfma_f32_16x16x16_bf16 v[108:111], a[106:107], a[70:71], v[108:111]\n\t" \
  "v_exp_f32_e32 v98, v98\n\t" \
  "v_mfma_f32_16x16x32_bf16 v[112:115], a[108:111], v[76:79], v[112:115]\n\t" \
  "v_exp_f32_e32 v99, v99\n\t" \
  "v_mfma_f32_16x16x32_bf16 v[116:119], a[112:115], v[76:79], v[116:119]\n\t" \
  "buffer_load_dwordx4 v59, s[16:19], s96 offen offset:3072 sc1 lds\n\t" \
  "v_mfma_f32_16x16x32_bf16 v[120:123], a[116:119], v[76:79], v[120:123]\n\t" \
  "v_cvt_pk_bf16_f32 v68, v96, v97\n\t" \
  "v_cvt_pk_bf16_f32 v69, v98, v99\n\t" \
  "v_mfma_f32_16x16x32_bf16 v[124:127], a[120:123], v[76:79], v[124:127]\n\t" \
  "v_exp_f32_e32 v100, v100\n\t" \
  "v_mfma_f32_16x16x32_bf16 v[128:131], a[124:127], v[76:79], v[128:131]\n\t" \
  "v_exp_f32_e32 v101, v101\n\t" \
  "v_mfma_f32_16x16x32_bf16 v[132:135], a[128:131], v[76:79], v[132:135]\n\t" \
  "v_exp_f32_e32 v102, v102\n\t" \
  "v_mfma_f32_16x16x32_bf16 v[136:139], a[132:135], v[76:79], v[136:139]\n\t" \
  "v_exp_f32_e32 v103, v103\n\t" \
  "v_mfma_f32_16x16x32_bf16 v[140:143], a[136:139], v[76:79], v[140:143]\n\t" \
  "v_cvt_pk_bf16_f32 v70, v100, v101\n\t" \
  "v_cvt_pk_bf16_f32 v71, v102, v103\n\t" \
  "v_mfma_f32_16x16x32_bf16 v[144:147], a[140:143], v[76:79], v[144:147]\n\t" \
  "v_exp_f32_e32 v104, v104\n\t" \
  "v_mfma_f32_16x16x32_bf16 v[148:151], a[108:111], v[72:75], v[148:151]\n\t" \
  "s_mov_b32 m0, s56\n\t" \
  "s_waitcnt vmcnt(13)\n\t" \
  "v_mfma_f32_16x16x32_bf16 v[152:155], a[112:115], v[72:75], v[152:155]\n\t" \
  "buffer_load_dwordx4 v57, s[16:19], s96 offen offset:0 sc1 lds\n\t" \
  "v_mfma_f32_16x16x32_bf16 v[156:159], a[116:119], v[72:75], v[156:159]\n\t" \
  "v_exp_f32_e32 v105, v105\n\t" \
  "v_mfma_f32_16x16x32_bf16 v[160:163], a[120:123], v[72:75], v[160:163]\n\t" \
  "v_exp_f32_e32 v106, v106\n\t" \
  "v_mfma_f32_16x16x32_bf16 v[164:167], a[124:127], v[72:75], v[164:167]\n\t" \
  "v_exp_f32_e32 v107, v107\n\t" \
  "v_mfma_f32_16x16x32_bf16 v[168:171], a[128:131], v[72:75], v[168:171]\n\t" \
  "v_cvt_pk_bf16_f32 v64, v104, v105\n\t" \
  "v_cvt_pk_bf16_f32 v65, v106, v107\n\t" \
  "v_mfma_f32_16x16x32_bf16 v[172:175], a[132:135], v[72:75], v[172:175]\n\t" \
  "v_exp_f32_e32 v108, v108\n\t" \
  "v_mfma_f32_16x16x32_bf16 v[176:179], a[136:139], v[72:75], v[176:179]\n\t" \
  "v_exp_f32_e32 v109, v109\n\t" \
  "v_mfma_f32_16x16x32_bf16 v[180:183], a[140:143], v[72:75], v[180:183]\n\t" \
  "v_exp_f32_e32 v110, v110\n\t" \
  "v_mfma_f32_16x16x32_bf16 v[184:187], a[108:111], v[68:71], v[184:187]\n\t" \
  "buffer_load_dwordx4 v57, s[16:19], s96 offen offset:1024 sc1 lds\n\t" \
  "v_mfma_f32_16x16x32_bf16 v[188:191], a[112:115], v[68:71], v[188:191]\n\t" \
  "v_exp_f32_e32 v111, v111\n\t" \
  "v_mfma_f32_16x16x32_bf16 v[192:195], a[116:119], v[68:71], v[192:195]\n\t" \
  "v_cvt_pk_bf16_f32 v66, v108, v109\n\t" \
  "v_cvt_pk_bf16_f32 v67, v110, v111\n\t" \
  "v_mfma_f32_16x16x32_bf16 v[196:199], a[120:123], v[68:71], v[196:199]\n\t" \
  "ds_read_b128 a[72:75], v50 offset:0\n\t" \
  "v_mfma_f32_16x16x32_bf16 v[200:203], a[124:127], v[68:71], v[200:203]\n\t" \
  "ds_read_b128 a[88:91], v50 offset:1024\n\t" \
  "v_mfma_f32_16x16x32_bf16 v[204:207], a[128:131], v[68:71], v[204:207]\n\t" \
  "ds_read_b128 a[76:79], v50 offset:2048\n\t" \
  "v_mfma_f32_16x16x32_bf16 v[208:211], a[132:135], v[68:71], v[208:211]\n\t" \
  "ds_read_b128 a[92:95], v50 offset:3072\n\t" \
  "v_mfma_f32_16x16x32_bf16 v[212:215], a[136:139], v[68:71], v[212:215]\n\t" \
  "ds_read_b128 a[80:83], v50 offset:4096\n\t" \
  "v_mfma_f32_16x16x32_bf16 v[216:219], a[140:143], v[68:71], v[216:219]\n\t" \
  "buffer_load_dwordx4 v57, s[16:19], s96 offen offset:2048 sc1 lds\n\t" \
  "v_mfma_f32_16x16x32_bf16 v[220:223], a[108:111], v[64:67], v[220:223]\n\t" \
  "ds_read_b128 a[96:99], v50 offset:5120\n\t" \
  "v_mfma_f32_16x16x32_bf16 v[224:227], a[112:115], v[64:67], v[224:227]\n\t" \
  "ds_read_b64_tr_b16 a[108:109], v53 offset:0\n\t" \
  "ds_read_b128 a[84:87], v50 offset:6144\n\t" \
  "v_mfma_f32_16x16x32_bf16 v[228:231], a[116:119], v[64:67], v[228:231]\n\t" \
  "ds_read_b64_tr_b16 a[110:111], v53 offset:1024\n\t" \
  "ds_read_b128 a[100:103], v50 offset:7168\n\t" \
  "v_mfma_f32_16x16x32_bf16 v[232:235], a[120:123], v[64:67], v[232:235]\n\t" \
  "ds_read_b64_tr_b16 a[112:113], v53 offset:512\n\t" \
  "ds_read2st64_b64 a[104:107], v56 offset0:16 offset1:17\n\t" \
  "v_mfma_f32_16x16x32_bf16 v[236:239], a[124:127], v[64:67], v[236:239]\n\t" \
  "ds_read_b64_tr_b16 a[114:115], v53 offset:1536\n\t" \
  "v_mfma_f32_16x16x32_bf16 v[240:243], a[128:131], v[64:67], v[240:243]\n\t" \
  "ds_read_b64_tr_b16 a[116:117], v53 offset:2048\n\t" \
  "v_mfma_f32_16x16x32_bf16 v[244:247], a[132:135], v[64:67], v[244:247]\n\t" \
  "ds_read_b64_tr_b16 a[118:119], v53 offset:3072\n\t" \
  "v_mfma_f32_16x16x32_bf16 v[248:251], a[136:139], v[64:67], v[248:251]\n\t" \
  "buffer_load_dwordx4 v57, s[16:19], s96 offen offset:3072 sc1 lds\n\t" \
  "v_mfma_f32_16x16x32_bf16 v[252:255], a[140:143], v[64:67], v[252:255]\n\t" \
  "s_waitcnt lgkmcnt(11)\n\t" \
  "v_mfma_f32_16x16x32_bf16 v[80:83], a[72:75], a[0:3], 0\n\t" \
  "ds_read_b64_tr_b16 a[120:121], v53 offset:2560\n\t" \
  "v_mfma_f32_16x16x32_bf16 v[84:87], a[88:91], a[0:3], 0\n\t" \
  "ds_read_b64_tr_b16 a[122:123], v53 offset:3584\n\t" \
  "v_mfma_f32_16x16x32_bf16 v[80:83], a[76:79], a[4:7], v[80:83]\n\t" \
  "ds_read_b64_tr_b16 a[124:125], v53 offset:4096\n\t" \
  "v_mfma_f32_16x16x32_bf16 v[84:87], a[92:95], a[4:7], v[84:87]\n\t" \
  "s_waitcnt lgkmcnt(6)\n\t" \
  "ds_read_b64_tr_b16 a[126:127], v53 offset:5120\n\t" \
  "v_mfma_f32_16x16x32_bf16 v[80:83], a[80:83], a[8:11], v[80:83]\n\t" \
  "ds_read_b64_tr_b16 a[128:129], v53 offset:4608\n\t" \
  "v_mfma_f32_16x16x32_bf16 v[84:87], a[96:99], a[8:11], v[84:87]\n\t" \
  "ds_read_b64_tr_b16 a[130:131], v53 offset:5632\n\t" \
  "s_add_i32 s97, s97, -1\n\t" \
  "v_mfma_f32_16x16x32_bf16 v[80:83], a[84:87], a[12:15], v[80:83]\n\t" \
  "s_mov_b32 m0, s57\n\t" \
  "ds_read_b64_tr_b16 a[132:133], v53 offset:6144\n\t" \
  "v_mfma_f32_16x16x32_bf16 v[84:87], a[100:103], a[12:15], v[84:87]\n\t" \
  "buffer_load_dwordx4 v58, s[16:19], s96 offen offset:0 sc1 lds\n\t" \
  "v_mfma_f32_16x16x16_bf16 v[80:83], a[104:105], a[16:17], v[80:83]\n\t" \
  "s_add_u32 s96, s96, 9216\n\t" \
  "ds_read_b64_tr_b16 a[134:135], v53 offset:7168\n\t" \
  "v_mfma_f32_16x16x16_bf16 v[84:87], a[106:107], a[16:17], v[84:87]\n\t" \
  "s_cmp_eq_u32 s96, s73\n\t" \
  "ds_read_b64_tr_b16 a[136:137], v53 offset:6656\n\t" \
  "v_mfma_f32_16x16x32_bf16 v[88:91], a[72:75], a[18:21], 0\n\t" \
  "s_cselect_b32 s96, s72, s96\n\t" \
  "ds_read_b64_tr_b16 a[138:139], v53 offset:7680\n\t" \
  "v_mfma_f32_16x16x32_bf16 v[92:95], a[88:91], a[18:21], 0\n\t" \
  "ds_read_b64_tr_b16 a[140:141], v53 offset:8192\n\t" \
  "v_mfma_f32_16x16x32_bf16 v[88:91], a[76:79], a[22:25], v[88:91]\n\t" \
  "ds_read_b64_tr_b16 a[142:143], v53 offset:8704\n\t" \
  "v_mfma_f32_16x16x32_bf16 v[92:95], a[92:95], a[22:25], v[92:95]\n\t" \
  "v_exp_f32_e32 v80, v80\n\t" \
  "v_mfma_f32_16x16x32_bf16 v[88:91], a[80:83], a[26:29], v[88:91]\n\t" \
  "v_exp_f32_e32 v81, v81\n\t" \
  "v_mfma_f32_16x16x32_bf16 v[92:95], a[96:99], a[26:29], v[92:95]\n\t" \
  "s_waitcnt lgkmcnt(0)\n\t" \
  "s_mov_b32 m0, s58\n\t" \
  "v_mfma_f32_16x16x32_bf16 v[88:91], a[84:87], a[30:33], v[88:91]\n\t" \
  "buffer_load_dwordx4 v59, s[16:19], s96 offen offset:0 sc1 lds\n\t" \
  "v_mfma_f32_16x16x32_bf16 v[92:95], a[100:103], a[30:33], v[92:95]\n\t" \
  "v_exp_f32_e32 v82, v82\n\t" \
  "v_mfma_f32_16x16x16_bf16 v[88:91], a[104:105], a[34:35], v[88:91]\n\t" \
  "v_exp_f32_e32 v83, v83\n\t" \
  "v_mfma_f32_16x16x16_bf16 v[92:95], a[106:107], a[34:35], v[92:95]\n\t" \
  "v_cvt_pk_bf16_f32 v76, v80, v81\n\t" \
  "v_cvt_pk_bf16_f32 v77, v82, v83\n\t" \
  "v_mfma_f32_16x16x32_bf16 v[96:99], a[72:75], a[36:39], 0\n\t" \
  "v_exp_f32_e32 v84, v84\n\t" \
  "v_mfma_f32_16x16x32_bf16 v[100:103], a[88:91], a[36:39], 0\n\t" \
  "v_exp_f32_e32 v85, v85\n\t" \
  "v_mfma_f32_16x16x32_bf16 v[96:99], a[76:79], a[40:43], v[96:99]\n\t" \
  "v_exp_f32_e32 v86, v86\n\t" \
  "v_mfma_f32_16x16x32_bf16 v[100:103], a[92:95], a[40:43], v[100:103]\n\t" \
  "v_exp_f32_e32 v87, v87\n\t" \
  "v_mfma_f32_16x16x32_bf16 v[96:99], a[80:83], a[44:47], v[96:99]\n\t" \
  "buffer_load_dwordx4 v59, s[16:19], s96 offen offset:1024 sc1 lds\n\t" \
  "v_mfma_f32_16x16x32_bf16 v[100:103], a[96:99], a[44:47], v[100:103]\n\t" \
  "v_cvt_pk_bf16_f32 v78, v84, v85\n\t" \
  "v_cvt_pk_bf16_f32 v79, v86, v87\n\t" \
  "v_mfma_f32_16x16x32_bf16 v[96:99], a[84:87], a[48:51], v[96:99]\n\t" \
  "v_exp_f32_e32 v88, v88\n\t" \
  "v_mfma_f32_16x16x32_bf16 v[100:103], a[100:103], a[48:51], v[100:103]\n\t" \
  "v_exp_f32_e32 v89, v89\n\t" \
  "v_mfma_f32_16x16x16_bf16 v[96:99], a[104:105], a[52:53], v[96:99]\n\t" \
  "v_exp_f32_e32 v90, v90\n\t" \
  "v_mfma_f32_16x16x16_bf16 v[100:103], a[106:107], a[52:53], v[100:103]\n\t" \
  "v_exp_f32_e32 v91, v91\n\t" \
  "v_mfma_f32_16x16x32_bf16 v[104:107], a[72:75], a[54:57], 0\n\t" \
  "v_cvt_pk_bf16_f32 v72, v88, v89\n\t" \
  "v_cvt_pk_bf16_f32 v73, v90, v91\n\t" \
  "v_mfma_f32_16x16x32_bf16 v[108:111], a[88:91], a[54:57], 0\n\t" \
  "v_exp_f32_e32 v92, v92\n\t" \
  "v_mfma_f32_16x16x32_bf16 v[104:107], a[76:79], a[58:61], v[104:107]\n\t" \
  "v_exp_f32_e32 v93, v93\n\t" \
  "v_mfma_f32_16x16x32_bf16 v[108:111], a[92:95], a[58:61], v[108:111]\n\t" \
  "buffer_load_dwordx4 v59, s[16:19], s96 offen offset:2048 sc1 lds\n\t" \
  "v_mfma_f32_16x16x32_bf16 v[104:107], a[80:83], a[62:65], v[104:107]\n\t" \
  "v_exp_f32_e32 v94, v94\n\t" \
  "v_mfma_f32_16x16x32_bf16 v[108:111], a[96:99], a[62:65], v[108:111]\n\t" \
  "v_exp_f32_e32 v95, v95\n\t" \
  "v_mfma_f32_16x16x32_bf16 v[104:107], a[84:87], a[66:69], v[104:107]\n\t" \
  "v_cvt_pk_bf16_f32 v74, v92, v93\n\t" \
  "v_cvt_pk_bf16_f32 v75, v94, v95\n\t" \
  "v_mfma_f32_16x16x32_bf16 v[108:111], a[100:103], a[66:69], v[108:111]\n\t" \
  "v_exp_f32_e32 v96, v96\n\t" \
  "v_mfma_f32_16x16x16_bf16 v[104:107], a[104:105], a[70:71], v[104:107]\n\t" \
  "v_exp_f32_e32 v97, v97\n\t" \
  "v_mfma_f32_16x16x16_bf16 v[108:111], a[106:107], a[70:71], v[108:111]\n\t" \
  "v_exp_f32_e32 v98, v98\n\t" \
  "v_mfma_f32_16x16x32_bf16 v[112:115], a[108:111], v[76:79], v[112:115]\n\t" \
  "v_exp_f32_e32 v99, v99\n\t" \
  "v_mfma_f32_16x16x32_bf16 v[116:119], a[112:115], v[76:79], v[116:119]\n\t" \
  "buffer_load_dwordx4 v59, s[16:19], s96 offen offset:3072 sc1 lds\n\t" \
  "v_mfma_f32_16x16x32_bf16 v[120:123], a[116:119], v[76:79], v[120:123]\n\t" \
  "v_cvt_pk_bf16_f32 v68, v96, v97\n\t" \
  "v_cvt_pk_bf16_f32 v69, v98, v99\n\t" \
  "v_mfma_f32_16x16x32_bf16 v[124:127], a[120:123], v[76:79], v[124:127]\n\t" \
  "v_exp_f32_e32 v100, v100\n\t" \
  "v_mfma_f32_16x16x32_bf16 v[128:131], a[124:127], v[76:79], v[128:131]\n\t" \
  "v_exp_f32_e32 v101, v101\n\t" \
  "v_mfma_f32_16x16x32_bf16 v[132:135], a[128:131], v[76:79], v[132:135]\n\t" \
  "v_exp_f32_e32 v102, v102\n\t" \
  "v_mfma_f32_16x16x32_bf16 v[136:139], a[132:135], v[76:79], v[136:139]\n\t" \
  "v_exp_f32_e32 v103, v103\n\t" \
  "v_mfma_f32_16x16x32_bf16 v[140:143], a[136:139], v[76:79], v[140:143]\n\t" \
  "v_cvt_pk_bf16_f32 v70, v100, v101\n\t" \
  "v_cvt_pk_bf16_f32 v71, v102, v103\n\t" \
  "v_mfma_f32_16x16x32_bf16 v[144:147], a[140:143], v[76:79], v[144:147]\n\t" \
  "v_exp_f32_e32 v104, v104\n\t" \
  "v_mfma_f32_16x16x32_bf16 v[148:151], a[108:111], v[72:75], v[148:151]\n\t" \
  "s_mov_b32 m0, s59\n\t" \
  "s_waitcnt vmcnt(13)\n\t" \
  "v_mfma_f32_16x16x32_bf16 v[152:155], a[112:115], v[72:75], v[152:155]\n\t" \
  "buffer_load_dwordx4 v57, s[16:19], s96 offen offset:0 sc1 lds\n\t" \
  "v_mfma_f32_16x16x32_bf16 v[156:159], a[116:119], v[72:75], v[156:159]\n\t" \
  "v_exp_f32_e32 v105, v105\n\t" \
  "v_mfma_f32_16x16x32_bf16 v[160:163], a[120:123], v[72:75], v[160:163]\n\t" \
  "v_exp_f32_e32 v106, v106\n\t" \
  "v_mfma_f32_16x16x32_bf16 v[164:167], a[124:127], v[72:75], v[164:167]\n\t" \
  "v_exp_f32_e32 v107, v107\n\t" \
  "v_mfma_f32_16x16x32_bf16 v[168:171], a[128:131], v[72:75], v[168:171]\n\t" \
  "v_cvt_pk_bf16_f32 v64, v104, v105\n\t" \
  "v_cvt_pk_bf16_f32 v65, v106, v107\n\t" \
  "v_mfma_f32_16x16x32_bf16 v[172:175], a[132:135], v[72:75], v[172:175]\n\t" \
  "v_exp_f32_e32 v108, v108\n\t" \
  "v_mfma_f32_16x16x32_bf16 v[176:179], a[136:139], v[72:75], v[176:179]\n\t" \
  "v_exp_f32_e32 v109, v109\n\t" \
  "v_mfma_f32_16x16x32_bf16 v[180:183], a[140:143], v[72:75], v[180:183]\n\t" \
  "v_exp_f32_e32 v110, v110\n\t" \
  "v_mfma_f32_16x16x32_bf16 v[184:187], a[108:111], v[68:71], v[184:187]\n\t" \
  "buffer_load_dwordx4 v57, s[16:19], s96 offen offset:1024 sc1 lds\n\t" \
  "v_mfma_f32_16x16x32_bf16 v[188:191], a[112:115], v[68:71], v[188:191]\n\t" \
  "v_exp_f32_e32 v111, v111\n\t" \
  "v_mfma_f32_16x16x32_bf16 v[192:195], a[116:119], v[68:71], v[192:195]\n\t" \
  "v_cvt_pk_bf16_f32 v66, v108, v109\n\t" \
  "v_cvt_pk_bf16_f32 v67, v110, v111\n\t" \
  "v_mfma_f32_16x16x32_bf16 v[196:199], a[120:123], v[68:71], v[196:199]\n\t" \
  "ds_read_b128 a[72:75], v48 offset:0\n\t" \
  "s_cmp_eq_u32 s97, 0\n\t" \
  "v_mfma_f32_16x16x32_bf16 v[200:203], a[124:127], v[68:71], v[200:203]\n\t" \
  "ds_read_b128 a[88:91], v48 offset:1024\n\t" \
  "v_mfma_f32_16x16x32_bf16 v[204:207], a[128:131], v[68:71], v[204:207]\n\t" \
  "ds_read_b128 a[76:79], v48 offset:2048\n\t" \
  "v_mfma_f32_16x16x32_bf16 v[208:211], a[132:135], v[68:71], v[208:211]\n\t" \
  "ds_read_b128 a[92:95], v48 offset:3072\n\t" \
  "v_mfma_f32_16x16x32_bf16 v[212:215], a[136:139], v[68:71], v[212:215]\n\t" \
  "ds_read_b128 a[80:83], v48 offset:4096\n\t" \
  "v_mfma_f32_16x16x32_bf16 v[216:219], a[140:143], v[68:71], v[216:219]\n\t" \
  "buffer_load_dwordx4 v57, s[16:19], s96 offen offset:2048 sc1 lds\n\t" \
  "v_mfma_f32_16x16x32_bf16 v[220:223], a[108:111], v[64:67], v[220:223]\n\t" \
  "ds_read_b128 a[96:99], v48 offset:5120\n\t" \
  "v_mfma_f32_16x16x32_bf16 v[224:227], a[112:115], v[64:67], v[224:227]\n\t" \
  "ds_read_b64_tr_b16 a[108:109], v51 offset:0\n\t" \
  "ds_read_b128 a[84:87], v48 offset:6144\n\t" \
  "v_mfma_f32_16x16x32_bf16 v[228:231], a[116:119], v[64:67], v[228:231]\n\t" \
  "ds_read_b64_tr_b16 a[110:111], v51 offset:1024\n\t" \
  "ds_read_b128 a[100:103], v48 offset:7168\n\t" \
  "v_mfma_f32_16x16x32_bf16 v[232:235], a[120:123], v[64:67], v[232:235]\n\t" \
  "ds_read_b64_tr_b16 a[112:113], v51 offset:512\n\t" \
  "ds_read2st64_b64 a[104:107], v54 offset0:16 offset1:17\n\t" \
  "buffer_store_dwordx2 v[30:31], v35, s[16:19], 0 offen sc1\n\t" \
  "v_mfma_f32_16x16x32_bf16 v[236:239], a[124:127], v[64:67], v[236:239]\n\t" \
  "ds_read_b64_tr_b16 a[114:115], v51 offset:1536\n\t" \
  "v_mfma_f32_16x16x32_bf16 v[240:243], a[128:131], v[64:67], v[240:243]\n\t" \
  "ds_read_b64_tr_b16 a[116:117], v51 offset:2048\n\t" \
  "v_mfma_f32_16x16x32_bf16 v[244:247], a[132:135], v[64:67], v[244:247]\n\t" \
  "ds_read_b64_tr_b16 a[118:119], v51 offset:3072\n\t" \
  "v_mfma_f32_16x16x32_bf16 v[248:251], a[136:139], v[64:67], v[248:251]\n\t" \
  "buffer_load_dwordx4 v57, s[16:19], s96 offen offset:3072 sc1 lds\n\t" \
  "v_mfma_f32_16x16x32_bf16 v[252:255], a[140:143], v[64:67], v[252:255]\n\t" \
  "s_waitcnt lgkmcnt(11)\n\t" \
  "s_cbranch_scc0 LOOP%=\n\t" \
  "v_mfma_f32_16x16x32_bf16 v[80:83], a[72:75], a[0:3], 0\n\t" \
  "ds_read_b64_tr_b16 a[120:121], v51 offset:2560\n\t" \
  "s_mul_i32 s27, s3, 126\n\t" \
  "v_mfma_f32_16x16x32_bf16 v[84:87], a[88:91], a[0:3], 0\n\t" \
  "ds_read_b64_tr_b16 a[122:123], v51 offset:3584\n\t" \
  "buffer_load_dwordx2 v[60:61], v32, s[16:19], 0 offen sc1\n\t" \
  "v_mfma_f32_16x16x32_bf16 v[80:83], a[76:79], a[4:7], v[80:83]\n\t" \
  "ds_read_b64_tr_b16 a[124:125], v51 offset:4096\n\t" \
  "s_add_u32 s27, s27, 120\n\t" \
  "v_mfma_f32_16x16x32_bf16 v[84:87], a[92:95], a[4:7], v[84:87]\n\t" \
  "s_waitcnt lgkmcnt(6)\n\t" \
  "ds_read_b64_tr_b16 a[126:127], v51 offset:5120\n\t" \
  "v_mfma_f32_16x16x32_bf16 v[80:83], a[80:83], a[8:11], v[80:83]\n\t" \
  "ds_read_b64_tr_b16 a[128:129], v51 offset:4608\n\t" \
  "s_mul_i32 s27, s27, 9216\n\t" \
  "v_mfma_f32_16x16x32_bf16 v[84:87], a[96:99], a[8:11], v[84:87]\n\t" \
  "ds_read_b64_tr_b16 a[130:131], v51 offset:5632\n\t" \
  "s_add_u32 s27, s27, 131072\n\t" \
  "v_mfma_f32_16x16x32_bf16 v[80:83], a[84:87], a[12:15], v[80:83]\n\t" \
  "s_mov_b32 m0, s60\n\t" \
  "ds_read_b64_tr_b16 a[132:133], v51 offset:6144\n\t" \
  "v_mfma_f32_16x16x32_bf16 v[84:87], a[100:103], a[12:15], v[84:87]\n\t" \
  "buffer_load_dwordx4 v58, s[16:19], s96 offen offset:0 sc1 lds\n\t" \
  "v_mfma_f32_16x16x16_bf16 v[80:83], a[104:105], a[16:17], v[80:83]\n\t" \
  "s_add_u32 s96, s96, 9216\n\t" \
  "ds_read_b64_tr_b16 a[134:135], v51 offset:7168\n\t" \
  "v_mfma_f32_16x16x16_bf16 v[84:87], a[106:107], a[16:17], v[84:87]\n\t" \
  "s_cmp_eq_u32 s96, s73\n\t" \
  "ds_read_b64_tr_b16 a[136:137], v51 offset:6656\n\t" \
  "v_mfma_f32_16x16x32_bf16 v[88:91], a[72:75], a[18:21], 0\n\t" \
  "s_cselect_b32 s96, s72, s96\n\t" \
  "ds_read_b64_tr_b16 a[138:139], v51 offset:7680\n\t" \
  "v_mfma_f32_16x16x32_bf16 v[92:95], a[88:91], a[18:21], 0\n\t" \
  "ds_read_b64_tr_b16 a[140:141], v51 offset:8192\n\t" \
  "v_add_u32_e32 v12, s76, v59\n\t" \
  "v_mfma_f32_16x16x32_bf16 v[88:91], a[76:79], a[22:25], v[88:91]\n\t" \
  "ds_read_b64_tr_b16 a[142:143], v51 offset:8704\n\t" \
  "v_add_u32_e32 v13, s78, v59\n\t" \
  "v_mfma_f32_16x16x32_bf16 v[92:95], a[92:95], a[22:25], v[92:95]\n\t" \
  "v_exp_f32_e32 v80, v80\n\t" \
  "v_mfma_f32_16x16x32_bf16 v[88:91], a[80:83], a[26:29], v[88:91]\n\t" \
  "v_exp_f32_e32 v81, v81\n\t" \
  "v_mfma_f32_16x16x32_bf16 v[92:95], a[96:99], a[26:29], v[92:95]\n\t" \
  "s_waitcnt lgkmcnt(0)\n\t" \
  "s_mov_b32 m0, s52\n\t" \
  "v_mfma_f32_16x16x32_bf16 v[88:91], a[84:87], a[30:33], v[88:91]\n\t" \
  "buffer_load_dwordx4 v59, s[16:19], s96 offen offset:0 sc1 lds\n\t" \
  "v_mfma_f32_16x16x32_bf16 v[92:95], a[100:103], a[30:33], v[92:95]\n\t" \
  "v_exp_f32_e32 v82, v82\n\t" \
  "v_mfma_f32_16x16x16_bf16 v[88:91], a[104:105], a[34:35], v[88:91]\n\t" \
  "v_exp_f32_e32 v83, v83\n\t" \
  "v_mfma_f32_16x16x16_bf16 v[92:95], a[106:107], a[34:35], v[92:95]\n\t" \
  "v_cvt_pk_bf16_f32 v76, v80, v81\n\t" \
  "v_cvt_pk_bf16_f32 v77, v82, v83\n\t" \
  "v_mfma_f32_16x16x32_bf16 v[96:99], a[72:75], a[36:39], 0\n\t" \
  "v_exp_f32_e32 v84, v84\n\t" \
  "v_mfma_f32_16x16x32_bf16 v[100:103], a[88:91], a[36:39], 0\n\t" \
  "v_exp_f32_e32 v85, v85\n\t" \
  "v_mfma_f32_16x16x32_bf16 v[96:99], a[76:79], a[40:43], v[96:99]\n\t" \
  "v_exp_f32_e32 v86, v86\n\t" \
  "v_mfma_f32_16x16x32_bf16 v[100:103], a[92:95], a[40:43], v[100:103]\n\t" \
  "v_exp_f32_e32 v87, v87\n\t" \
  "v_mfma_f32_16x16x32_bf16 v[96:99], a[80:83], a[44:47], v[96:99]\n\t" \
  "buffer_load_dwordx4 v59, s[16:19], s96 offen offset:1024 sc1 lds\n\t" \
  "v_mfma_f32_16x16x32_bf16 v[100:103], a[96:99], a[44:47], v[100:103]\n\t" \
  "v_cvt_pk_bf16_f32 v78, v84, v85\n\t" \
  "v_cvt_pk_bf16_f32 v79, v86, v87\n\t" \
  "v_mfma_f32_16x16x32_bf16 v[96:99], a[84:87], a[48:51], v[96:99]\n\t" \
  "v_exp_f32_e32 v88, v88\n\t" \
  "v_mfma_f32_16x16x32_bf16 v[100:103], a[100:103], a[48:51], v[100:103]\n\t" \
  "v_exp_f32_e32 v89, v89\n\t" \
  "v_mfma_f32_16x16x16_bf16 v[96:99], a[104:105], a[52:53], v[96:99]\n\t" \
  "v_exp_f32_e32 v90, v90\n\t" \
  "v_mfma_f32_16x16x16_bf16 v[100:103], a[106:107], a[52:53], v[100:103]\n\t" \
  "v_exp_f32_e32 v91, v91\n\t" \
  "v_mfma_f32_16x16x32_bf16 v[104:107], a[72:75], a[54:57], 0\n\t" \
  "v_cvt_pk_bf16_f32 v72, v88, v89\n\t" \
  "v_cvt_pk_bf16_f32 v73, v90, v91\n\t" \
  "v_mfma_f32_16x16x32_bf16 v[108:111], a[88:91], a[54:57], 0\n\t" \
  "v_exp_f32_e32 v92, v92\n\t" \
  "v_mfma_f32_16x16x32_bf16 v[104:107], a[76:79], a[58:61], v[104:107]\n\t" \
  "v_exp_f32_e32 v93, v93\n\t" \
  "v_mfma_f32_16x16x32_bf16 v[108:111], a[92:95], a[58:61], v[108:111]\n\t" \
  "buffer_load_dwordx4 v59, s[16:19], s96 offen offset:2048 sc1 lds\n\t" \
  "v_mfma_f32_16x16x32_bf16 v[104:107], a[80:83], a[62:65], v[104:107]\n\t" \
  "v_exp_f32_e32 v94, v94\n\t" \
  "v_mfma_f32_16x16x32_bf16 v[108:111], a[96:99], a[62:65], v[108:111]\n\t" \
  "v_exp_f32_e32 v95, v95\n\t" \
  "v_mfma_f32_16x16x32_bf16 v[104:107], a[84:87], a[66:69], v[104:107]\n\t" \
  "v_cvt_pk_bf16_f32 v74, v92, v93\n\t" \
  "v_cvt_pk_bf16_f32 v75, v94, v95\n\t" \
  "v_mfma_f32_16x16x32_bf16 v[108:111], a[100:103], a[66:69], v[108:111]\n\t" \
  "v_exp_f32_e32 v96, v96\n\t" \
  "v_mfma_f32_16x16x16_bf16 v[104:107], a[104:105], a[70:71], v[104:107]\n\t" \
  "v_exp_f32_e32 v97, v97\n\t" \
  "v_mfma_f32_16x16x16_bf16 v[108:111], a[106:107], a[70:71], v[108:111]\n\t" \
  "v_exp_f32_e32 v98, v98\n\t" \
  "v_mfma_f32_16x16x32_bf16 v[112:115], a[108:111], v[76:79], v[112:115]\n\t" \
  "v_exp_f32_e32 v99, v99\n\t" \
  "v_mfma_f32_16x16x32_bf16 v[116:119], a[112:115], v[76:79], v[116:119]\n\t" \
  "buffer_load_dwordx4 v59, s[16:19], s96 offen offset:3072 sc1 lds\n\t" \
  "v_mfma_f32_16x16x32_bf16 v[120:123], a[116:119], v[76:79], v[120:123]\n\t" \
  "v_cvt_pk_bf16_f32 v68, v96, v97\n\t" \
  "v_cvt_pk_bf16_f32 v69, v98, v99\n\t" \
  "v_mfma_f32_16x16x32_bf16 v[124:127], a[120:123], v[76:79], v[124:127]\n\t" \
  "v_exp_f32_e32 v100, v100\n\t" \
  "v_mfma_f32_16x16x32_bf16 v[128:131], a[124:127], v[76:79], v[128:131]\n\t" \
  "v_exp_f32_e32 v101, v101\n\t" \
  "v_mfma_f32_16x16x32_bf16 v[132:135], a[128:131], v[76:79], v[132:135]\n\t" \
  "v_exp_f32_e32 v102, v102\n\t" \
  "v_mfma_f32_16x16x32_bf16 v[136:139], a[132:135], v[76:79], v[136:139]\n\t" \
  "v_exp_f32_e32 v103, v103\n\t" \
  "v_mfma_f32_16x16x32_bf16 v[140:143], a[136:139], v[76:79], v[140:143]\n\t" \
  "v_cvt_pk_bf16_f32 v70, v100, v101\n\t" \
  "v_cvt_pk_bf16_f32 v71, v102, v103\n\t" \
  "v_mfma_f32_16x16x32_bf16 v[144:147], a[140:143], v[76:79], v[144:147]\n\t" \
  "v_exp_f32_e32 v104, v104\n\t" \
  "v_mfma_f32_16x16x32_bf16 v[148:151], a[108:111], v[72:75], v[148:151]\n\t" \
  "s_mov_b32 m0, s53\n\t" \
  "s_waitcnt vmcnt(15)\n\t" \
  "v_mfma_f32_16x16x32_bf16 v[152:155], a[112:115], v[72:75], v[152:155]\n\t" \
  "buffer_load_dwordx4 v57, s[16:19], s96 offen offset:0 sc1 lds\n\t" \
  "v_mfma_f32_16x16x32_bf16 v[156:159], a[116:119], v[72:75], v[156:159]\n\t" \
  "v_exp_f32_e32 v105, v105\n\t" \
  "v_mfma_f32_16x16x32_bf16 v[160:163], a[120:123], v[72:75], v[160:163]\n\t" \
  "v_exp_f32_e32 v106, v106\n\t" \
  "v_mfma_f32_16x16x32_bf16 v[164:167], a[124:127], v[72:75], v[164:167]\n\t" \
  "v_exp_f32_e32 v107, v107\n\t" \
  "v_mfma_f32_16x16x32_bf16 v[168:171], a[128:131], v[72:75], v[168:171]\n\t" \
  "v_cvt_pk_bf16_f32 v64, v104, v105\n\t" \
  "v_cvt_pk_bf16_f32 v65, v106, v107\n\t" \
  "v_mfma_f32_16x16x32_bf16 v[172:175], a[132:135], v[72:75], v[172:175]\n\t" \
  "v_exp_f32_e32 v108, v108\n\t" \
  "v_mfma_f32_16x16x32_bf16 v[176:179], a[136:139], v[72:75], v[176:179]\n\t" \
  "v_exp_f32_e32 v109, v109\n\t" \
  "v_mfma_f32_16x16x32_bf16 v[180:183], a[140:143], v[72:75], v[180:183]\n\t" \
  "v_exp_f32_e32 v110, v110\n\t" \
  "v_mfma_f32_16x16x32_bf16 v[184:187], a[108:111], v[68:71], v[184:187]\n\t" \
  "buffer_load_dwordx4 v57, s[16:19], s96 offen offset:1024 sc1 lds\n\t" \
  "v_mfma_f32_16x16x32_bf16 v[188:191], a[112:115], v[68:71], v[188:191]\n\t" \
  "v_exp_f32_e32 v111, v111\n\t" \
  "v_mfma_f32_16x16x32_bf16 v[192:195], a[116:119], v[68:71], v[192:195]\n\t" \
  "v_cvt_pk_bf16_f32 v66, v108, v109\n\t" \
  "v_cvt_pk_bf16_f32 v67, v110, v111\n\t" \
  "v_mfma_f32_16x16x32_bf16 v[196:199], a[120:123], v[68:71], v[196:199]\n\t" \
  "ds_read_b128 a[72:75], v49 offset:0\n\t" \
  "v_add_u32_e32 v14, s77, v59\n\t" \
  "v_mfma_f32_16x16x32_bf16 v[200:203], a[124:127], v[68:71], v[200:203]\n\t" \
  "ds_read_b128 a[88:91], v49 offset:1024\n\t" \
  "s_waitcnt vmcnt(7)\n\t" \
  "v_cmp_ne_u64_e32 vcc, s[92:93], v[60:61]\n\t" \
  "v_mfma_f32_16x16x32_bf16 v[204:207], a[128:131], v[68:71], v[204:207]\n\t" \
  "ds_read_b128 a[76:79], v49 offset:2048\n\t" \
  "s_cbranch_vccnz POLLB%=\n\t" \
  "BACKB%=:\n\t" \
  "v_mfma_f32_16x16x32_bf16 v[208:211], a[132:135], v[68:71], v[208:211]\n\t" \
  "ds_read_b128 a[92:95], v49 offset:3072\n\t" \
  "v_add_u32_e32 v15, s79, v59\n\t" \
  "v_mfma_f32_16x16x32_bf16 v[212:215], a[136:139], v[68:71], v[212:215]\n\t" \
  "ds_read_b128 a[80:83], v49 offset:4096\n\t" \
  "s_lshl_b32 s94, s1, 1\n\t" \
  "v_mfma_f32_16x16x32_bf16 v[216:219], a[140:143], v[68:71], v[216:219]\n\t" \
  "buffer_load_dwordx4 v57, s[16:19], s96 offen offset:2048 sc1 lds\n\t" \
  "v_mfma_f32_16x16x32_bf16 v[220:223], a[108:111], v[64:67], v[220:223]\n\t" \
  "ds_read_b128 a[96:99], v49 offset:5120\n\t" \
  "s_add_u32 s94, s94, s91\n\t" \
  "v_mfma_f32_16x16x32_bf16 v[224:227], a[112:115], v[64:67], v[224:227]\n\t" \
  "ds_read_b64_tr_b16 a[108:109], v52 offset:0\n\t" \
  "ds_read_b128 a[84:87], v49 offset:6144\n\t" \
  "v_mfma_f32_16x16x32_bf16 v[228:231], a[116:119], v[64:67], v[228:231]\n\t" \
  "ds_read_b64_tr_b16 a[110:111], v52 offset:1024\n\t" \
  "ds_read_b128 a[100:103], v49 offset:7168\n\t" \
  "v_mfma_f32_16x16x32_bf16 v[232:235], a[120:123], v[64:67], v[232:235]\n\t" \
  "ds_read_b64_tr_b16 a[112:113], v52 offset:512\n\t" \
  "ds_read2st64_b64 a[104:107], v55 offset0:16 offset1:17\n\t" \
  "v_mfma_f32_16x16x32_bf16 v[236:239], a[124:127], v[64:67], v[236:239]\n\t" \
  "ds_read_b64_tr_b16 a[114:115], v52 offset:1536\n\t" \
  "s_mul_i32 s21, s94, 4608\n\t" \
  "v_mfma_f32_16x16x32_bf16 v[240:243], a[128:131], v[64:67], v[240:243]\n\t" \
  "ds_read_b64_tr_b16 a[116:117], v52 offset:2048\n\t" \
  "s_add_u32 s20, s21, s75\n\t" \
  "v_mfma_f32_16x16x32_bf16 v[244:247], a[132:135], v[64:67], v[244:247]\n\t" \
  "ds_read_b64_tr_b16 a[118:119], v52 offset:3072\n\t" \
  "s_add_u32 s21, s21, 256\n\t" \
  "v_mfma_f32_16x16x32_bf16 v[248:251], a[136:139], v[64:67], v[248:251]\n\t" \
  "buffer_load_dwordx4 v57, s[16:19], s96 offen offset:3072 sc1 lds\n\t" \
  "v_mfma_f32_16x16x32_bf16 v[252:255], a[140:143], v[64:67], v[252:255]\n\t" \
  "s_waitcnt lgkmcnt(11)\n\t" \
  "v_mfma_f32_16x16x32_bf16 v[80:83], a[72:75], a[0:3], 0\n\t" \
  "ds_read_b64_tr_b16 a[120:121], v52 offset:2560\n\t" \
  "v_add_u32 v62, 0xffffffe0, v33\n\t" \
  "s_mov_b32 exec_lo, 0x33333333\n\t" \
  "s_mov_b32 exec_hi, 0x33333333\n\t" \
  "v_add_u32 v62, 0xffffdc20, v33\n\t" \
  "s_mov_b64 exec, -1\n\t" \
  "v_mfma_f32_16x16x32_bf16 v[84:87], a[88:91], a[0:3], 0\n\t" \
  "ds_read_b64_tr_b16 a[122:123], v52 offset:3584\n\t" \
  "s_lshl_b32 s94, s2, 1\n\t" \
  "v_mfma_f32_16x16x32_bf16 v[80:83], a[76:79], a[4:7], v[80:83]\n\t" \
  "ds_read_b64_tr_b16 a[124:125], v52 offset:4096\n\t" \
  "buffer_load_dwordx4 a[144:147], v62, s[12:15], s51 offen sc1\n\t" \
  "v_mfma_f32_16x16x32_bf16 v[84:87], a[92:95], a[4:7], v[84:87]\n\t" \
  "s_waitcnt lgkmcnt(6)\n\t" \
  "ds_read_b64_tr_b16 a[126:127], v52 offset:5120\n\t" \
  "v_mfma_f32_16x16x32_bf16 v[80:83], a[80:83], a[8:11], v[80:83]\n\t" \
  "ds_read_b64_tr_b16 a[128:129], v52 offset:4608\n\t" \
  "s_add_u32 s94, s94, s91\n\t" \
  "v_mfma_f32_16x16x32_bf16 v[84:87], a[96:99], a[8:11], v[84:87]\n\t" \
  "ds_read_b64_tr_b16 a[130:131], v52 offset:5632\n\t" \
  "s_lshl_b32 s95, s46, 4\n\t" \
  "v_mfma_f32_16x16x32_bf16 v[80:83], a[84:87], a[12:15], v[80:83]\n\t" \
  "s_mov_b32 m0, s54\n\t" \
  "ds_read_b64_tr_b16 a[132:133], v52 offset:6144\n\t" \
  "v_mfma_f32_16x16x32_bf16 v[84:87], a[100:103], a[12:15], v[84:87]\n\t" \
  "buffer_load_dwordx4 v58, s[16:19], s96 offen offset:0 sc1 lds\n\t" \
  "v_mfma_f32_16x16x16_bf16 v[80:83], a[104:105], a[16:17], v[80:83]\n\t" \
  "s_add_u32 s96, s96, 9216\n\t" \
  "ds_read_b64_tr_b16 a[134:135], v52 offset:7168\n\t" \
  "v_mfma_f32_16x16x16_bf16 v[84:87], a[106:107], a[16:17], v[84:87]\n\t" \
  "s_cmp_eq_u32 s96, s73\n\t" \
  "ds_read_b64_tr_b16 a[136:137], v52 offset:6656\n\t" \
  "v_mfma_f32_16x16x32_bf16 v[88:91], a[72:75], a[18:21], 0\n\t" \
  "s_cselect_b32 s96, s72, s96\n\t" \
  "ds_read_b64_tr_b16 a[138:139], v52 offset:7680\n\t" \
  "v_mfma_f32_16x16x32_bf16 v[92:95], a[88:91], a[18:21], 0\n\t" \
  "ds_read_b64_tr_b16 a[140:141], v52 offset:8192\n\t" \
  "buffer_load_dwordx4 a[182:185], v62, s[12:15], s88 offen sc1\n\t" \
  "v_mfma_f32_16x16x32_bf16 v[88:91], a[76:79], a[22:25], v[88:91]\n\t" \
  "ds_read_b64_tr_b16 a[142:143], v52 offset:8704\n\t" \
  "s_xor_b32 s94, s94, s95\n\t" \
  "v_mfma_f32_16x16x32_bf16 v[92:95], a[92:95], a[22:25], v[92:95]\n\t" \
  "v_exp_f32_e32 v80, v80\n\t" \
  "v_mfma_f32_16x16x32_bf16 v[88:91], a[80:83], a[26:29], v[88:91]\n\t" \
  "v_exp_f32_e32 v81, v81\n\t" \
  "v_mfma_f32_16x16x32_bf16 v[92:95], a[96:99], a[26:29], v[92:95]\n\t" \
  "s_waitcnt lgkmcnt(0)\n\t" \
  "s_mov_b32 m0, s55\n\t" \
  "v_mfma_f32_16x16x32_bf16 v[88:91], a[84:87], a[30:33], v[88:91]\n\t" \
  "buffer_load_dwordx4 v59, s[16:19], s96 offen offset:0 sc1 lds\n\t" \
  "v_mfma_f32_16x16x32_bf16 v[92:95], a[100:103], a[30:33], v[92:95]\n\t" \
  "v_exp_f32_e32 v82, v82\n\t" \
  "v_mfma_f32_16x16x16_bf16 v[88:91], a[104:105], a[34:35], v[88:91]\n\t" \
  "v_exp_f32_e32 v83, v83\n\t" \
  "v_mfma_f32_16x16x16_bf16 v[92:95], a[106:107], a[34:35], v[92:95]\n\t" \
  "v_cvt_pk_bf16_f32 v76, v80, v81\n\t" \
  "v_cvt_pk_bf16_f32 v77, v82, v83\n\t" \
  "v_mfma_f32_16x16x32_bf16 v[96:99], a[72:75], a[36:39], 0\n\t" \
  "v_exp_f32_e32 v84, v84\n\t" \
  "v_mfma_f32_16x16x32_bf16 v[100:103], a[88:91], a[36:39], 0\n\t" \
  "v_exp_f32_e32 v85, v85\n\t" \
  "v_mfma_f32_16x16x32_bf16 v[96:99], a[76:79], a[40:43], v[96:99]\n\t" \
  "v_exp_f32_e32 v86, v86\n\t" \
  "v_mfma_f32_16x16x32_bf16 v[100:103], a[92:95], a[40:43], v[100:103]\n\t" \
  "v_exp_f32_e32 v87, v87\n\t" \
  "v_mfma_f32_16x16x32_bf16 v[96:99], a[80:83], a[44:47], v[96:99]\n\t" \
  "buffer_load_dwordx4 v59, s[16:19], s96 offen offset:1024 sc1 lds\n\t" \
  "v_mfma_f32_16x16x32_bf16 v[100:103], a[96:99], a[44:47], v[100:103]\n\t" \
  "v_cvt_pk_bf16_f32 v78, v84, v85\n\t" \
  "v_cvt_pk_bf16_f32 v79, v86, v87\n\t" \
  "v_mfma_f32_16x16x32_bf16 v[96:99], a[84:87], a[48:51], v[96:99]\n\t" \
  "v_exp_f32_e32 v88, v88\n\t" \
  "v_mfma_f32_16x16x32_bf16 v[100:103], a[100:103], a[48:51], v[100:103]\n\t" \
  "v_exp_f32_e32 v89, v89\n\t" \
  "v_mfma_f32_16x16x16_bf16 v[96:99], a[104:105], a[52:53], v[96:99]\n\t" \
  "v_exp_f32_e32 v90, v90\n\t" \
  "v_mfma_f32_16x16x16_bf16 v[100:103], a[106:107], a[52:53], v[100:103]\n\t" \
  "v_exp_f32_e32 v91, v91\n\t" \
  "v_mfma_f32_16x16x32_bf16 v[104:107], a[72:75], a[54:57], 0\n\t" \
  "v_cvt_pk_bf16_f32 v72, v88, v89\n\t" \
  "v_cvt_pk_bf16_f32 v73, v90, v91\n\t" \
  "v_mfma_f32_16x16x32_bf16 v[108:111], a[88:91], a[54:57], 0\n\t" \
  "v_exp_f32_e32 v92, v92\n\t" \
  "v_mfma_f32_16x16x32_bf16 v[104:107], a[76:79], a[58:61], v[104:107]\n\t" \
  "v_exp_f32_e32 v93, v93\n\t" \
  "v_mfma_f32_16x16x32_bf16 v[108:111], a[92:95], a[58:61], v[108:111]\n\t" \
  "buffer_load_dwordx4 v59, s[16:19], s96 offen offset:2048 sc1 lds\n\t" \
  "v_mfma_f32_16x16x32_bf16 v[104:107], a[80:83], a[62:65], v[104:107]\n\t" \
  "v_exp_f32_e32 v94, v94\n\t" \
  "v_mfma_f32_16x16x32_bf16 v[108:111], a[96:99], a[62:65], v[108:111]\n\t" \
  "v_exp_f32_e32 v95, v95\n\t" \
  "v_mfma_f32_16x16x32_bf16 v[104:107], a[84:87], a[66:69], v[104:107]\n\t" \
  "v_cvt_pk_bf16_f32 v74, v92, v93\n\t" \
  "v_cvt_pk_bf16_f32 v75, v94, v95\n\t" \
  "v_mfma_f32_16x16x32_bf16 v[108:111], a[100:103], a[66:69], v[108:111]\n\t" \
  "v_exp_f32_e32 v96, v96\n\t" \
  "v_mfma_f32_16x16x16_bf16 v[104:107], a[104:105], a[70:71], v[104:107]\n\t" \
  "v_exp_f32_e32 v97, v97\n\t" \
  "v_mfma_f32_16x16x16_bf16 v[108:111], a[106:107], a[70:71], v[108:111]\n\t" \
  "v_exp_f32_e32 v98, v98\n\t" \
  "v_mfma_f32_16x16x32_bf16 v[112:115], a[108:111], v[76:79], v[112:115]\n\t" \
  "v_exp_f32_e32 v99, v99\n\t" \
  "v_mfma_f32_16x16x32_bf16 v[116:119], a[112:115], v[76:79], v[116:119]\n\t" \
  "buffer_load_dwordx4 v59, s[16:19], s96 offen offset:3072 sc1 lds\n\t" \
  "v_mfma_f32_16x16x32_bf16 v[120:123], a[116:119], v[76:79], v[120:123]\n\t" \
  "v_cvt_pk_bf16_f32 v68, v96, v97\n\t" \
  "v_cvt_pk_bf16_f32 v69, v98, v99\n\t" \
  "v_mfma_f32_16x16x32_bf16 v[124:127], a[120:123], v[76:79], v[124:127]\n\t" \
  "v_exp_f32_e32 v100, v100\n\t" \
  "v_mfma_f32_16x16x32_bf16 v[128:131], a[124:127], v[76:79], v[128:131]\n\t" \
  "v_exp_f32_e32 v101, v101\n\t" \
  "v_mfma_f32_16x16x32_bf16 v[132:135], a[128:131], v[76:79], v[132:135]\n\t" \
  "v_exp_f32_e32 v102, v102\n\t" \
  "v_mfma_f32_16x16x32_bf16 v[136:139], a[132:135], v[76:79], v[136:139]\n\t" \
  "v_exp_f32_e32 v103, v103\n\t" \
  "v_mfma_f32_16x16x32_bf16 v[140:143], a[136:139], v[76:79], v[140:143]\n\t" \
  "v_cvt_pk_bf16_f32 v70, v100, v101\n\t" \
  "v_cvt_pk_bf16_f32 v71, v102, v103\n\t" \
  "v_mfma_f32_16x16x32_bf16 v[144:147], a[140:143], v[76:79], v[144:147]\n\t" \
  "v_exp_f32_e32 v104, v104\n\t" \
  "v_mfma_f32_16x16x32_bf16 v[148:151], a[108:111], v[72:75], v[148:151]\n\t" \
  "s_mov_b32 m0, s56\n\t" \
  "s_waitcnt vmcnt(15)\n\t" \
  "v_mfma_f32_16x16x32_bf16 v[152:155], a[112:115], v[72:75], v[152:155]\n\t" \
  "buffer_load_dwordx4 v57, s[16:19], s96 offen offset:0 sc1 lds\n\t" \
  "v_mfma_f32_16x16x32_bf16 v[156:159], a[116:119], v[72:75], v[156:159]\n\t" \
  "v_exp_f32_e32 v105, v105\n\t" \
  "v_mfma_f32_16x16x32_bf16 v[160:163], a[120:123], v[72:75], v[160:163]\n\t" \
  "v_exp_f32_e32 v106, v106\n\t" \
  "v_mfma_f32_16x16x32_bf16 v[164:167], a[124:127], v[72:75], v[164:167]\n\t" \
  "v_exp_f32_e32 v107, v107\n\t" \
  "v_mfma_f32_16x16x32_bf16 v[168:171], a[128:131], v[72:75], v[168:171]\n\t" \
  "v_cvt_pk_bf16_f32 v64, v104, v105\n\t" \
  "v_cvt_pk_bf16_f32 v65, v106, v107\n\t" \
  "v_mfma_f32_16x16x32_bf16 v[172:175], a[132:135], v[72:75], v[172:175]\n\t" \
  "v_exp_f32_e32 v108, v108\n\t" \
  "v_mfma_f32_16x16x32_bf16 v[176:179], a[136:139], v[72:75], v[176:179]\n\t" \
  "v_exp_f32_e32 v109, v109\n\t" \
  "v_mfma_f32_16x16x32_bf16 v[180:183], a[140:143], v[72:75], v[180:183]\n\t" \
  "v_exp_f32_e32 v110, v110\n\t" \
  "v_mfma_f32_16x16x32_bf16 v[184:187], a[108:111], v[68:71], v[184:187]\n\t" \
  "buffer_load_dwordx4 v57, s[16:19], s96 offen offset:1024 sc1 lds\n\t" \
  "v_mfma_f32_16x16x32_bf16 v[188:191], a[112:115], v[68:71], v[188:191]\n\t" \
  "v_exp_f32_e32 v111, v111\n\t" \
  "v_mfma_f32_16x16x32_bf16 v[192:195], a[116:119], v[68:71], v[192:195]\n\t" \
  "v_cvt_pk_bf16_f32 v66, v108, v109\n\t" \
  "v_cvt_pk_bf16_f32 v67, v110, v111\n\t" \
  "v_mfma_f32_16x16x32_bf16 v[196:199], a[120:123], v[68:71], v[196:199]\n\t" \
  "ds_read_b128 a[72:75], v50 offset:0\n\t" \
  "s_mul_i32 s22, s94, 9216\n\t" \
  "v_mfma_f32_16x16x32_bf16 v[200:203], a[124:127], v[68:71], v[200:203]\n\t" \
  "ds_read_b128 a[88:91], v50 offset:1024\n\t" \
  "buffer_load_dwordx4 a[220:223], v62, s[12:15], s71 offen sc1\n\t" \
  "v_add_u32 v62, 0xfffffff0, v33\n\t" \
  "s_mov_b32 exec_lo, 0x11111111\n\t" \
  "s_mov_b32 exec_hi, 0x11111111\n\t" \
  "v_add_u32 v62, 0xffffdc30, v33\n\t" \
  "s_mov_b64 exec, -1\n\t" \
  "v_mfma_f32_16x16x32_bf16 v[204:207], a[128:131], v[68:71], v[204:207]\n\t" \
  "ds_read_b128 a[76:79], v50 offset:2048\n\t" \
  "s_add_u32 s22, s22, 4757504\n\t" \
  "v_mfma_f32_16x16x32_bf16 v[208:211], a[132:135], v[68:71], v[208:211]\n\t" \
  "ds_read_b128 a[92:95], v50 offset:3072\n\t" \
  "buffer_load_dwordx4 a[148:151], v62, s[12:15], s51 offen sc1\n\t" \
  "v_mfma_f32_16x16x32_bf16 v[212:215], a[136:139], v[68:71], v[212:215]\n\t" \
  "ds_read_b128 a[80:83], v50 offset:4096\n\t" \
  "s_add_u32 s23, s22, 4096\n\t" \
  "v_mfma_f32_16x16x32_bf16 v[216:219], a[140:143], v[68:71], v[216:219]\n\t" \
  "buffer_load_dwordx4 v57, s[16:19], s96 offen offset:2048 sc1 lds\n\t" \
  "v_mfma_f32_16x16x32_bf16 v[220:223], a[108:111], v[64:67], v[220:223]\n\t" \
  "ds_read_b128 a[96:99], v50 offset:5120\n\t" \
  "s_add_u32 s26, s22, 8192\n\t" \
  "v_mfma_f32_16x16x32_bf16 v[224:227], a[112:115], v[64:67], v[224:227]\n\t" \
  "ds_read_b64_tr_b16 a[108:109], v53 offset:0\n\t" \
  "ds_read_b128 a[84:87], v50 offset:6144\n\t" \
  "v_mfma_f32_16x16x32_bf16 v[228:231], a[116:119], v[64:67], v[228:231]\n\t" \
  "ds_read_b64_tr_b16 a[110:111], v53 offset:1024\n\t" \
  "ds_read_b128 a[100:103], v50 offset:7168\n\t" \
  "v_mfma_f32_16x16x32_bf16 v[232:235], a[120:123], v[64:67], v[232:235]\n\t" \
  "ds_read_b64_tr_b16 a[112:113], v53 offset:512\n\t" \
  "ds_read2st64_b64 a[104:107], v56 offset0:16 offset1:17\n\t" \
  "buffer_load_dwordx4 a[186:189], v62, s[12:15], s88 offen sc1\n\t" \
  "v_mfma_f32_16x16x32_bf16 v[236:239], a[124:127], v[64:67], v[236:239]\n\t" \
  "ds_read_b64_tr_b16 a[114:115], v53 offset:1536\n\t" \
  "s_mov_b64 s[24:25], -1\n\t" \
  "v_mfma_f32_16x16x32_bf16 v[240:243], a[128:131], v[64:67], v[240:243]\n\t" \
  "ds_read_b64_tr_b16 a[116:117], v53 offset:2048\n\t" \
  "buffer_load_dwordx4 a[224:227], v62, s[12:15], s71 offen sc1\n\t" \
  "v_mfma_f32_16x16x32_bf16 v[244:247], a[132:135], v[64:67], v[244:247]\n\t" \
  "ds_read_b64_tr_b16 a[118:119], v53 offset:3072\n\t" \
  "v_lshrrev_b32_e32 v1, 4, v0\n\t" \
  "v_mfma_f32_16x16x32_bf16 v[248:251], a[136:139], v[64:67], v[248:251]\n\t" \
  "buffer_load_dwordx4 v57, s[16:19], s96 offen offset:3072 sc1 lds\n\t" \
  "v_mfma_f32_16x16x32_bf16 v[252:255], a[140:143], v[64:67], v[252:255]\n\t" \
  "s_waitcnt lgkmcnt(11)\n\t" \
  "v_mfma_f32_16x16x32_bf16 v[80:83], a[72:75], a[0:3], 0\n\t" \
  "ds_read_b64_tr_b16 a[120:121], v53 offset:2560\n\t" \
  "v_mul_u32_u24_e32 v1, 20, v1\n\t" \
  "v_mfma_f32_16x16x32_bf16 v[84:87], a[88:91], a[0:3], 0\n\t" \
  "ds_read_b64_tr_b16 a[122:123], v53 offset:3584\n\t" \
  "v_sub_u32_e32 v1, v0, v1\n\t" \
  "v_mfma_f32_16x16x32_bf16 v[80:83], a[76:79], a[4:7], v[80:83]\n\t" \
  "ds_read_b64_tr_b16 a[124:125], v53 offset:4096\n\t" \
  "buffer_load_dwordx4 a[152:155], v33, s[12:15], s51 offen sc1\n\t" \
  "v_mfma_f32_16x16x32_bf16 v[84:87], a[92:95], a[4:7], v[84:87]\n\t" \
  "s_waitcnt lgkmcnt(6)\n\t" \
  "ds_read_b64_tr_b16 a[126:127], v53 offset:5120\n\t" \
  "v_mfma_f32_16x16x32_bf16 v[80:83], a[80:83], a[8:11], v[80:83]\n\t" \
  "ds_read_b64_tr_b16 a[128:129], v53 offset:4608\n\t" \
  "v_lshrrev_b32_e32 v30, 2, v1\n\t" \
  "v_mfma_f32_16x16x32_bf16 v[84:87], a[96:99], a[8:11], v[84:87]\n\t" \
  "ds_read_b64_tr_b16 a[130:131], v53 offset:5632\n\t" \
  "v_min_u32_e32 v30, 1, v30\n\t" \
  "v_mfma_f32_16x16x32_bf16 v[80:83], a[84:87], a[12:15], v[80:83]\n\t" \
  "s_mov_b32 m0, s57\n\t" \
  "ds_read_b64_tr_b16 a[132:133], v53 offset:6144\n\t" \
  "v_mfma_f32_16x16x32_bf16 v[84:87], a[100:103], a[12:15], v[84:87]\n\t" \
  "buffer_load_dwordx4 v58, s[16:19], s96 offen offset:0 sc1 lds\n\t" \
  "v_mfma_f32_16x16x16_bf16 v[80:83], a[104:105], a[16:17], v[80:83]\n\t" \
  "s_add_u32 s96, s96, 9216\n\t" \
  "ds_read_b64_tr_b16 a[134:135], v53 offset:7168\n\t" \
  "v_mfma_f32_16x16x16_bf16 v[84:87], a[106:107], a[16:17], v[84:87]\n\t" \
  "s_cmp_eq_u32 s96, s73\n\t" \
  "ds_read_b64_tr_b16 a[136:137], v53 offset:6656\n\t" \
  "v_mfma_f32_16x16x32_bf16 v[88:91], a[72:75], a[18:21], 0\n\t" \
  "s_cselect_b32 s96, s72, s96\n\t" \
  "ds_read_b64_tr_b16 a[138:139], v53 offset:7680\n\t" \
  "v_mfma_f32_16x16x32_bf16 v[92:95], a[88:91], a[18:21], 0\n\t" \
  "ds_read_b64_tr_b16 a[140:141], v53 offset:8192\n\t" \
  "buffer_load_dwordx4 a[190:193], v33, s[12:15], s88 offen sc1\n\t" \
  "v_mfma_f32_16x16x32_bf16 v[88:91], a[76:79], a[22:25], v[88:91]\n\t" \
  "ds_read_b64_tr_b16 a[142:143], v53 offset:8704\n\t" \
  "v_sub_u32_e32 v30, 1, v30\n\t" \
  "v_mfma_f32_16x16x32_bf16 v[92:95], a[92:95], a[22:25], v[92:95]\n\t" \
  "v_exp_f32_e32 v80, v80\n\t" \
  "v_mfma_f32_16x16x32_bf16 v[88:91], a[80:83], a[26:29], v[88:91]\n\t" \
  "v_exp_f32_e32 v81, v81\n\t" \
  "v_mfma_f32_16x16x32_bf16 v[92:95], a[96:99], a[26:29], v[92:95]\n\t" \
  "s_waitcnt lgkmcnt(0)\n\t" \
  "s_mov_b32 m0, s58\n\t" \
  "v_mfma_f32_16x16x32_bf16 v[88:91], a[84:87], a[30:33], v[88:91]\n\t" \
  "buffer_load_dwordx4 v59, s[16:19], s96 offen offset:0 sc1 lds\n\t" \
  "v_mfma_f32_16x16x32_bf16 v[92:95], a[100:103], a[30:33], v[92:95]\n\t" \
  "v_exp_f32_e32 v82, v82\n\t" \
  "v_mfma_f32_16x16x16_bf16 v[88:91], a[104:105], a[34:35], v[88:91]\n\t" \
  "v_exp_f32_e32 v83, v83\n\t" \
  "v_mfma_f32_16x16x16_bf16 v[92:95], a[106:107], a[34:35], v[92:95]\n\t" \
  "v_cvt_pk_bf16_f32 v76, v80, v81\n\t" \
  "v_cvt_pk_bf16_f32 v77, v82, v83\n\t" \
  "v_mfma_f32_16x16x32_bf16 v[96:99], a[72:75], a[36:39], 0\n\t" \
  "v_exp_f32_e32 v84, v84\n\t" \
  "v_mfma_f32_16x16x32_bf16 v[100:103], a[88:91], a[36:39], 0\n\t" \
  "v_exp_f32_e32 v85, v85\n\t" \
  "v_mfma_f32_16x16x32_bf16 v[96:99], a[76:79], a[40:43], v[96:99]\n\t" \
  "v_exp_f32_e32 v86, v86\n\t" \
  "v_mfma_f32_16x16x32_bf16 v[100:103], a[92:95], a[40:43], v[100:103]\n\t" \
  "v_exp_f32_e32 v87, v87\n\t" \
  "v_mfma_f32_16x16x32_bf16 v[96:99], a[80:83], a[44:47], v[96:99]\n\t" \
  "buffer_load_dwordx4 v59, s[16:19], s96 offen offset:1024 sc1 lds\n\t" \
  "v_mfma_f32_16x16x32_bf16 v[100:103], a[96:99], a[44:47], v[100:103]\n\t" \
  "v_cvt_pk_bf16_f32 v78, v84, v85\n\t" \
  "v_cvt_pk_bf16_f32 v79, v86, v87\n\t" \
  "v_mfma_f32_16x16x32_bf16 v[96:99], a[84:87], a[48:51], v[96:99]\n\t" \
  "v_exp_f32_e32 v88, v88\n\t" \
  "v_mfma_f32_16x16x32_bf16 v[100:103], a[100:103], a[48:51], v[100:103]\n\t" \
  "v_exp_f32_e32 v89, v89\n\t" \
  "v_mfma_f32_16x16x16_bf16 v[96:99], a[104:105], a[52:53], v[96:99]\n\t" \
  "v_exp_f32_e32 v90, v90\n\t" \
  "v_mfma_f32_16x16x16_bf16 v[100:103], a[106:107], a[52:53], v[100:103]\n\t" \
  "v_exp_f32_e32 v91, v91\n\t" \
  "v_mfma_f32_16x16x32_bf16 v[104:107], a[72:75], a[54:57], 0\n\t" \
  "v_cvt_pk_bf16_f32 v72, v88, v89\n\t" \
  "v_cvt_pk_bf16_f32 v73, v90, v91\n\t" \
  "v_mfma_f32_16x16x32_bf16 v[108:111], a[88:91], a[54:57], 0\n\t" \
  "v_exp_f32_e32 v92, v92\n\t" \
  "v_mfma_f32_16x16x32_bf16 v[104:107], a[76:79], a[58:61], v[104:107]\n\t" \
  "v_exp_f32_e32 v93, v93\n\t" \
  "v_mfma_f32_16x16x32_bf16 v[108:111], a[92:95], a[58:61], v[108:111]\n\t" \
  "buffer_load_dwordx4 v59, s[16:19], s96 offen offset:2048 sc1 lds\n\t" \
  "v_mfma_f32_16x16x32_bf16 v[104:107], a[80:83], a[62:65], v[104:107]\n\t" \
  "v_exp_f32_e32 v94, v94\n\t" \
  "v_mfma_f32_16x16x32_bf16 v[108:111], a[96:99], a[62:65], v[108:111]\n\t" \
  "v_exp_f32_e32 v95, v95\n\t" \
  "v_mfma_f32_16x16x32_bf16 v[104:107], a[84:87], a[66:69], v[104:107]\n\t" \
  "v_cvt_pk_bf16_f32 v74, v92, v93\n\t" \
  "v_cvt_pk_bf16_f32 v75, v94, v95\n\t" \
  "v_mfma_f32_16x16x32_bf16 v[108:111], a[100:103], a[66:69], v[108:111]\n\t" \
  "v_exp_f32_e32 v96, v96\n\t" \
  "v_mfma_f32_16x16x16_bf16 v[104:107], a[104:105], a[70:71], v[104:107]\n\t" \
  "v_exp_f32_e32 v97, v97\n\t" \
  "v_mfma_f32_16x16x16_bf16 v[108:111], a[106:107], a[70:71], v[108:111]\n\t" \
  "v_exp_f32_e32 v98, v98\n\t" \
  "v_mfma_f32_16x16x32_bf16 v[112:115], a[108:111], v[76:79], v[112:115]\n\t" \
  "v_exp_f32_e32 v99, v99\n\t" \
  "v_mfma_f32_16x16x32_bf16 v[116:119], a[112:115], v[76:79], v[116:119]\n\t" \
  "buffer_load_dwordx4 v59, s[16:19], s96 offen offset:3072 sc1 lds\n\t" \
  "v_mfma_f32_16x16x32_bf16 v[120:123], a[116:119], v[76:79], v[120:123]\n\t" \
  "v_cvt_pk_bf16_f32 v68, v96, v97\n\t" \
  "v_cvt_pk_bf16_f32 v69, v98, v99\n\t" \
  "v_mfma_f32_16x16x32_bf16 v[124:127], a[120:123], v[76:79], v[124:127]\n\t" \
  "v_exp_f32_e32 v100, v100\n\t" \
  "v_mfma_f32_16x16x32_bf16 v[128:131], a[124:127], v[76:79], v[128:131]\n\t" \
  "v_exp_f32_e32 v101, v101\n\t" \
  "v_mfma_f32_16x16x32_bf16 v[132:135], a[128:131], v[76:79], v[132:135]\n\t" \
  "v_exp_f32_e32 v102, v102\n\t" \
  "v_mfma_f32_16x16x32_bf16 v[136:139], a[132:135], v[76:79], v[136:139]\n\t" \
  "v_exp_f32_e32 v103, v103\n\t" \
  "v_mfma_f32_16x16x32_bf16 v[140:143], a[136:139], v[76:79], v[140:143]\n\t" \
  "v_cvt_pk_bf16_f32 v70, v100, v101\n\t" \
  "v_cvt_pk_bf16_f32 v71, v102, v103\n\t" \
  "v_mfma_f32_16x16x32_bf16 v[144:147], a[140:143], v[76:79], v[144:147]\n\t" \
  "v_exp_f32_e32 v104, v104\n\t" \
  "v_mfma_f32_16x16x32_bf16 v[148:151], a[108:111], v[72:75], v[148:151]\n\t" \
  "s_mov_b32 m0, s59\n\t" \
  "s_waitcnt vmcnt(20)\n\t" \
  "v_mfma_f32_16x16x32_bf16 v[152:155], a[112:115], v[72:75], v[152:155]\n\t" \
  "buffer_load_dwordx4 v57, s[16:19], s96 offen offset:0 sc1 lds\n\t" \
  "v_mfma_f32_16x16x32_bf16 v[156:159], a[116:119], v[72:75], v[156:159]\n\t" \
  "v_exp_f32_e32 v105, v105\n\t" \
  "v_mfma_f32_16x16x32_bf16 v[160:163], a[120:123], v[72:75], v[160:163]\n\t" \
  "v_exp_f32_e32 v106, v106\n\t" \
  "v_mfma_f32_16x16x32_bf16 v[164:167], a[124:127], v[72:75], v[164:167]\n\t" \
  "v_exp_f32_e32 v107, v107\n\t" \
  "v_mfma_f32_16x16x32_bf16 v[168:171], a[128:131], v[72:75], v[168:171]\n\t" \
  "v_cvt_pk_bf16_f32 v64, v104, v105\n\t" \
  "v_cvt_pk_bf16_f32 v65, v106, v107\n\t" \
  "v_mfma_f32_16x16x32_bf16 v[172:175], a[132:135], v[72:75], v[172:175]\n\t" \
  "v_exp_f32_e32 v108, v108\n\t" \
  "v_mfma_f32_16x16x32_bf16 v[176:179], a[136:139], v[72:75], v[176:179]\n\t" \
  "v_exp_f32_e32 v109, v109\n\t" \
  "v_mfma_f32_16x16x32_bf16 v[180:183], a[140:143], v[72:75], v[180:183]\n\t" \
  "v_exp_f32_e32 v110, v110\n\t" \
  "v_mfma_f32_16x16x32_bf16 v[184:187], a[108:111], v[68:71], v[184:187]\n\t" \
  "buffer_load_dwordx4 v57, s[16:19], s96 offen offset:1024 sc1 lds\n\t" \
  "v_mfma_f32_16x16x32_bf16 v[188:191], a[112:115], v[68:71], v[188:191]\n\t" \
  "v_exp_f32_e32 v111, v111\n\t" \
  "v_mfma_f32_16x16x32_bf16 v[192:195], a[116:119], v[68:71], v[192:195]\n\t" \
  "v_cvt_pk_bf16_f32 v66, v108, v109\n\t" \
  "v_cvt_pk_bf16_f32 v67, v110, v111\n\t" \
  "v_mfma_f32_16x16x32_bf16 v[196:199], a[120:123], v[68:71], v[196:199]\n\t" \
  "ds_read_b128 a[72:75], v48 offset:0\n\t" \
  "v_mul_u32_u24_e32 v30, 16256, v30\n\t" \
  "v_mfma_f32_16x16x32_bf16 v[200:203], a[124:127], v[68:71], v[200:203]\n\t" \
  "ds_read_b128 a[88:91], v48 offset:1024\n\t" \
  "buffer_load_dwordx4 a[228:231], v33, s[12:15], s71 offen sc1\n\t" \
  "v_add_u32 v62, 0x10, v33\n\t" \
  "s_mov_b32 exec_lo, 0x88888888\n\t" \
  "s_mov_b32 exec_hi, 0x88888888\n\t" \
  "v_add_u32 v62, 0x23d0, v33\n\t" \
  "s_mov_b64 exec, -1\n\t" \
  "v_mfma_f32_16x16x32_bf16 v[204:207], a[128:131], v[68:71], v[204:207]\n\t" \
  "ds_read_b128 a[76:79], v48 offset:2048\n\t" \
  "v_mov_b32_e32 v31, 0\n\t" \
  "v_mfma_f32_16x16x32_bf16 v[208:211], a[132:135], v[68:71], v[208:211]\n\t" \
  "ds_read_b128 a[92:95], v48 offset:3072\n\t" \
  "buffer_load_dwordx4 a[156:159], v62, s[12:15], s51 offen sc1\n\t" \
  "v_mfma_f32_16x16x32_bf16 v[212:215], a[136:139], v[68:71], v[212:215]\n\t" \
  "ds_read_b128 a[80:83], v48 offset:4096\n\t" \
  "v_lshlrev_b32_e32 v1, 4, v1\n\t" \
  "v_mfma_f32_16x16x32_bf16 v[216:219], a[140:143], v[68:71], v[216:219]\n\t" \
  "buffer_load_dwordx4 v57, s[16:19], s96 offen offset:2048 sc1 lds\n\t" \
  "v_mfma_f32_16x16x32_bf16 v[220:223], a[108:111], v[64:67], v[220:223]\n\t" \
  "ds_read_b128 a[96:99], v48 offset:5120\n\t" \
  "v_lshlrev_b64 v[30:31], v1, v[30:31]\n\t" \
  "v_mfma_f32_16x16x32_bf16 v[224:227], a[112:115], v[64:67], v[224:227]\n\t" \
  "ds_read_b64_tr_b16 a[108:109], v51 offset:0\n\t" \
  "ds_read_b128 a[84:87], v48 offset:6144\n\t" \
  "v_mfma_f32_16x16x32_bf16 v[228:231], a[116:119], v[64:67], v[228:231]\n\t" \
  "ds_read_b64_tr_b16 a[110:111], v51 offset:1024\n\t" \
  "ds_read_b128 a[100:103], v48 offset:7168\n\t" \
  "v_mfma_f32_16x16x32_bf16 v[232:235], a[120:123], v[64:67], v[232:235]\n\t" \
  "ds_read_b64_tr_b16 a[112:113], v51 offset:512\n\t" \
  "ds_read2st64_b64 a[104:107], v54 offset0:16 offset1:17\n\t" \
  "buffer_load_dwordx4 a[194:197], v62, s[12:15], s88 offen sc1\n\t" \
  "v_mfma_f32_16x16x32_bf16 v[236:239], a[124:127], v[64:67], v[236:239]\n\t" \
  "ds_read_b64_tr_b16 a[114:115], v51 offset:1536\n\t" \
  "v_mfma_f32_16x16x32_bf16 v[240:243], a[128:131], v[64:67], v[240:243]\n\t" \
  "ds_read_b64_tr_b16 a[116:117], v51 offset:2048\n\t" \
  "buffer_load_dwordx4 a[232:235], v62, s[12:15], s71 offen sc1\n\t" \
  "v_mfma_f32_16x16x32_bf16 v[244:247], a[132:135], v[64:67], v[244:247]\n\t" \
  "ds_read_b64_tr_b16 a[118:119], v51 offset:3072\n\t" \
  "v_mfma_f32_16x16x32_bf16 v[248:251], a[136:139], v[64:67], v[248:251]\n\t" \
  "buffer_load_dwordx4 v57, s[16:19], s96 offen offset:3072 sc1 lds\n\t" \
  "v_mfma_f32_16x16x32_bf16 v[252:255], a[140:143], v[64:67], v[252:255]\n\t" \
  "s_waitcnt lgkmcnt(11)\n\t" \
  "v_mfma_f32_16x16x32_bf16 v[80:83], a[72:75], a[0:3], 0\n\t" \
  "ds_read_b64_tr_b16 a[120:121], v51 offset:2560\n\t" \
  "v_add_u32 v62, 0x20, v33\n\t" \
  "s_mov_b32 exec_lo, 0xcccccccc\n\t" \
  "s_mov_b32 exec_hi, 0xcccccccc\n\t" \
  "v_add_u32 v62, 0x23e0, v33\n\t" \
  "s_mov_b64 exec, -1\n\t" \
  "v_mfma_f32_16x16x32_bf16 v[84:87], a[88:91], a[0:3], 0\n\t" \
  "ds_read_b64_tr_b16 a[122:123], v51 offset:3584\n\t" \
  "v_mfma_f32_16x16x32_bf16 v[80:83], a[76:79], a[4:7], v[80:83]\n\t" \
  "ds_read_b64_tr_b16 a[124:125], v51 offset:4096\n\t" \
  "buffer_load_dwordx4 a[160:163], v62, s[12:15], s51 offen sc1\n\t" \
  "v_mfma_f32_16x16x32_bf16 v[84:87], a[92:95], a[4:7], v[84:87]\n\t" \
  "s_waitcnt lgkmcnt(6)\n\t" \
  "ds_read_b64_tr_b16 a[126:127], v51 offset:5120\n\t" \
  "v_mfma_f32_16x16x32_bf16 v[80:83], a[80:83], a[8:11], v[80:83]\n\t" \
  "ds_read_b64_tr_b16 a[128:129], v51 offset:4608\n\t" \
  "v_mfma_f32_16x16x32_bf16 v[84:87], a[96:99], a[8:11], v[84:87]\n\t" \
  "ds_read_b64_tr_b16 a[130:131], v51 offset:5632\n\t" \
  "v_mfma_f32_16x16x32_bf16 v[80:83], a[84:87], a[12:15], v[80:83]\n\t" \
  "s_mov_b32 m0, s60\n\t" \
  "ds_read_b64_tr_b16 a[132:133], v51 offset:6144\n\t" \
  "v_mfma_f32_16x16x32_bf16 v[84:87], a[100:103], a[12:15], v[84:87]\n\t" \
  "buffer_load_dwordx4 v58, s[16:19], s96 offen offset:0 sc1 lds\n\t" \
  "v_mfma_f32_16x16x16_bf16 v[80:83], a[104:105], a[16:17], v[80:83]\n\t" \
  "s_add_u32 s96, s96, 9216\n\t" \
  "ds_read_b64_tr_b16 a[134:135], v51 offset:7168\n\t" \
  "v_mfma_f32_16x16x16_bf16 v[84:87], a[106:107], a[16:17], v[84:87]\n\t" \
  "s_cmp_eq_u32 s96, s73\n\t" \
  "ds_read_b64_tr_b16 a[136:137], v51 offset:6656\n\t" \
  "v_mfma_f32_16x16x32_bf16 v[88:91], a[72:75], a[18:21], 0\n\t" \
  "s_cselect_b32 s96, s72, s96\n\t" \
  "ds_read_b64_tr_b16 a[138:139], v51 offset:7680\n\t" \
  "v_mfma_f32_16x16x32_bf16 v[92:95], a[88:91], a[18:21], 0\n\t" \
  "ds_read_b64_tr_b16 a[140:141], v51 offset:8192\n\t" \
  "buffer_load_dwordx4 a[198:201], v62, s[12:15], s88 offen sc1\n\t" \
  "v_mfma_f32_16x16x32_bf16 v[88:91], a[76:79], a[22:25], v[88:91]\n\t" \
  "ds_read_b64_tr_b16 a[142:143], v51 offset:8704\n\t" \
  "v_mfma_f32_16x16x32_bf16 v[92:95], a[92:95], a[22:25], v[92:95]\n\t" \
  "v_exp_f32_e32 v80, v80\n\t" \
  "v_mfma_f32_16x16x32_bf16 v[88:91], a[80:83], a[26:29], v[88:91]\n\t" \
  "v_exp_f32_e32 v81, v81\n\t" \
  "v_mfma_f32_16x16x32_bf16 v[92:95], a[96:99], a[26:29], v[92:95]\n\t" \
  "s_waitcnt lgkmcnt(0)\n\t" \
  "s_mov_b32 m0, s52\n\t" \
  "v_mfma_f32_16x16x32_bf16 v[88:91], a[84:87], a[30:33], v[88:91]\n\t" \
  "buffer_load_dwordx4 v59, s[16:19], s96 offen offset:0 sc1 lds\n\t" \
  "v_mfma_f32_16x16x32_bf16 v[92:95], a[100:103], a[30:33], v[92:95]\n\t" \
  "v_exp_f32_e32 v82, v82\n\t" \
  "v_mfma_f32_16x16x16_bf16 v[88:91], a[104:105], a[34:35], v[88:91]\n\t" \
  "v_exp_f32_e32 v83, v83\n\t" \
  "v_mfma_f32_16x16x16_bf16 v[92:95], a[106:107], a[34:35], v[92:95]\n\t" \
  "v_cvt_pk_bf16_f32 v76, v80, v81\n\t" \
  "v_cvt_pk_bf16_f32 v77, v82, v83\n\t" \
  "v_mfma_f32_16x16x32_bf16 v[96:99], a[72:75], a[36:39], 0\n\t" \
  "v_exp_f32_e32 v84, v84\n\t" \
  "v_mfma_f32_16x16x32_bf16 v[100:103], a[88:91], a[36:39], 0\n\t" \
  "v_exp_f32_e32 v85, v85\n\t" \
  "v_mfma_f32_16x16x32_bf16 v[96:99], a[76:79], a[40:43], v[96:99]\n\t" \
  "v_exp_f32_e32 v86, v86\n\t" \
  "v_mfma_f32_16x16x32_bf16 v[100:103], a[92:95], a[40:43], v[100:103]\n\t" \
  "v_exp_f32_e32 v87, v87\n\t" \
  "v_mfma_f32_16x16x32_bf16 v[96:99], a[80:83], a[44:47], v[96:99]\n\t" \
  "buffer_load_dwordx4 v59, s[16:19], s96 offen offset:1024 sc1 lds\n\t" \
  "v_mfma_f32_16x16x32_bf16 v[100:103], a[96:99], a[44:47], v[100:103]\n\t" \
  "v_cvt_pk_bf16_f32 v78, v84, v85\n\t" \
  "v_cvt_pk_bf16_f32 v79, v86, v87\n\t" \
  "v_mfma_f32_16x16x32_bf16 v[96:99], a[84:87], a[48:51], v[96:99]\n\t" \
  "v_exp_f32_e32 v88, v88\n\t" \
  "v_mfma_f32_16x16x32_bf16 v[100:103], a[100:103], a[48:51], v[100:103]\n\t" \
  "v_exp_f32_e32 v89, v89\n\t" \
  "v_mfma_f32_16x16x16_bf16 v[96:99], a[104:105], a[52:53], v[96:99]\n\t" \
  "v_exp_f32_e32 v90, v90\n\t" \
  "v_mfma_f32_16x16x16_bf16 v[100:103], a[106:107], a[52:53], v[100:103]\n\t" \
  "v_exp_f32_e32 v91, v91\n\t" \
  "v_mfma_f32_16x16x32_bf16 v[104:107], a[72:75], a[54:57], 0\n\t" \
  "v_cvt_pk_bf16_f32 v72, v88, v89\n\t" \
  "v_cvt_pk_bf16_f32 v73, v90, v91\n\t" \
  "v_mfma_f32_16x16x32_bf16 v[108:111], a[88:91], a[54:57], 0\n\t" \
  "v_exp_f32_e32 v92, v92\n\t" \
  "v_mfma_f32_16x16x32_bf16 v[104:107], a[76:79], a[58:61], v[104:107]\n\t" \
  "v_exp_f32_e32 v93, v93\n\t" \
  "v_mfma_f32_16x16x32_bf16 v[108:111], a[92:95], a[58:61], v[108:111]\n\t" \
  "buffer_load_dwordx4 v59, s[16:19], s96 offen offset:2048 sc1 lds\n\t" \
  "v_mfma_f32_16x16x32_bf16 v[104:107], a[80:83], a[62:65], v[104:107]\n\t" \
  "v_exp_f32_e32 v94, v94\n\t" \
  "v_mfma_f32_16x16x32_bf16 v[108:111], a[96:99], a[62:65], v[108:111]\n\t" \
  "v_exp_f32_e32 v95, v95\n\t" \
  "v_mfma_f32_16x16x32_bf16 v[104:107], a[84:87], a[66:69], v[104:107]\n\t" \
  "v_cvt_pk_bf16_f32 v74, v92, v93\n\t" \
  "v_cvt_pk_bf16_f32 v75, v94, v95\n\t" \
  "v_mfma_f32_16x16x32_bf16 v[108:111], a[100:103], a[66:69], v[108:111]\n\t" \
  "v_exp_f32_e32 v96, v96\n\t" \
  "v_mfma_f32_16x16x16_bf16 v[104:107], a[104:105], a[70:71], v[104:107]\n\t" \
  "v_exp_f32_e32 v97, v97\n\t" \
  "v_mfma_f32_16x16x16_bf16 v[108:111], a[106:107], a[70:71], v[108:111]\n\t" \
  "v_exp_f32_e32 v98, v98\n\t" \
  "v_mfma_f32_16x16x32_bf16 v[112:115], a[108:111], v[76:79], v[112:115]\n\t" \
  "v_exp_f32_e32 v99, v99\n\t" \
  "v_mfma_f32_16x16x32_bf16 v[116:119], a[112:115], v[76:79], v[116:119]\n\t" \
  "buffer_load_dwordx4 v59, s[16:19], s96 offen offset:3072 sc1 lds\n\t" \
  "v_mfma_f32_16x16x32_bf16 v[120:123], a[116:119], v[76:79], v[120:123]\n\t" \
  "v_cvt_pk_bf16_f32 v68, v96, v97\n\t" \
  "v_cvt_pk_bf16_f32 v69, v98, v99\n\t" \
  "v_mfma_f32_16x16x32_bf16 v[124:127], a[120:123], v[76:79], v[124:127]\n\t" \
  "v_exp_f32_e32 v100, v100\n\t" \
  "v_mfma_f32_16x16x32_bf16 v[128:131], a[124:127], v[76:79], v[128:131]\n\t" \
  "v_exp_f32_e32 v101, v101\n\t" \
  "v_mfma_f32_16x16x32_bf16 v[132:135], a[128:131], v[76:79], v[132:135]\n\t" \
  "v_exp_f32_e32 v102, v102\n\t" \
  "v_mfma_f32_16x16x32_bf16 v[136:139], a[132:135], v[76:79], v[136:139]\n\t" \
  "v_exp_f32_e32 v103, v103\n\t" \
  "v_mfma_f32_16x16x32_bf16 v[140:143], a[136:139], v[76:79], v[140:143]\n\t" \
  "v_cvt_pk_bf16_f32 v70, v100, v101\n\t" \
  "v_cvt_pk_bf16_f32 v71, v102, v103\n\t" \
  "v_mfma_f32_16x16x32_bf16 v[144:147], a[140:143], v[76:79], v[144:147]\n\t" \
  "v_exp_f32_e32 v104, v104\n\t" \
  "v_mfma_f32_16x16x32_bf16 v[148:151], a[108:111], v[72:75], v[148:151]\n\t" \
  "s_mov_b32 m0, s53\n\t" \
  "s_waitcnt vmcnt(20)\n\t" \
  "v_mfma_f32_16x16x32_bf16 v[152:155], a[112:115], v[72:75], v[152:155]\n\t" \
  "buffer_load_dwordx4 v57, s[16:19], s96 offen offset:0 sc1 lds\n\t" \
  "v_mfma_f32_16x16x32_bf16 v[156:159], a[116:119], v[72:75], v[156:159]\n\t" \
  "v_exp_f32_e32 v105, v105\n\t" \
  "v_mfma_f32_16x16x32_bf16 v[160:163], a[120:123], v[72:75], v[160:163]\n\t" \
  "v_exp_f32_e32 v106, v106\n\t" \
  "v_mfma_f32_16x16x32_bf16 v[164:167], a[124:127], v[72:75], v[164:167]\n\t" \
  "v_exp_f32_e32 v107, v107\n\t" \
  "v_mfma_f32_16x16x32_bf16 v[168:171], a[128:131], v[72:75], v[168:171]\n\t" \
  "v_cvt_pk_bf16_f32 v64, v104, v105\n\t" \
  "v_cvt_pk_bf16_f32 v65, v106, v107\n\t" \
  "v_mfma_f32_16x16x32_bf16 v[172:175], a[132:135], v[72:75], v[172:175]\n\t" \
  "v_exp_f32_e32 v108, v108\n\t" \
  "v_mfma_f32_16x16x32_bf16 v[176:179], a[136:139], v[72:75], v[176:179]\n\t" \
  "v_exp_f32_e32 v109, v109\n\t" \
  "v_mfma_f32_16x16x32_bf16 v[180:183], a[140:143], v[72:75], v[180:183]\n\t" \
  "v_exp_f32_e32 v110, v110\n\t" \
  "v_mfma_f32_16x16x32_bf16 v[184:187], a[108:111], v[68:71], v[184:187]\n\t" \
  "buffer_load_dwordx4 v57, s[16:19], s96 offen offset:1024 sc1 lds\n\t" \
  "v_mfma_f32_16x16x32_bf16 v[188:191], a[112:115], v[68:71], v[188:191]\n\t" \
  "v_exp_f32_e32 v111, v111\n\t" \
  "v_mfma_f32_16x16x32_bf16 v[192:195], a[116:119], v[68:71], v[192:195]\n\t" \
  "v_cvt_pk_bf16_f32 v66, v108, v109\n\t" \
  "v_cvt_pk_bf16_f32 v67, v110, v111\n\t" \
  "v_mfma_f32_16x16x32_bf16 v[196:199], a[120:123], v[68:71], v[196:199]\n\t" \
  "ds_read_b128 a[72:75], v49 offset:0\n\t" \
  "v_mfma_f32_16x16x32_bf16 v[200:203], a[124:127], v[68:71], v[200:203]\n\t" \
  "ds_read_b128 a[88:91], v49 offset:1024\n\t" \
  "buffer_load_dwordx4 a[236:239], v62, s[12:15], s71 offen sc1\n\t" \
  "v_mfma_f32_16x16x32_bf16 v[204:207], a[128:131], v[68:71], v[204:207]\n\t" \
  "ds_read_b128 a[76:79], v49 offset:2048\n\t" \
  "v_mfma_f32_16x16x32_bf16 v[208:211], a[132:135], v[68:71], v[208:211]\n\t" \
  "ds_read_b128 a[92:95], v49 offset:3072\n\t" \
  "buffer_load_dwordx4 a[164:167], v59, s[16:19], s68 offen offset:0 sc1\n\t" \
  "v_mfma_f32_16x16x32_bf16 v[212:215], a[136:139], v[68:71], v[212:215]\n\t" \
  "ds_read_b128 a[80:83], v49 offset:4096\n\t" \
  "v_mfma_f32_16x16x32_bf16 v[216:219], a[140:143], v[68:71], v[216:219]\n\t" \
  "buffer_load_dwordx4 v57, s[16:19], s96 offen offset:2048 sc1 lds\n\t" \
  "v_mfma_f32_16x16x32_bf16 v[220:223], a[108:111], v[64:67], v[220:223]\n\t" \
  "ds_read_b128 a[96:99], v49 offset:5120\n\t" \
  "v_mfma_f32_16x16x32_bf16 v[224:227], a[112:115], v[64:67], v[224:227]\n\t" \
  "ds_read_b64_tr_b16 a[108:109], v52 offset:0\n\t" \
  "ds_read_b128 a[84:87], v49 offset:6144\n\t" \
  "v_mfma_f32_16x16x32_bf16 v[228:231], a[116:119], v[64:67], v[228:231]\n\t" \
  "ds_read_b64_tr_b16 a[110:111], v52 offset:1024\n\t" \
  "ds_read_b128 a[100:103], v49 offset:7168\n\t" \
  "v_mfma_f32_16x16x32_bf16 v[232:235], a[120:123], v[64:67], v[232:235]\n\t" \
  "ds_read_b64_tr_b16 a[112:113], v52 offset:512\n\t" \
  "ds_read2st64_b64 a[104:107], v55 offset0:16 offset1:17\n\t" \
  "buffer_load_dwordx4 a[168:171], v59, s[16:19], s68 offen offset:1024 sc1\n\t" \
  "v_mfma_f32_16x16x32_bf16 v[236:239], a[124:127], v[64:67], v[236:239]\n\t" \
  "ds_read_b64_tr_b16 a[114:115], v52 offset:1536\n\t" \
  "v_mfma_f32_16x16x32_bf16 v[240:243], a[128:131], v[64:67], v[240:243]\n\t" \
  "ds_read_b64_tr_b16 a[116:117], v52 offset:2048\n\t" \
  "buffer_load_dwordx4 a[172:175], v59, s[16:19], s68 offen offset:2048 sc1\n\t" \
  "v_mfma_f32_16x16x32_bf16 v[244:247], a[132:135], v[64:67], v[244:247]\n\t" \
  "ds_read_b64_tr_b16 a[118:119], v52 offset:3072\n\t" \
  "v_mfma_f32_16x16x32_bf16 v[248:251], a[136:139], v[64:67], v[248:251]\n\t" \
  "buffer_load_dwordx4 v57, s[16:19], s96 offen offset:3072 sc1 lds\n\t" \
  "v_mfma_f32_16x16x32_bf16 v[252:255], a[140:143], v[64:67], v[252:255]\n\t" \
  "s_waitcnt lgkmcnt(11)\n\t" \
  "v_mfma_f32_16x16x32_bf16 v[80:83], a[72:75], a[0:3], 0\n\t" \
  "ds_read_b64_tr_b16 a[120:121], v52 offset:2560\n\t" \
  "v_mfma_f32_16x16x32_bf16 v[84:87], a[88:91], a[0:3], 0\n\t" \
  "ds_read_b64_tr_b16 a[122:123], v52 offset:3584\n\t" \
  "v_mfma_f32_16x16x32_bf16 v[80:83], a[76:79], a[4:7], v[80:83]\n\t" \
  "ds_read_b64_tr_b16 a[124:125], v52 offset:4096\n\t" \
  "buffer_load_dwordx4 a[176:179], v59, s[16:19], s68 offen offset:3072 sc1\n\t" \
  "v_mfma_f32_16x16x32_bf16 v[84:87], a[92:95], a[4:7], v[84:87]\n\t" \
  "s_waitcnt lgkmcnt(6)\n\t" \
  "ds_read_b64_tr_b16 a[126:127], v52 offset:5120\n\t" \
  "v_mfma_f32_16x16x32_bf16 v[80:83], a[80:83], a[8:11], v[80:83]\n\t" \
  "ds_read_b64_tr_b16 a[128:129], v52 offset:4608\n\t" \
  "v_mfma_f32_16x16x32_bf16 v[84:87], a[96:99], a[8:11], v[84:87]\n\t" \
  "ds_read_b64_tr_b16 a[130:131], v52 offset:5632\n\t" \
  "v_mfma_f32_16x16x32_bf16 v[80:83], a[84:87], a[12:15], v[80:83]\n\t" \
  "s_mov_b32 m0, s54\n\t" \
  "ds_read_b64_tr_b16 a[132:133], v52 offset:6144\n\t" \
  "v_mfma_f32_16x16x32_bf16 v[84:87], a[100:103], a[12:15], v[84:87]\n\t" \
  "buffer_load_dwordx4 v58, s[16:19], s96 offen offset:0 sc1 lds\n\t" \
  "v_mfma_f32_16x16x16_bf16 v[80:83], a[104:105], a[16:17], v[80:83]\n\t" \
  "s_add_u32 s96, s96, 9216\n\t" \
  "ds_read_b64_tr_b16 a[134:135], v52 offset:7168\n\t" \
  "v_mfma_f32_16x16x16_bf16 v[84:87], a[106:107], a[16:17], v[84:87]\n\t" \
  "s_cmp_eq_u32 s96, s73\n\t" \
  "ds_read_b64_tr_b16 a[136:137], v52 offset:6656\n\t" \
  "v_mfma_f32_16x16x32_bf16 v[88:91], a[72:75], a[18:21], 0\n\t" \
  "s_cselect_b32 s96, s72, s96\n\t" \
  "ds_read_b64_tr_b16 a[138:139], v52 offset:7680\n\t" \
  "s_add_u32 s99, s68, 4096\n\t" \
  "v_mfma_f32_16x16x32_bf16 v[92:95], a[88:91], a[18:21], 0\n\t" \
  "ds_read_b64_tr_b16 a[140:141], v52 offset:8192\n\t" \
  "buffer_load_dwordx2 a[180:181], v63, s[16:19], s99 offen sc1\n\t" \
  "v_mfma_f32_16x16x32_bf16 v[88:91], a[76:79], a[22:25], v[88:91]\n\t" \
  "ds_read_b64_tr_b16 a[142:143], v52 offset:8704\n\t" \
  "v_mfma_f32_16x16x32_bf16 v[92:95], a[92:95], a[22:25], v[92:95]\n\t" \
  "v_exp_f32_e32 v80, v80\n\t" \
  "v_mfma_f32_16x16x32_bf16 v[88:91], a[80:83], a[26:29], v[88:91]\n\t" \
  "v_exp_f32_e32 v81, v81\n\t" \
  "v_mfma_f32_16x16x32_bf16 v[92:95], a[96:99], a[26:29], v[92:95]\n\t" \
  "s_waitcnt lgkmcnt(0)\n\t" \
  "s_mov_b32 m0, s55\n\t" \
  "v_mfma_f32_16x16x32_bf16 v[88:91], a[84:87], a[30:33], v[88:91]\n\t" \
  "buffer_load_dwordx4 v59, s[16:19], s96 offen offset:0 sc1 lds\n\t" \
  "v_mfma_f32_16x16x32_bf16 v[92:95], a[100:103], a[30:33], v[92:95]\n\t" \
  "v_exp_f32_e32 v82, v82\n\t" \
  "v_mfma_f32_16x16x16_bf16 v[88:91], a[104:105], a[34:35], v[88:91]\n\t" \
  "v_exp_f32_e32 v83, v83\n\t" \
  "v_mfma_f32_16x16x16_bf16 v[92:95], a[106:107], a[34:35], v[92:95]\n\t" \
  "v_cvt_pk_bf16_f32 v76, v80, v81\n\t" \
  "v_cvt_pk_bf16_f32 v77, v82, v83\n\t" \
  "v_mfma_f32_16x16x32_bf16 v[96:99], a[72:75], a[36:39], 0\n\t" \
  "v_exp_f32_e32 v84, v84\n\t" \
  "v_mfma_f32_16x16x32_bf16 v[100:103], a[88:91], a[36:39], 0\n\t" \
  "v_exp_f32_e32 v85, v85\n\t" \
  "v_mfma_f32_16x16x32_bf16 v[96:99], a[76:79], a[40:43], v[96:99]\n\t" \
  "v_exp_f32_e32 v86, v86\n\t" \
  "v_mfma_f32_16x16x32_bf16 v[100:103], a[92:95], a[40:43], v[100:103]\n\t" \
  "v_exp_f32_e32 v87, v87\n\t" \
  "v_mfma_f32_16x16x32_bf16 v[96:99], a[80:83], a[44:47], v[96:99]\n\t" \
  "buffer_load_dwordx4 v59, s[16:19], s96 offen offset:1024 sc1 lds\n\t" \
  "v_mfma_f32_16x16x32_bf16 v[100:103], a[96:99], a[44:47], v[100:103]\n\t" \
  "v_cvt_pk_bf16_f32 v78, v84, v85\n\t" \
  "v_cvt_pk_bf16_f32 v79, v86, v87\n\t" \
  "v_mfma_f32_16x16x32_bf16 v[96:99], a[84:87], a[48:51], v[96:99]\n\t" \
  "v_exp_f32_e32 v88, v88\n\t" \
  "v_mfma_f32_16x16x32_bf16 v[100:103], a[100:103], a[48:51], v[100:103]\n\t" \
  "v_exp_f32_e32 v89, v89\n\t" \
  "v_mfma_f32_16x16x16_bf16 v[96:99], a[104:105], a[52:53], v[96:99]\n\t" \
  "v_exp_f32_e32 v90, v90\n\t" \
  "v_mfma_f32_16x16x16_bf16 v[100:103], a[106:107], a[52:53], v[100:103]\n\t" \
  "v_exp_f32_e32 v91, v91\n\t" \
  "v_mfma_f32_16x16x32_bf16 v[104:107], a[72:75], a[54:57], 0\n\t" \
  "v_cvt_pk_bf16_f32 v72, v88, v89\n\t" \
  "v_cvt_pk_bf16_f32 v73, v90, v91\n\t" \
  "v_mfma_f32_16x16x32_bf16 v[108:111], a[88:91], a[54:57], 0\n\t" \
  "v_exp_f32_e32 v92, v92\n\t" \
  "v_mfma_f32_16x16x32_bf16 v[104:107], a[76:79], a[58:61], v[104:107]\n\t" \
  "v_exp_f32_e32 v93, v93\n\t" \
  "v_mfma_f32_16x16x32_bf16 v[108:111], a[92:95], a[58:61], v[108:111]\n\t" \
  "buffer_load_dwordx4 v59, s[16:19], s96 offen offset:2048 sc1 lds\n\t" \
  "v_mfma_f32_16x16x32_bf16 v[104:107], a[80:83], a[62:65], v[104:107]\n\t" \
  "v_exp_f32_e32 v94, v94\n\t" \
  "v_mfma_f32_16x16x32_bf16 v[108:111], a[96:99], a[62:65], v[108:111]\n\t" \
  "v_exp_f32_e32 v95, v95\n\t" \
  "v_mfma_f32_16x16x32_bf16 v[104:107], a[84:87], a[66:69], v[104:107]\n\t" \
  "v_cvt_pk_bf16_f32 v74, v92, v93\n\t" \
  "v_cvt_pk_bf16_f32 v75, v94, v95\n\t" \
  "v_mfma_f32_16x16x32_bf16 v[108:111], a[100:103], a[66:69], v[108:111]\n\t" \
  "v_exp_f32_e32 v96, v96\n\t" \
  "v_mfma_f32_16x16x16_bf16 v[104:107], a[104:105], a[70:71], v[104:107]\n\t" \
  "v_exp_f32_e32 v97, v97\n\t" \
  "v_mfma_f32_16x16x16_bf16 v[108:111], a[106:107], a[70:71], v[108:111]\n\t" \
  "v_exp_f32_e32 v98, v98\n\t" \
  "v_mfma_f32_16x16x32_bf16 v[112:115], a[108:111], v[76:79], v[112:115]\n\t" \
  "v_exp_f32_e32 v99, v99\n\t" \
  "v_mfma_f32_16x16x32_bf16 v[116:119], a[112:115], v[76:79], v[116:119]\n\t" \
  "buffer_load_dwordx4 v59, s[16:19], s96 offen offset:3072 sc1 lds\n\t" \
  "v_mfma_f32_16x16x32_bf16 v[120:123], a[116:119], v[76:79], v[120:123]\n\t" \
  "v_cvt_pk_bf16_f32 v68, v96, v97\n\t" \
  "v_cvt_pk_bf16_f32 v69, v98, v99\n\t" \
  "v_mfma_f32_16x16x32_bf16 v[124:127], a[120:123], v[76:79], v[124:127]\n\t" \
  "v_exp_f32_e32 v100, v100\n\t" \
  "v_mfma_f32_16x16x32_bf16 v[128:131], a[124:127], v[76:79], v[128:131]\n\t" \
  "v_exp_f32_e32 v101, v101\n\t" \
  "v_mfma_f32_16x16x32_bf16 v[132:135], a[128:131], v[76:79], v[132:135]\n\t" \
  "v_exp_f32_e32 v102, v102\n\t" \
  "v_mfma_f32_16x16x32_bf16 v[136:139], a[132:135], v[76:79], v[136:139]\n\t" \
  "v_exp_f32_e32 v103, v103\n\t" \
  "v_mfma_f32_16x16x32_bf16 v[140:143], a[136:139], v[76:79], v[140:143]\n\t" \
  "v_cvt_pk_bf16_f32 v70, v100, v101\n\t" \
  "v_cvt_pk_bf16_f32 v71, v102, v103\n\t" \
  "v_mfma_f32_16x16x32_bf16 v[144:147], a[140:143], v[76:79], v[144:147]\n\t" \
  "v_exp_f32_e32 v104, v104\n\t" \
  "v_mfma_f32_16x16x32_bf16 v[148:151], a[108:111], v[72:75], v[148:151]\n\t" \
  "s_mov_b32 m0, s56\n\t" \
  "s_waitcnt vmcnt(20)\n\t" \
  "v_mfma_f32_16x16x32_bf16 v[152:155], a[112:115], v[72:75], v[152:155]\n\t" \
  "buffer_load_dwordx4 v57, s[16:19], s96 offen offset:0 sc1 lds\n\t" \
  "v_mfma_f32_16x16x32_bf16 v[156:159], a[116:119], v[72:75], v[156:159]\n\t" \
  "v_exp_f32_e32 v105, v105\n\t" \
  "v_mfma_f32_16x16x32_bf16 v[160:163], a[120:123], v[72:75], v[160:163]\n\t" \
  "v_exp_f32_e32 v106, v106\n\t" \
  "v_mfma_f32_16x16x32_bf16 v[164:167], a[124:127], v[72:75], v[164:167]\n\t" \
  "v_exp_f32_e32 v107, v107\n\t" \
  "v_mfma_f32_16x16x32_bf16 v[168:171], a[128:131], v[72:75], v[168:171]\n\t" \
  "v_cvt_pk_bf16_f32 v64, v104, v105\n\t" \
  "v_cvt_pk_bf16_f32 v65, v106, v107\n\t" \
  "v_mfma_f32_16x16x32_bf16 v[172:175], a[132:135], v[72:75], v[172:175]\n\t" \
  "v_exp_f32_e32 v108, v108\n\t" \
  "v_mfma_f32_16x16x32_bf16 v[176:179], a[136:139], v[72:75], v[176:179]\n\t" \
  "v_exp_f32_e32 v109, v109\n\t" \
  "v_mfma_f32_16x16x32_bf16 v[180:183], a[140:143], v[72:75], v[180:183]\n\t" \
  "v_exp_f32_e32 v110, v110\n\t" \
  "v_mfma_f32_16x16x32_bf16 v[184:187], a[108:111], v[68:71], v[184:187]\n\t" \
  "buffer_load_dwordx4 v57, s[16:19], s96 offen offset:1024 sc1 lds\n\t" \
  "v_mfma_f32_16x16x32_bf16 v[188:191], a[112:115], v[68:71], v[188:191]\n\t" \
  "v_exp_f32_e32 v111, v111\n\t" \
  "v_mfma_f32_16x16x32_bf16 v[192:195], a[116:119], v[68:71], v[192:195]\n\t" \
  "v_cvt_pk_bf16_f32 v66, v108, v109\n\t" \
  "v_cvt_pk_bf16_f32 v67, v110, v111\n\t" \
  "v_mfma_f32_16x16x32_bf16 v[196:199], a[120:123], v[68:71], v[196:199]\n\t" \
  "ds_read_b128 a[72:75], v50 offset:0\n\t" \
  "v_mfma_f32_16x16x32_bf16 v[200:203], a[124:127], v[68:71], v[200:203]\n\t" \
  "ds_read_b128 a[88:91], v50 offset:1024\n\t" \
  "buffer_load_dwordx4 a[202:205], v59, s[16:19], s69 offen offset:0 sc1\n\t" \
  "v_mfma_f32_16x16x32_bf16 v[204:207], a[128:131], v[68:71], v[204:207]\n\t" \
  "ds_read_b128 a[76:79], v50 offset:2048\n\t" \
  "v_mfma_f32_16x16x32_bf16 v[208:211], a[132:135], v[68:71], v[208:211]\n\t" \
  "ds_read_b128 a[92:95], v50 offset:3072\n\t" \
  "buffer_load_dwordx4 a[206:209], v59, s[16:19], s69 offen offset:1024 sc1\n\t" \
  "v_mfma_f32_16x16x32_bf16 v[212:215], a[136:139], v[68:71], v[212:215]\n\t" \
  "ds_read_b128 a[80:83], v50 offset:4096\n\t" \
  "v_mfma_f32_16x16x32_bf16 v[216:219], a[140:143], v[68:71], v[216:219]\n\t" \
  "buffer_load_dwordx4 v57, s[16:19], s96 offen offset:2048 sc1 lds\n\t" \
  "v_mfma_f32_16x16x32_bf16 v[220:223], a[108:111], v[64:67], v[220:223]\n\t" \
  "ds_read_b128 a[96:99], v50 offset:5120\n\t" \
  "v_mfma_f32_16x16x32_bf16 v[224:227], a[112:115], v[64:67], v[224:227]\n\t" \
  "ds_read_b64_tr_b16 a[108:109], v53 offset:0\n\t" \
  "ds_read_b128 a[84:87], v50 offset:6144\n\t" \
  "v_mfma_f32_16x16x32_bf16 v[228:231], a[116:119], v[64:67], v[228:231]\n\t" \
  "ds_read_b64_tr_b16 a[110:111], v53 offset:1024\n\t" \
  "ds_read_b128 a[100:103], v50 offset:7168\n\t" \
  "v_mfma_f32_16x16x32_bf16 v[232:235], a[120:123], v[64:67], v[232:235]\n\t" \
  "ds_read_b64_tr_b16 a[112:113], v53 offset:512\n\t" \
  "ds_read2st64_b64 a[104:107], v56 offset0:16 offset1:17\n\t" \
  "buffer_load_dwordx4 a[210:213], v59, s[16:19], s69 offen offset:2048 sc1\n\t" \
  "v_mfma_f32_16x16x32_bf16 v[236:239], a[124:127], v[64:67], v[236:239]\n\t" \
  "ds_read_b64_tr_b16 a[114:115], v53 offset:1536\n\t" \
  "v_mfma_f32_16x16x32_bf16 v[240:243], a[128:131], v[64:67], v[240:243]\n\t" \
  "ds_read_b64_tr_b16 a[116:117], v53 offset:2048\n\t" \
  "buffer_load_dwordx4 a[214:217], v59, s[16:19], s69 offen offset:3072 sc1\n\t" \
  "v_mfma_f32_16x16x32_bf16 v[244:247], a[132:135], v[64:67], v[244:247]\n\t" \
  "ds_read_b64_tr_b16 a[118:119], v53 offset:3072\n\t" \
  "v_mfma_f32_16x16x32_bf16 v[248:251], a[136:139], v[64:67], v[248:251]\n\t" \
  "buffer_load_dwordx4 v57, s[16:19], s96 offen offset:3072 sc1 lds\n\t" \
  "v_mfma_f32_16x16x32_bf16 v[252:255], a[140:143], v[64:67], v[252:255]\n\t" \
  "s_waitcnt lgkmcnt(11)\n\t" \
  "v_mfma_f32_16x16x32_bf16 v[80:83], a[72:75], a[0:3], 0\n\t" \
  "ds_read_b64_tr_b16 a[120:121], v53 offset:2560\n\t" \
  "v_mfma_f32_16x16x32_bf16 v[84:87], a[88:91], a[0:3], 0\n\t" \
  "ds_read_b64_tr_b16 a[122:123], v53 offset:3584\n\t" \
  "s_add_u32 s99, s69, 4096\n\t" \
  "v_mfma_f32_16x16x32_bf16 v[80:83], a[76:79], a[4:7], v[80:83]\n\t" \
  "ds_read_b64_tr_b16 a[124:125], v53 offset:4096\n\t" \
  "buffer_load_dwordx2 a[218:219], v63, s[16:19], s99 offen sc1\n\t" \
  "v_mfma_f32_16x16x32_bf16 v[84:87], a[92:95], a[4:7], v[84:87]\n\t" \
  "s_waitcnt lgkmcnt(6)\n\t" \
  "ds_read_b64_tr_b16 a[126:127], v53 offset:5120\n\t" \
  "v_mfma_f32_16x16x32_bf16 v[80:83], a[80:83], a[8:11], v[80:83]\n\t" \
  "ds_read_b64_tr_b16 a[128:129], v53 offset:4608\n\t" \
  "v_mfma_f32_16x16x32_bf16 v[84:87], a[96:99], a[8:11], v[84:87]\n\t" \
  "ds_read_b64_tr_b16 a[130:131], v53 offset:5632\n\t" \
  "v_mfma_f32_16x16x32_bf16 v[80:83], a[84:87], a[12:15], v[80:83]\n\t" \
  "s_mov_b32 m0, s57\n\t" \
  "ds_read_b64_tr_b16 a[132:133], v53 offset:6144\n\t" \
  "v_mfma_f32_16x16x32_bf16 v[84:87], a[100:103], a[12:15], v[84:87]\n\t" \
  "buffer_load_dwordx4 v58, s[16:19], s96 offen offset:0 sc1 lds\n\t" \
  "v_mfma_f32_16x16x16_bf16 v[80:83], a[104:105], a[16:17], v[80:83]\n\t" \
  "s_add_u32 s96, s27, s48\n\t" \
  "ds_read_b64_tr_b16 a[134:135], v53 offset:7168\n\t" \
  "v_mfma_f32_16x16x16_bf16 v[84:87], a[106:107], a[16:17], v[84:87]\n\t" \
  "ds_read_b64_tr_b16 a[136:137], v53 offset:6656\n\t" \
  "v_mfma_f32_16x16x32_bf16 v[88:91], a[72:75], a[18:21], 0\n\t" \
  "ds_read_b64_tr_b16 a[138:139], v53 offset:7680\n\t" \
  "v_mfma_f32_16x16x32_bf16 v[92:95], a[88:91], a[18:21], 0\n\t" \
  "ds_read_b64_tr_b16 a[140:141], v53 offset:8192\n\t" \
  "buffer_load_dwordx4 a[240:243], v59, s[16:19], s70 offen offset:0 sc1\n\t" \
  "v_mfma_f32_16x16x32_bf16 v[88:91], a[76:79], a[22:25], v[88:91]\n\t" \
  "ds_read_b64_tr_b16 a[142:143], v53 offset:8704\n\t" \
  "v_mfma_f32_16x16x32_bf16 v[92:95], a[92:95], a[22:25], v[92:95]\n\t" \
  "v_exp_f32_e32 v80, v80\n\t" \
  "v_mfma_f32_16x16x32_bf16 v[88:91], a[80:83], a[26:29], v[88:91]\n\t" \
  "v_exp_f32_e32 v81, v81\n\t" \
  "v_mfma_f32_16x16x32_bf16 v[92:95], a[96:99], a[26:29], v[92:95]\n\t" \
  "s_waitcnt lgkmcnt(0)\n\t" \
  "s_mov_b32 m0, s58\n\t" \
  "v_mfma_f32_16x16x32_bf16 v[88:91], a[84:87], a[30:33], v[88:91]\n\t" \
  "buffer_load_dwordx4 v59, s[16:19], s96 offen offset:0 sc1 lds\n\t" \
  "v_mfma_f32_16x16x32_bf16 v[92:95], a[100:103], a[30:33], v[92:95]\n\t" \
  "v_exp_f32_e32 v82, v82\n\t" \
  "v_mfma_f32_16x16x16_bf16 v[88:91], a[104:105], a[34:35], v[88:91]\n\t" \
  "v_exp_f32_e32 v83, v83\n\t" \
  "v_mfma_f32_16x16x16_bf16 v[92:95], a[106:107], a[34:35], v[92:95]\n\t" \
  "v_cvt_pk_bf16_f32 v76, v80, v81\n\t" \
  "v_cvt_pk_bf16_f32 v77, v82, v83\n\t" \
  "v_mfma_f32_16x16x32_bf16 v[96:99], a[72:75], a[36:39], 0\n\t" \
  "v_exp_f32_e32 v84, v84\n\t" \
  "v_mfma_f32_16x16x32_bf16 v[100:103], a[88:91], a[36:39], 0\n\t" \
  "v_exp_f32_e32 v85, v85\n\t" \
  "v_mfma_f32_16x16x32_bf16 v[96:99], a[76:79], a[40:43], v[96:99]\n\t" \
  "v_exp_f32_e32 v86, v86\n\t" \
  "v_mfma_f32_16x16x32_bf16 v[100:103], a[92:95], a[40:43], v[100:103]\n\t" \
  "v_exp_f32_e32 v87, v87\n\t" \
  "v_mfma_f32_16x16x32_bf16 v[96:99], a[80:83], a[44:47], v[96:99]\n\t" \
  "buffer_load_dwordx4 v59, s[16:19], s96 offen offset:1024 sc1 lds\n\t" \
  "v_mfma_f32_16x16x32_bf16 v[100:103], a[96:99], a[44:47], v[100:103]\n\t" \
  "v_cvt_pk_bf16_f32 v78, v84, v85\n\t" \
  "v_cvt_pk_bf16_f32 v79, v86, v87\n\t" \
  "v_mfma_f32_16x16x32_bf16 v[96:99], a[84:87], a[48:51], v[96:99]\n\t" \
  "v_exp_f32_e32 v88, v88\n\t" \
  "v_mfma_f32_16x16x32_bf16 v[100:103], a[100:103], a[48:51], v[100:103]\n\t" \
  "v_exp_f32_e32 v89, v89\n\t" \
  "v_mfma_f32_16x16x16_bf16 v[96:99], a[104:105], a[52:53], v[96:99]\n\t" \
  "v_exp_f32_e32 v90, v90\n\t" \
  "v_mfma_f32_16x16x16_bf16 v[100:103], a[106:107], a[52:53], v[100:103]\n\t" \
  "v_exp_f32_e32 v91, v91\n\t" \
  "v_mfma_f32_16x16x32_bf16 v[104:107], a[72:75], a[54:57], 0\n\t" \
  "v_cvt_pk_bf16_f32 v72, v88, v89\n\t" \
  "v_cvt_pk_bf16_f32 v73, v90, v91\n\t" \
  "v_mfma_f32_16x16x32_bf16 v[108:111], a[88:91], a[54:57], 0\n\t" \
  "v_exp_f32_e32 v92, v92\n\t" \
  "v_mfma_f32_16x16x32_bf16 v[104:107], a[76:79], a[58:61], v[104:107]\n\t" \
  "v_exp_f32_e32 v93, v93\n\t" \
  "v_mfma_f32_16x16x32_bf16 v[108:111], a[92:95], a[58:61], v[108:111]\n\t" \
  "buffer_load_dwordx4 v59, s[16:19], s96 offen offset:2048 sc1 lds\n\t" \
  "v_mfma_f32_16x16x32_bf16 v[104:107], a[80:83], a[62:65], v[104:107]\n\t" \
  "v_exp_f32_e32 v94, v94\n\t" \
  "v_mfma_f32_16x16x32_bf16 v[108:111], a[96:99], a[62:65], v[108:111]\n\t" \
  "v_exp_f32_e32 v95, v95\n\t" \
  "v_mfma_f32_16x16x32_bf16 v[104:107], a[84:87], a[66:69], v[104:107]\n\t" \
  "v_cvt_pk_bf16_f32 v74, v92, v93\n\t" \
  "v_cvt_pk_bf16_f32 v75, v94, v95\n\t" \
  "v_mfma_f32_16x16x32_bf16 v[108:111], a[100:103], a[66:69], v[108:111]\n\t" \
  "v_exp_f32_e32 v96, v96\n\t" \
  "v_mfma_f32_16x16x16_bf16 v[104:107], a[104:105], a[70:71], v[104:107]\n\t" \
  "v_exp_f32_e32 v97, v97\n\t" \
  "v_mfma_f32_16x16x16_bf16 v[108:111], a[106:107], a[70:71], v[108:111]\n\t" \
  "v_exp_f32_e32 v98, v98\n\t" \
  "v_mfma_f32_16x16x32_bf16 v[112:115], a[108:111], v[76:79], v[112:115]\n\t" \
  "v_exp_f32_e32 v99, v99\n\t" \
  "v_mfma_f32_16x16x32_bf16 v[116:119], a[112:115], v[76:79], v[116:119]\n\t" \
  "buffer_load_dwordx4 v59, s[16:19], s96 offen offset:3072 sc1 lds\n\t" \
  "v_mfma_f32_16x16x32_bf16 v[120:123], a[116:119], v[76:79], v[120:123]\n\t" \
  "v_cvt_pk_bf16_f32 v68, v96, v97\n\t" \
  "v_cvt_pk_bf16_f32 v69, v98, v99\n\t" \
  "v_mfma_f32_16x16x32_bf16 v[124:127], a[120:123], v[76:79], v[124:127]\n\t" \
  "v_exp_f32_e32 v100, v100\n\t" \
  "v_mfma_f32_16x16x32_bf16 v[128:131], a[124:127], v[76:79], v[128:131]\n\t" \
  "v_exp_f32_e32 v101, v101\n\t" \
  "v_mfma_f32_16x16x32_bf16 v[132:135], a[128:131], v[76:79], v[132:135]\n\t" \
  "v_exp_f32_e32 v102, v102\n\t" \
  "v_mfma_f32_16x16x32_bf16 v[136:139], a[132:135], v[76:79], v[136:139]\n\t" \
  "v_exp_f32_e32 v103, v103\n\t" \
  "v_mfma_f32_16x16x32_bf16 v[140:143], a[136:139], v[76:79], v[140:143]\n\t" \
  "v_cvt_pk_bf16_f32 v70, v100, v101\n\t" \
  "v_cvt_pk_bf16_f32 v71, v102, v103\n\t" \
  "v_mfma_f32_16x16x32_bf16 v[144:147], a[140:143], v[76:79], v[144:147]\n\t" \
  "v_exp_f32_e32 v104, v104\n\t" \
  "v_mfma_f32_16x16x32_bf16 v[148:151], a[108:111], v[72:75], v[148:151]\n\t" \
  "s_mov_b32 m0, s59\n\t" \
  "s_waitcnt vmcnt(20)\n\t" \
  "v_mfma_f32_16x16x32_bf16 v[152:155], a[112:115], v[72:75], v[152:155]\n\t" \
  "buffer_load_dwordx4 v57, s[16:19], s96 offen offset:0 sc1 lds\n\t" \
  "v_mfma_f32_16x16x32_bf16 v[156:159], a[116:119], v[72:75], v[156:159]\n\t" \
  "v_exp_f32_e32 v105, v105\n\t" \
  "v_mfma_f32_16x16x32_bf16 v[160:163], a[120:123], v[72:75], v[160:163]\n\t" \
  "v_exp_f32_e32 v106, v106\n\t" \
  "v_mfma_f32_16x16x32_bf16 v[164:167], a[124:127], v[72:75], v[164:167]\n\t" \
  "v_exp_f32_e32 v107, v107\n\t" \
  "v_mfma_f32_16x16x32_bf16 v[168:171], a[128:131], v[72:75], v[168:171]\n\t" \
  "v_cvt_pk_bf16_f32 v64, v104, v105\n\t" \
  "v_cvt_pk_bf16_f32 v65, v106, v107\n\t" \
  "v_mfma_f32_16x16x32_bf16 v[172:175], a[132:135], v[72:75], v[172:175]\n\t" \
  "v_exp_f32_e32 v108, v108\n\t" \
  "v_mfma_f32_16x16x32_bf16 v[176:179], a[136:139], v[72:75], v[176:179]\n\t" \
  "v_exp_f32_e32 v109, v109\n\t" \
  "v_mfma_f32_16x16x32_bf16 v[180:183], a[140:143], v[72:75], v[180:183]\n\t" \
  "v_exp_f32_e32 v110, v110\n\t" \
  "v_mfma_f32_16x16x32_bf16 v[184:187], a[108:111], v[68:71], v[184:187]\n\t" \
  "buffer_load_dwordx4 v57, s[16:19], s96 offen offset:1024 sc1 lds\n\t" \
  "v_mfma_f32_16x16x32_bf16 v[188:191], a[112:115], v[68:71], v[188:191]\n\t" \
  "v_exp_f32_e32 v111, v111\n\t" \
  "v_mfma_f32_16x16x32_bf16 v[192:195], a[116:119], v[68:71], v[192:195]\n\t" \
  "v_cvt_pk_bf16_f32 v66, v108, v109\n\t" \
  "v_cvt_pk_bf16_f32 v67, v110, v111\n\t" \
  "v_mfma_f32_16x16x32_bf16 v[196:199], a[120:123], v[68:71], v[196:199]\n\t" \
  "ds_read_b128 a[72:75], v48 offset:0\n\t" \
  "v_mfma_f32_16x16x32_bf16 v[200:203], a[124:127], v[68:71], v[200:203]\n\t" \
  "ds_read_b128 a[88:91], v48 offset:1024\n\t" \
  "buffer_load_dwordx4 a[244:247], v59, s[16:19], s70 offen offset:1024 sc1\n\t" \
  "v_mfma_f32_16x16x32_bf16 v[204:207], a[128:131], v[68:71], v[204:207]\n\t" \
  "ds_read_b128 a[76:79], v48 offset:2048\n\t" \
  "v_mfma_f32_16x16x32_bf16 v[208:211], a[132:135], v[68:71], v[208:211]\n\t" \
  "ds_read_b128 a[92:95], v48 offset:3072\n\t" \
  "buffer_load_dwordx4 a[248:251], v59, s[16:19], s70 offen offset:2048 sc1\n\t" \
  "v_mfma_f32_16x16x32_bf16 v[212:215], a[136:139], v[68:71], v[212:215]\n\t" \
  "ds_read_b128 a[80:83], v48 offset:4096\n\t" \
  "v_mfma_f32_16x16x32_bf16 v[216:219], a[140:143], v[68:71], v[216:219]\n\t" \
  "buffer_load_dwordx4 v57, s[16:19], s96 offen offset:2048 sc1 lds\n\t" \
  "v_mfma_f32_16x16x32_bf16 v[220:223], a[108:111], v[64:67], v[220:223]\n\t" \
  "ds_read_b128 a[96:99], v48 offset:5120\n\t" \
  "v_mfma_f32_16x16x32_bf16 v[224:227], a[112:115], v[64:67], v[224:227]\n\t" \
  "ds_read_b64_tr_b16 a[108:109], v51 offset:0\n\t" \
  "ds_read_b128 a[84:87], v48 offset:6144\n\t" \
  "v_mfma_f32_16x16x32_bf16 v[228:231], a[116:119], v[64:67], v[228:231]\n\t" \
  "ds_read_b64_tr_b16 a[110:111], v51 offset:1024\n\t" \
  "ds_read_b128 a[100:103], v48 offset:7168\n\t" \
  "v_mfma_f32_16x16x32_bf16 v[232:235], a[120:123], v[64:67], v[232:235]\n\t" \
  "ds_read_b64_tr_b16 a[112:113], v51 offset:512\n\t" \
  "ds_read2st64_b64 a[104:107], v54 offset0:16 offset1:17\n\t" \
  "buffer_load_dwordx4 a[252:255], v59, s[16:19], s70 offen offset:3072 sc1\n\t" \
  "v_mfma_f32_16x16x32_bf16 v[236:239], a[124:127], v[64:67], v[236:239]\n\t" \
  "ds_read_b64_tr_b16 a[114:115], v51 offset:1536\n\t" \
  "s_add_u32 s99, s70, 4096\n\t" \
  "v_mfma_f32_16x16x32_bf16 v[240:243], a[128:131], v[64:67], v[240:243]\n\t" \
  "ds_read_b64_tr_b16 a[116:117], v51 offset:2048\n\t" \
  "buffer_load_dwordx2 v[46:47], v63, s[16:19], s99 offen sc1\n\t" \
  "v_mfma_f32_16x16x32_bf16 v[244:247], a[132:135], v[64:67], v[244:247]\n\t" \
  "ds_read_b64_tr_b16 a[118:119], v51 offset:3072\n\t" \
  "v_mfma_f32_16x16x32_bf16 v[248:251], a[136:139], v[64:67], v[248:251]\n\t" \
  "buffer_load_dwordx4 v57, s[16:19], s96 offen offset:3072 sc1 lds\n\t" \
  "v_mfma_f32_16x16x32_bf16 v[252:255], a[140:143], v[64:67], v[252:255]\n\t" \
  "s_waitcnt lgkmcnt(11)\n\t" \
  "v_mfma_f32_16x16x32_bf16 v[80:83], a[72:75], a[0:3], 0\n\t" \
  "ds_read_b64_tr_b16 a[120:121], v51 offset:2560\n\t" \
  "v_mfma_f32_16x16x32_bf16 v[84:87], a[88:91], a[0:3], 0\n\t" \
  "ds_read_b64_tr_b16 a[122:123], v51 offset:3584\n\t" \
  "v_mfma_f32_16x16x32_bf16 v[80:83], a[76:79], a[4:7], v[80:83]\n\t" \
  "ds_read_b64_tr_b16 a[124:125], v51 offset:4096\n\t" \
  "v_mfma_f32_16x16x32_bf16 v[84:87], a[92:95], a[4:7], v[84:87]\n\t" \
  "s_waitcnt lgkmcnt(6)\n\t" \
  "ds_read_b64_tr_b16 a[126:127], v51 offset:5120\n\t" \
  "v_mfma_f32_16x16x32_bf16 v[80:83], a[80:83], a[8:11], v[80:83]\n\t" \
  "ds_read_b64_tr_b16 a[128:129], v51 offset:4608\n\t" \
  "v_mfma_f32_16x16x32_bf16 v[84:87], a[96:99], a[8:11], v[84:87]\n\t" \
  "ds_read_b64_tr_b16 a[130:131], v51 offset:5632\n\t" \
  "v_mfma_f32_16x16x32_bf16 v[80:83], a[84:87], a[12:15], v[80:83]\n\t" \
  "s_mov_b32 m0, s60\n\t" \
  "ds_read_b64_tr_b16 a[132:133], v51 offset:6144\n\t" \
  "v_mfma_f32_16x16x32_bf16 v[84:87], a[100:103], a[12:15], v[84:87]\n\t" \
  "buffer_load_dwordx4 v58, s[16:19], s96 offen offset:0 sc1 lds\n\t" \
  "v_mfma_f32_16x16x16_bf16 v[80:83], a[104:105], a[16:17], v[80:83]\n\t" \
  "ds_read_b64_tr_b16 a[134:135], v51 offset:7168\n\t" \
  "v_mfma_f32_16x16x16_bf16 v[84:87], a[106:107], a[16:17], v[84:87]\n\t" \
  "ds_read_b64_tr_b16 a[136:137], v51 offset:6656\n\t" \
  "v_mfma_f32_16x16x32_bf16 v[88:91], a[72:75], a[18:21], 0\n\t" \
  "ds_read_b64_tr_b16 a[138:139], v51 offset:7680\n\t" \
  "s_add_u32 m0, s87, 110592\n\t" \
  "s_add_u32 s98, s27, s87\n\t" \
  "s_add_u32 s98, s98, 36864\n\t" \
  "v_mfma_f32_16x16x32_bf16 v[92:95], a[88:91], a[18:21], 0\n\t" \
  "ds_read_b64_tr_b16 a[140:141], v51 offset:8192\n\t" \
  "buffer_load_dwordx4 v59, s[16:19], s98 offen offset:0 sc1 lds\n\t" \
  "v_mfma_f32_16x16x32_bf16 v[88:91], a[76:79], a[22:25], v[88:91]\n\t" \
  "ds_read_b64_tr_b16 a[142:143], v51 offset:8704\n\t" \
  "v_mfma_f32_16x16x32_bf16 v[92:95], a[92:95], a[22:25], v[92:95]\n\t" \
  "buffer_load_dwordx4 v59, s[16:19], s98 offen offset:1024 sc1 lds\n\t" \
  "v_exp_f32_e32 v80, v80\n\t" \
  "v_mfma_f32_16x16x32_bf16 v[88:91], a[80:83], a[26:29], v[88:91]\n\t" \
  "v_exp_f32_e32 v81, v81\n\t" \
  "v_mfma_f32_16x16x32_bf16 v[92:95], a[96:99], a[26:29], v[92:95]\n\t" \
  "s_waitcnt lgkmcnt(0)\n\t" \
  "v_mfma_f32_16x16x32_bf16 v[88:91], a[84:87], a[30:33], v[88:91]\n\t" \
  "buffer_load_dwordx4 v59, s[16:19], s98 offen offset:2048 sc1 lds\n\t" \
  "v_mfma_f32_16x16x32_bf16 v[92:95], a[100:103], a[30:33], v[92:95]\n\t" \
  "v_exp_f32_e32 v82, v82\n\t" \
  "v_mfma_f32_16x16x16_bf16 v[88:91], a[104:105], a[34:35], v[88:91]\n\t" \
  "v_exp_f32_e32 v83, v83\n\t" \
  "v_mfma_f32_16x16x16_bf16 v[92:95], a[106:107], a[34:35], v[92:95]\n\t" \
  "s_add_u32 m0, s49, 122368\n\t" \
  "s_add_u32 s98, s49, 9476096\n\t" \
  "v_cvt_pk_bf16_f32 v76, v80, v81\n\t" \
  "v_cvt_pk_bf16_f32 v77, v82, v83\n\t" \
  "v_mfma_f32_16x16x32_bf16 v[96:99], a[72:75], a[36:39], 0\n\t" \
  "buffer_load_dwordx4 v59, s[16:19], s98 offen offset:0 sc1 lds\n\t" \
  "v_exp_f32_e32 v84, v84\n\t" \
  "v_mfma_f32_16x16x32_bf16 v[100:103], a[88:91], a[36:39], 0\n\t" \
  "v_exp_f32_e32 v85, v85\n\t" \
  "v_mfma_f32_16x16x32_bf16 v[96:99], a[76:79], a[40:43], v[96:99]\n\t" \
  "v_exp_f32_e32 v86, v86\n\t" \
  "v_mfma_f32_16x16x32_bf16 v[100:103], a[92:95], a[40:43], v[100:103]\n\t" \
  "v_exp_f32_e32 v87, v87\n\t" \
  "v_mfma_f32_16x16x32_bf16 v[96:99], a[80:83], a[44:47], v[96:99]\n\t" \
  "buffer_load_dwordx4 v59, s[16:19], s98 offen offset:1024 sc1 lds\n\t" \
  "v_mfma_f32_16x16x32_bf16 v[100:103], a[96:99], a[44:47], v[100:103]\n\t" \
  "v_cvt_pk_bf16_f32 v78, v84, v85\n\t" \
  "v_cvt_pk_bf16_f32 v79, v86, v87\n\t" \
  "v_mfma_f32_16x16x32_bf16 v[96:99], a[84:87], a[48:51], v[96:99]\n\t" \
  "v_exp_f32_e32 v88, v88\n\t" \
  "v_mfma_f32_16x16x32_bf16 v[100:103], a[100:103], a[48:51], v[100:103]\n\t" \
  "v_exp_f32_e32 v89, v89\n\t" \
  "v_mfma_f32_16x16x16_bf16 v[96:99], a[104:105], a[52:53], v[96:99]\n\t" \
  "v_exp_f32_e32 v90, v90\n\t" \
  "v_mfma_f32_16x16x16_bf16 v[100:103], a[106:107], a[52:53], v[100:103]\n\t" \
  "buffer_load_dwordx4 v59, s[16:19], s98 offen offset:2048 sc1 lds\n\t" \
  "v_exp_f32_e32 v91, v91\n\t" \
  "v_mfma_f32_16x16x32_bf16 v[104:107], a[72:75], a[54:57], 0\n\t" \
  "v_cvt_pk_bf16_f32 v72, v88, v89\n\t" \
  "v_cvt_pk_bf16_f32 v73, v90, v91\n\t" \
  "v_mfma_f32_16x16x32_bf16 v[108:111], a[88:91], a[54:57], 0\n\t" \
  "v_exp_f32_e32 v92, v92\n\t" \
  "v_mfma_f32_16x16x32_bf16 v[104:107], a[76:79], a[58:61], v[104:107]\n\t" \
  "v_exp_f32_e32 v93, v93\n\t" \
  "v_mfma_f32_16x16x32_bf16 v[108:111], a[92:95], a[58:61], v[108:111]\n\t" \
  "buffer_load_dwordx4 v59, s[16:19], s98 offen offset:3072 sc1 lds\n\t" \
  "v_mfma_f32_16x16x32_bf16 v[104:107], a[80:83], a[62:65], v[104:107]\n\t" \
  "v_exp_f32_e32 v94, v94\n\t" \
  "v_mfma_f32_16x16x32_bf16 v[108:111], a[96:99], a[62:65], v[108:111]\n\t" \
  "v_exp_f32_e32 v95, v95\n\t" \
  "v_mfma_f32_16x16x32_bf16 v[104:107], a[84:87], a[66:69], v[104:107]\n\t" \
  "s_add_u32 m0, s49, 126464\n\t" \
  "s_add_u32 s98, s49, 9480192\n\t" \
  "v_cvt_pk_bf16_f32 v74, v92, v93\n\t" \
  "v_cvt_pk_bf16_f32 v75, v94, v95\n\t" \
  "v_mfma_f32_16x16x32_bf16 v[108:111], a[100:103], a[66:69], v[108:111]\n\t" \
  "buffer_load_dwordx4 v59, s[16:19], s98 offen offset:0 sc1 lds\n\t" \
  "v_exp_f32_e32 v96, v96\n\t" \
  "v_mfma_f32_16x16x16_bf16 v[104:107], a[104:105], a[70:71], v[104:107]\n\t" \
  "v_exp_f32_e32 v97, v97\n\t" \
  "v_mfma_f32_16x16x16_bf16 v[108:111], a[106:107], a[70:71], v[108:111]\n\t" \
  "v_exp_f32_e32 v98, v98\n\t" \
  "v_mfma_f32_16x16x32_bf16 v[112:115], a[108:111], v[76:79], v[112:115]\n\t" \
  "v_exp_f32_e32 v99, v99\n\t" \
  "v_mfma_f32_16x16x32_bf16 v[116:119], a[112:115], v[76:79], v[116:119]\n\t" \
  "buffer_load_dwordx4 v59, s[16:19], s98 offen offset:1024 sc1 lds\n\t" \
  "v_mfma_f32_16x16x32_bf16 v[120:123], a[116:119], v[76:79], v[120:123]\n\t" \
  "v_cvt_pk_bf16_f32 v68, v96, v97\n\t" \
  "v_cvt_pk_bf16_f32 v69, v98, v99\n\t" \
  "v_mfma_f32_16x16x32_bf16 v[124:127], a[120:123], v[76:79], v[124:127]\n\t" \
  "v_exp_f32_e32 v100, v100\n\t" \
  "v_mfma_f32_16x16x32_bf16 v[128:131], a[124:127], v[76:79], v[128:131]\n\t" \
  "v_exp_f32_e32 v101, v101\n\t" \
  "v_mfma_f32_16x16x32_bf16 v[132:135], a[128:131], v[76:79], v[132:135]\n\t" \
  "v_exp_f32_e32 v102, v102\n\t" \
  "v_mfma_f32_16x16x32_bf16 v[136:139], a[132:135], v[76:79], v[136:139]\n\t" \
  "buffer_load_dwordx4 v59, s[16:19], s98 offen offset:2048 sc1 lds\n\t" \
  "v_exp_f32_e32 v103, v103\n\t" \
  "v_mfma_f32_16x16x32_bf16 v[140:143], a[136:139], v[76:79], v[140:143]\n\t" \
  "v_cvt_pk_bf16_f32 v70, v100, v101\n\t" \
  "v_cvt_pk_bf16_f32 v71, v102, v103\n\t" \
  "v_mfma_f32_16x16x32_bf16 v[144:147], a[140:143], v[76:79], v[144:147]\n\t" \
  "v_exp_f32_e32 v104, v104\n\t" \
  "v_mfma_f32_16x16x32_bf16 v[148:151], a[108:111], v[72:75], v[148:151]\n\t" \
  "s_waitcnt vmcnt(24)\n\t" \
  "v_mfma_f32_16x16x32_bf16 v[152:155], a[112:115], v[72:75], v[152:155]\n\t" \
  "buffer_load_dwordx4 v59, s[16:19], s98 offen offset:3072 sc1 lds\n\t" \
  "v_mfma_f32_16x16x32_bf16 v[156:159], a[116:119], v[72:75], v[156:159]\n\t" \
  "v_exp_f32_e32 v105, v105\n\t" \
  "v_mfma_f32_16x16x32_bf16 v[160:163], a[120:123], v[72:75], v[160:163]\n\t" \
  "v_exp_f32_e32 v106, v106\n\t" \
  "v_mfma_f32_16x16x32_bf16 v[164:167], a[124:127], v[72:75], v[164:167]\n\t" \
  "s_add_u32 m0, s49, 130560\n\t" \
  "s_add_u32 s98, s49, 9484288\n\t" \
  "v_exp_f32_e32 v107, v107\n\t" \
  "v_mfma_f32_16x16x32_bf16 v[168:171], a[128:131], v[72:75], v[168:171]\n\t" \
  "buffer_load_dwordx4 v59, s[16:19], s98 offen offset:0 sc1 lds\n\t" \
  "v_cvt_pk_bf16_f32 v64, v104, v105\n\t" \
  "v_cvt_pk_bf16_f32 v65, v106, v107\n\t" \
  "v_mfma_f32_16x16x32_bf16 v[172:175], a[132:135], v[72:75], v[172:175]\n\t" \
  "v_exp_f32_e32 v108, v108\n\t" \
  "v_mfma_f32_16x16x32_bf16 v[176:179], a[136:139], v[72:75], v[176:179]\n\t" \
  "v_exp_f32_e32 v109, v109\n\t" \
  "v_mfma_f32_16x16x32_bf16 v[180:183], a[140:143], v[72:75], v[180:183]\n\t" \
  "v_exp_f32_e32 v110, v110\n\t" \
  "v_mfma_f32_16x16x32_bf16 v[184:187], a[108:111], v[68:71], v[184:187]\n\t" \
  "buffer_load_dwordx4 v59, s[16:19], s98 offen offset:1024 sc1 lds\n\t" \
  "v_mfma_f32_16x16x32_bf16 v[188:191], a[112:115], v[68:71], v[188:191]\n\t" \
  "v_exp_f32_e32 v111, v111\n\t" \
  "v_mfma_f32_16x16x32_bf16 v[192:195], a[116:119], v[68:71], v[192:195]\n\t" \
  "v_cvt_pk_bf16_f32 v66, v108, v109\n\t" \
  "v_cvt_pk_bf16_f32 v67, v110, v111\n\t" \
  "v_mfma_f32_16x16x32_bf16 v[196:199], a[120:123], v[68:71], v[196:199]\n\t" \
  "ds_read_b128 a[72:75], v49 offset:0\n\t" \
  "v_mfma_f32_16x16x32_bf16 v[200:203], a[124:127], v[68:71], v[200:203]\n\t" \
  "ds_read_b128 a[88:91], v49 offset:1024\n\t" \
  "buffer_load_dwordx4 v59, s[16:19], s98 offen offset:2048 sc1 lds\n\t" \
  "v_mfma_f32_16x16x32_bf16 v[204:207], a[128:131], v[68:71], v[204:207]\n\t" \
  "ds_read_b128 a[76:79], v49 offset:2048\n\t" \
  "s_add_u32 m0, s48, 0\n\t" \
  "s_add_u32 s98, s48, 9517568\n\t" \
  "v_mfma_f32_16x16x32_bf16 v[208:211], a[132:135], v[68:71], v[208:211]\n\t" \
  "ds_read_b128 a[92:95], v49 offset:3072\n\t" \
  "buffer_load_dwordx4 v59, s[16:19], s98 offen offset:0 sc1 lds\n\t" \
  "v_mfma_f32_16x16x32_bf16 v[212:215], a[136:139], v[68:71], v[212:215]\n\t" \
  "ds_read_b128 a[80:83], v49 offset:4096\n\t" \
  "v_mfma_f32_16x16x32_bf16 v[216:219], a[140:143], v[68:71], v[216:219]\n\t" \
  "buffer_load_dwordx4 v59, s[16:19], s98 offen offset:1024 sc1 lds\n\t" \
  "v_mfma_f32_16x16x32_bf16 v[220:223], a[108:111], v[64:67], v[220:223]\n\t" \
  "ds_read_b128 a[96:99], v49 offset:5120\n\t" \
  "v_mfma_f32_16x16x32_bf16 v[224:227], a[112:115], v[64:67], v[224:227]\n\t" \
  "ds_read_b64_tr_b16 a[108:109], v52 offset:0\n\t" \
  "ds_read_b128 a[84:87], v49 offset:6144\n\t" \
  "v_mfma_f32_16x16x32_bf16 v[228:231], a[116:119], v[64:67], v[228:231]\n\t" \
  "ds_read_b64_tr_b16 a[110:111], v52 offset:1024\n\t" \
  "ds_read_b128 a[100:103], v49 offset:7168\n\t" \
  "v_mfma_f32_16x16x32_bf16 v[232:235], a[120:123], v[64:67], v[232:235]\n\t" \
  "ds_read_b64_tr_b16 a[112:113], v52 offset:512\n\t" \
  "ds_read2st64_b64 a[104:107], v55 offset0:16 offset1:17\n\t" \
  "buffer_load_dwordx4 v59, s[16:19], s98 offen offset:2048 sc1 lds\n\t" \
  "v_mfma_f32_16x16x32_bf16 v[236:239], a[124:127], v[64:67], v[236:239]\n\t" \
  "ds_read_b64_tr_b16 a[114:115], v52 offset:1536\n\t" \
  "v_mfma_f32_16x16x32_bf16 v[240:243], a[128:131], v[64:67], v[240:243]\n\t" \
  "ds_read_b64_tr_b16 a[116:117], v52 offset:2048\n\t" \
  "v_mfma_f32_16x16x32_bf16 v[244:247], a[132:135], v[64:67], v[244:247]\n\t" \
  "ds_read_b64_tr_b16 a[118:119], v52 offset:3072\n\t" \
  "v_mfma_f32_16x16x32_bf16 v[248:251], a[136:139], v[64:67], v[248:251]\n\t" \
  "buffer_load_dwordx4 v59, s[16:19], s98 offen offset:3072 sc1 lds\n\t" \
  "v_mfma_f32_16x16x32_bf16 v[252:255], a[140:143], v[64:67], v[252:255]\n\t" \
  "s_waitcnt lgkmcnt(11)\n\t" \
  "v_mfma_f32_16x16x32_bf16 v[80:83], a[72:75], a[0:3], 0\n\t" \
  "ds_read_b64_tr_b16 a[120:121], v52 offset:2560\n\t" \
  "v_mfma_f32_16x16x32_bf16 v[84:87], a[88:91], a[0:3], 0\n\t" \
  "ds_read_b64_tr_b16 a[122:123], v52 offset:3584\n\t" \
  "s_add_u32 m0, s48, 4096\n\t" \
  "s_add_u32 s98, s48, 9521664\n\t" \
  "v_mfma_f32_16x16x32_bf16 v[80:83], a[76:79], a[4:7], v[80:83]\n\t" \
  "ds_read_b64_tr_b16 a[124:125], v52 offset:4096\n\t" \
  "buffer_load_dwordx4 v59, s[16:19], s98 offen offset:0 sc1 lds\n\t" \
  "v_mfma_f32_16x16x32_bf16 v[84:87], a[92:95], a[4:7], v[84:87]\n\t" \
  "s_waitcnt lgkmcnt(6)\n\t" \
  "ds_read_b64_tr_b16 a[126:127], v52 offset:5120\n\t" \
  "v_mfma_f32_16x16x32_bf16 v[80:83], a[80:83], a[8:11], v[80:83]\n\t" \
  "ds_read_b64_tr_b16 a[128:129], v52 offset:4608\n\t" \
  "v_mfma_f32_16x16x32_bf16 v[84:87], a[96:99], a[8:11], v[84:87]\n\t" \
  "ds_read_b64_tr_b16 a[130:131], v52 offset:5632\n\t" \
  "v_mfma_f32_16x16x32_bf16 v[80:83], a[84:87], a[12:15], v[80:83]\n\t" \
  "ds_read_b64_tr_b16 a[132:133], v52 offset:6144\n\t" \
  "v_mfma_f32_16x16x32_bf16 v[84:87], a[100:103], a[12:15], v[84:87]\n\t" \
  "buffer_load_dwordx4 v59, s[16:19], s98 offen offset:1024 sc1 lds\n\t" \
  "v_mfma_f32_16x16x16_bf16 v[80:83], a[104:105], a[16:17], v[80:83]\n\t" \
  "ds_read_b64_tr_b16 a[134:135], v52 offset:7168\n\t" \
  "v_mfma_f32_16x16x16_bf16 v[84:87], a[106:107], a[16:17], v[84:87]\n\t" \
  "ds_read_b64_tr_b16 a[136:137], v52 offset:6656\n\t" \
  "v_mfma_f32_16x16x32_bf16 v[88:91], a[72:75], a[18:21], 0\n\t" \
  "ds_read_b64_tr_b16 a[138:139], v52 offset:7680\n\t" \
  "v_mfma_f32_16x16x32_bf16 v[92:95], a[88:91], a[18:21], 0\n\t" \
  "ds_read_b64_tr_b16 a[140:141], v52 offset:8192\n\t" \
  "buffer_load_dwordx4 v59, s[16:19], s98 offen offset:2048 sc1 lds\n\t" \
  "v_mfma_f32_16x16x32_bf16 v[88:91], a[76:79], a[22:25], v[88:91]\n\t" \
  "ds_read_b64_tr_b16 a[142:143], v52 offset:8704\n\t" \
  "v_mfma_f32_16x16x32_bf16 v[92:95], a[92:95], a[22:25], v[92:95]\n\t" \
  "v_exp_f32_e32 v80, v80\n\t" \
  "v_mfma_f32_16x16x32_bf16 v[88:91], a[80:83], a[26:29], v[88:91]\n\t" \
  "v_exp_f32_e32 v81, v81\n\t" \
  "v_mfma_f32_16x16x32_bf16 v[92:95], a[96:99], a[26:29], v[92:95]\n\t" \
  "s_waitcnt lgkmcnt(0)\n\t" \
  "v_mfma_f32_16x16x32_bf16 v[88:91], a[84:87], a[30:33], v[88:91]\n\t" \
  "buffer_load_dwordx4 v59, s[16:19], s98 offen offset:3072 sc1 lds\n\t" \
  "v_mfma_f32_16x16x32_bf16 v[92:95], a[100:103], a[30:33], v[92:95]\n\t" \
  "v_exp_f32_e32 v82, v82\n\t" \
  "v_mfma_f32_16x16x16_bf16 v[88:91], a[104:105], a[34:35], v[88:91]\n\t" \
  "v_exp_f32_e32 v83, v83\n\t" \
  "v_mfma_f32_16x16x16_bf16 v[92:95], a[106:107], a[34:35], v[92:95]\n\t" \
  "s_add_u32 m0, s48, 8192\n\t" \
  "s_add_u32 s98, s48, 9525760\n\t" \
  "v_cvt_pk_bf16_f32 v76, v80, v81\n\t" \
  "v_cvt_pk_bf16_f32 v77, v82, v83\n\t" \
  "v_mfma_f32_16x16x32_bf16 v[96:99], a[72:75], a[36:39], 0\n\t" \
  "buffer_load_dwordx4 v59, s[16:19], s98 offen offset:0 sc1 lds\n\t" \
  "v_exp_f32_e32 v84, v84\n\t" \
  "v_mfma_f32_16x16x32_bf16 v[100:103], a[88:91], a[36:39], 0\n\t" \
  "v_exp_f32_e32 v85, v85\n\t" \
  "v_mfma_f32_16x16x32_bf16 v[96:99], a[76:79], a[40:43], v[96:99]\n\t" \
  "v_exp_f32_e32 v86, v86\n\t" \
  "v_mfma_f32_16x16x32_bf16 v[100:103], a[92:95], a[40:43], v[100:103]\n\t" \
  "v_exp_f32_e32 v87, v87\n\t" \
  "v_mfma_f32_16x16x32_bf16 v[96:99], a[80:83], a[44:47], v[96:99]\n\t" \
  "buffer_load_dwordx4 v[22:25], v2, s[4:7], s20 offen offset:0\n\t" \
  "v_mfma_f32_16x16x32_bf16 v[100:103], a[96:99], a[44:47], v[100:103]\n\t" \
  "v_cvt_pk_bf16_f32 v78, v84, v85\n\t" \
  "v_cvt_pk_bf16_f32 v79, v86, v87\n\t" \
  "v_mfma_f32_16x16x32_bf16 v[96:99], a[84:87], a[48:51], v[96:99]\n\t" \
  "v_exp_f32_e32 v88, v88\n\t" \
  "v_mfma_f32_16x16x32_bf16 v[100:103], a[100:103], a[48:51], v[100:103]\n\t" \
  "v_exp_f32_e32 v89, v89\n\t" \
  "v_mfma_f32_16x16x16_bf16 v[96:99], a[104:105], a[52:53], v[96:99]\n\t" \
  "v_exp_f32_e32 v90, v90\n\t" \
  "v_mfma_f32_16x16x16_bf16 v[100:103], a[106:107], a[52:53], v[100:103]\n\t" \
  "buffer_load_dwordx4 v[26:29], v2, s[4:7], s20 offen offset:64\n\t" \
  "v_exp_f32_e32 v91, v91\n\t" \
  "v_mfma_f32_16x16x32_bf16 v[104:107], a[72:75], a[54:57], 0\n\t" \
  "v_cvt_pk_bf16_f32 v72, v88, v89\n\t" \
  "v_cvt_pk_bf16_f32 v73, v90, v91\n\t" \
  "v_mfma_f32_16x16x32_bf16 v[108:111], a[88:91], a[54:57], 0\n\t" \
  "v_exp_f32_e32 v92, v92\n\t" \
  "v_mfma_f32_16x16x32_bf16 v[104:107], a[76:79], a[58:61], v[104:107]\n\t" \
  "v_exp_f32_e32 v93, v93\n\t" \
  "v_mfma_f32_16x16x32_bf16 v[108:111], a[92:95], a[58:61], v[108:111]\n\t" \
  "buffer_load_dwordx2 v[60:61], v3, s[4:7], s21 offen\n\t" \
  "v_mfma_f32_16x16x32_bf16 v[104:107], a[80:83], a[62:65], v[104:107]\n\t" \
  "v_exp_f32_e32 v94, v94\n\t" \
  "v_mfma_f32_16x16x32_bf16 v[108:111], a[96:99], a[62:65], v[108:111]\n\t" \
  "v_exp_f32_e32 v95, v95\n\t" \
  "v_mfma_f32_16x16x32_bf16 v[104:107], a[84:87], a[66:69], v[104:107]\n\t" \
  "v_add_u32_e32 v48, 110592, v17\n\t" \
  "v_cvt_pk_bf16_f32 v74, v92, v93\n\t" \
  "v_cvt_pk_bf16_f32 v75, v94, v95\n\t" \
  "v_mfma_f32_16x16x32_bf16 v[108:111], a[100:103], a[66:69], v[108:111]\n\t" \
  "v_add_u32_e32 v51, 110592, v18\n\t" \
  "v_exp_f32_e32 v96, v96\n\t" \
  "v_mfma_f32_16x16x16_bf16 v[104:107], a[104:105], a[70:71], v[104:107]\n\t" \
  "v_add_u32_e32 v54, 110592, v19\n\t" \
  "v_exp_f32_e32 v97, v97\n\t" \
  "v_mfma_f32_16x16x16_bf16 v[108:111], a[106:107], a[70:71], v[108:111]\n\t" \
  "v_exp_f32_e32 v98, v98\n\t" \
  "v_mfma_f32_16x16x32_bf16 v[112:115], a[108:111], v[76:79], v[112:115]\n\t" \
  "v_exp_f32_e32 v99, v99\n\t" \
  "v_mfma_f32_16x16x32_bf16 v[116:119], a[112:115], v[76:79], v[116:119]\n\t" \
  "v_mfma_f32_16x16x32_bf16 v[120:123], a[116:119], v[76:79], v[120:123]\n\t" \
  "v_cvt_pk_bf16_f32 v68, v96, v97\n\t" \
  "v_cvt_pk_bf16_f32 v69, v98, v99\n\t" \
  "v_mfma_f32_16x16x32_bf16 v[124:127], a[120:123], v[76:79], v[124:127]\n\t" \
  "v_exp_f32_e32 v100, v100\n\t" \
  "v_mfma_f32_16x16x32_bf16 v[128:131], a[124:127], v[76:79], v[128:131]\n\t" \
  "v_exp_f32_e32 v101, v101\n\t" \
  "v_mfma_f32_16x16x32_bf16 v[132:135], a[128:131], v[76:79], v[132:135]\n\t" \
  "v_exp_f32_e32 v102, v102\n\t" \
  "v_mfma_f32_16x16x32_bf16 v[136:139], a[132:135], v[76:79], v[136:139]\n\t" \
  "v_exp_f32_e32 v103, v103\n\t" \
  "v_mfma_f32_16x16x32_bf16 v[140:143], a[136:139], v[76:79], v[140:143]\n\t" \
  "v_cvt_pk_bf16_f32 v70, v100, v101\n\t" \
  "v_cvt_pk_bf16_f32 v71, v102, v103\n\t" \
  "v_mfma_f32_16x16x32_bf16 v[144:147], a[140:143], v[76:79], v[144:147]\n\t" \
  "v_exp_f32_e32 v104, v104\n\t" \
  "v_mfma_f32_16x16x32_bf16 v[148:151], a[108:111], v[72:75], v[148:151]\n\t" \
  "s_waitcnt vmcnt(26)\n\t" \
  "v_cvt_pk_bf16_f32 v80, v112, v113\n\t" \
  "v_mfma_f32_16x16x32_bf16 v[152:155], a[112:115], v[72:75], v[152:155]\n\t" \
  "v_cvt_pk_bf16_f32 v81, v114, v115\n\t" \
  "v_cvt_pk_bf16_f32 v82, v116, v117\n\t" \
  "v_mfma_f32_16x16x32_bf16 v[156:159], a[116:119], v[72:75], v[156:159]\n\t" \
  "v_exp_f32_e32 v105, v105\n\t" \
  "v_mfma_f32_16x16x32_bf16 v[160:163], a[120:123], v[72:75], v[160:163]\n\t" \
  "v_exp_f32_e32 v106, v106\n\t" \
  "v_mfma_f32_16x16x32_bf16 v[164:167], a[124:127], v[72:75], v[164:167]\n\t" \
  "v_exp_f32_e32 v107, v107\n\t" \
  "v_mfma_f32_16x16x32_bf16 v[168:171], a[128:131], v[72:75], v[168:171]\n\t" \
  "v_cvt_pk_bf16_f32 v64, v104, v105\n\t" \
  "v_cvt_pk_bf16_f32 v65, v106, v107\n\t" \
  "v_mfma_f32_16x16x32_bf16 v[172:175], a[132:135], v[72:75], v[172:175]\n\t" \
  "v_exp_f32_e32 v108, v108\n\t" \
  "v_mfma_f32_16x16x32_bf16 v[176:179], a[136:139], v[72:75], v[176:179]\n\t" \
  "v_exp_f32_e32 v109, v109\n\t" \
  "v_mfma_f32_16x16x32_bf16 v[180:183], a[140:143], v[72:75], v[180:183]\n\t" \
  "v_exp_f32_e32 v110, v110\n\t" \
  "v_mfma_f32_16x16x32_bf16 v[184:187], a[108:111], v[68:71], v[184:187]\n\t" \
  "v_cvt_pk_bf16_f32 v83, v118, v119\n\t" \
  "ds_write_b128 v34, v[80:83] offset:0\n\t" \
  "v_mfma_f32_16x16x32_bf16 v[188:191], a[112:115], v[68:71], v[188:191]\n\t" \
  "v_exp_f32_e32 v111, v111\n\t" \
  "v_mfma_f32_16x16x32_bf16 v[192:195], a[116:119], v[68:71], v[192:195]\n\t" \
  "v_cvt_pk_bf16_f32 v66, v108, v109\n\t" \
  "v_cvt_pk_bf16_f32 v67, v110, v111\n\t" \
  "v_mfma_f32_16x16x32_bf16 v[196:199], a[120:123], v[68:71], v[196:199]\n\t" \
  "ds_read_b128 a[72:75], v50 offset:0\n\t" \
  "v_cvt_pk_bf16_f32 v84, v120, v121\n\t" \
  "v_mfma_f32_16x16x32_bf16 v[200:203], a[124:127], v[68:71], v[200:203]\n\t" \
  "ds_read_b128 a[88:91], v50 offset:1024\n\t" \
  "v_cvt_pk_bf16_f32 v85, v122, v123\n\t" \
  "v_mfma_f32_16x16x32_bf16 v[204:207], a[128:131], v[68:71], v[204:207]\n\t" \
  "ds_read_b128 a[76:79], v50 offset:2048\n\t" \
  "v_cvt_pk_bf16_f32 v86, v124, v125\n\t" \
  "v_mfma_f32_16x16x32_bf16 v[208:211], a[132:135], v[68:71], v[208:211]\n\t" \
  "ds_read_b128 a[92:95], v50 offset:3072\n\t" \
  "v_cvt_pk_bf16_f32 v87, v126, v127\n\t" \
  "v_mfma_f32_16x16x32_bf16 v[212:215], a[136:139], v[68:71], v[212:215]\n\t" \
  "ds_read_b128 a[80:83], v50 offset:4096\n\t" \
  "ds_write_b128 v34, v[84:87] offset:1024\n\t" \
  "v_mfma_f32_16x16x32_bf16 v[216:219], a[140:143], v[68:71], v[216:219]\n\t" \
  "v_cvt_pk_bf16_f32 v88, v128, v129\n\t" \
  "v_cvt_pk_bf16_f32 v89, v130, v131\n\t" \
  "v_mfma_f32_16x16x32_bf16 v[220:223], a[108:111], v[64:67], v[220:223]\n\t" \
  "ds_read_b128 a[96:99], v50 offset:5120\n\t" \
  "v_cvt_pk_bf16_f32 v90, v132, v133\n\t" \
  "v_mfma_f32_16x16x32_bf16 v[224:227], a[112:115], v[64:67], v[224:227]\n\t" \
  "ds_read_b64_tr_b16 a[108:109], v53 offset:0\n\t" \
  "ds_read_b128 a[84:87], v50 offset:6144\n\t" \
  "v_mfma_f32_16x16x32_bf16 v[228:231], a[116:119], v[64:67], v[228:231]\n\t" \
  "ds_read_b64_tr_b16 a[110:111], v53 offset:1024\n\t" \
  "ds_read_b128 a[100:103], v50 offset:7168\n\t" \
  "v_mfma_f32_16x16x32_bf16 v[232:235], a[120:123], v[64:67], v[232:235]\n\t" \
  "ds_read_b64_tr_b16 a[112:113], v53 offset:512\n\t" \
  "ds_read2st64_b64 a[104:107], v56 offset0:16 offset1:17\n\t" \
  "v_mfma_f32_16x16x32_bf16 v[236:239], a[124:127], v[64:67], v[236:239]\n\t" \
  "ds_read_b64_tr_b16 a[114:115], v53 offset:1536\n\t" \
  "v_cvt_pk_bf16_f32 v91, v134, v135\n\t" \
  "v_mfma_f32_16x16x32_bf16 v[240:243], a[128:131], v[64:67], v[240:243]\n\t" \
  "ds_read_b64_tr_b16 a[116:117], v53 offset:2048\n\t" \
  "ds_write_b128 v34, v[88:91] offset:2048\n\t" \
  "v_mfma_f32_16x16x32_bf16 v[244:247], a[132:135], v[64:67], v[244:247]\n\t" \
  "ds_read_b64_tr_b16 a[118:119], v53 offset:3072\n\t" \
  "v_cvt_pk_bf16_f32 v92, v136, v137\n\t" \
  "v_mfma_f32_16x16x32_bf16 v[248:251], a[136:139], v[64:67], v[248:251]\n\t" \
  "v_cvt_pk_bf16_f32 v93, v138, v139\n\t" \
  "v_cvt_pk_bf16_f32 v94, v140, v141\n\t" \
  "v_mfma_f32_16x16x32_bf16 v[252:255], a[140:143], v[64:67], v[252:255]\n\t" \
  "s_waitcnt lgkmcnt(13)\n\t" \
  "v_cvt_pk_bf16_f32 v95, v142, v143\n\t" \
  "ds_write_b128 v34, v[92:95] offset:3072\n\t" \
  "v_cvt_pk_bf16_f32 v80, v144, v145\n\t" \
  "v_cvt_pk_bf16_f32 v81, v146, v147\n\t" \
  "ds_write_b64 v34, v[80:81] offset:8192\n\t" \
  "v_cvt_pk_bf16_f32 v84, v148, v149\n\t" \
  "v_cvt_pk_bf16_f32 v85, v150, v151\n\t" \
  "v_cvt_pk_bf16_f32 v86, v152, v153\n\t" \
  "v_cvt_pk_bf16_f32 v87, v154, v155\n\t" \
  "ds_write_b128 v34, v[84:87] offset:4096\n\t" \
  "v_cvt_pk_bf16_f32 v88, v156, v157\n\t" \
  "v_cvt_pk_bf16_f32 v89, v158, v159\n\t" \
  "v_cvt_pk_bf16_f32 v90, v160, v161\n\t" \
  "v_cvt_pk_bf16_f32 v91, v162, v163\n\t" \
  "ds_write_b128 v34, v[88:91] offset:5120\n\t" \
  "v_cvt_pk_bf16_f32 v92, v164, v165\n\t" \
  "v_cvt_pk_bf16_f32 v93, v166, v167\n\t" \
  "v_cvt_pk_bf16_f32 v94, v168, v169\n\t" \
  "v_cvt_pk_bf16_f32 v95, v170, v171\n\t" \
  "ds_write_b128 v34, v[92:95] offset:6144\n\t" \
  "v_cvt_pk_bf16_f32 v80, v172, v173\n\t" \
  "v_cvt_pk_bf16_f32 v81, v174, v175\n\t" \
  "v_cvt_pk_bf16_f32 v82, v176, v177\n\t" \
  "v_cvt_pk_bf16_f32 v83, v178, v179\n\t" \
  "ds_write_b128 v34, v[80:83] offset:7168\n\t" \
  "v_cvt_pk_bf16_f32 v84, v180, v181\n\t" \
  "v_cvt_pk_bf16_f32 v85, v182, v183\n\t" \
  "ds_write_b64 v34, v[84:85] offset:8200\n\t" \
  "s_nop 7\n\t" \
  "s_waitcnt vmcnt(0) lgkmcnt(0)\n\t" \
  "s_barrier\n\t" \
  "s_cmp_eq_u32 s47, 0\n\t" \
  "s_cbranch_scc0 SA%=\n\t" \
  "ds_read_b128 a[0:3], v12 offset:0\n\t" \
  "ds_read_b128 a[18:21], v13 offset:0\n\t" \
  "ds_read_b128 v[72:75], v14 offset:4096\n\t" \
  "ds_read_b128 a[4:7], v12 offset:1024\n\t" \
  "ds_read_b128 a[22:25], v13 offset:1024\n\t" \
  "ds_read_b128 v[76:79], v14 offset:5120\n\t" \
  "ds_read_b128 a[8:11], v12 offset:2048\n\t" \
  "ds_read_b128 a[26:29], v13 offset:2048\n\t" \
  "ds_read_b128 v[80:83], v14 offset:6144\n\t" \
  "ds_read_b128 a[12:15], v12 offset:3072\n\t" \
  "ds_read_b128 a[30:33], v13 offset:3072\n\t" \
  "ds_read_b128 v[84:87], v14 offset:7168\n\t" \
  "ds_read_b64 a[16:17], v12 offset:8192\n\t" \
  "ds_read_b64 a[34:35], v13 offset:8192\n\t" \
  "ds_read_b64 v[88:89], v14 offset:8200\n\t" \
  "v_mov_b32_e32 v113, s92\n\t" \
  "v_mov_b32_e32 v115, s92\n\t" \
  "v_mov_b32_e32 v117, s92\n\t" \
  "v_mov_b32_e32 v119, s92\n\t" \
  "v_mov_b32_e32 v121, s92\n\t" \
  "v_mov_b32_e32 v123, s92\n\t" \
  "v_mov_b32_e32 v125, s92\n\t" \
  "v_mov_b32_e32 v127, s92\n\t" \
  "v_mov_b32_e32 v129, s92\n\t" \
  "v_mov_b32_e32 v131, s92\n\t" \
  "v_mov_b32_e32 v133, s92\n\t" \
  "v_mov_b32_e32 v135, s92\n\t" \
  "v_mov_b32_e32 v137, s92\n\t" \
  "v_mov_b32_e32 v139, s92\n\t" \
  "v_mov_b32_e32 v141, s92\n\t" \
  "v_mov_b32_e32 v143, s92\n\t" \
  "v_mov_b32_e32 v145, s92\n\t" \
  "v_mov_b32_e32 v147, s92\n\t" \
  "s_waitcnt lgkmcnt(0)\n\t" \
  "v_mfma_f32_16x16x16_bf16 v[148:151], v[30:31], a[0:1], v[148:151]\n\t" \
  "v_mfma_f32_16x16x16_bf16 v[148:151], v[30:31], a[18:19], v[148:151]\n\t" \
  "v_mfma_f32_16x16x16_bf16 v[148:151], v[30:31], v[72:73], v[148:151]\n\t" \
  "v_mfma_f32_16x16x16_bf16 v[152:155], v[30:31], a[2:3], v[152:155]\n\t" \
  "v_mfma_f32_16x16x16_bf16 v[152:155], v[30:31], a[20:21], v[152:155]\n\t" \
  "v_mfma_f32_16x16x16_bf16 v[152:155], v[30:31], v[74:75], v[152:155]\n\t" \
  "v_cvt_pk_bf16_f32 v112, v148, v149\n\t" \
  "v_cvt_pk_bf16_f32 v114, v150, v151\n\t" \
  "v_mfma_f32_16x16x16_bf16 v[156:159], v[30:31], a[4:5], v[156:159]\n\t" \
  "buffer_store_dwordx4 v[112:115], v59, s[16:19], s22 offen offset:0 sc1\n\t" \
  "v_mfma_f32_16x16x16_bf16 v[156:159], v[30:31], a[22:23], v[156:159]\n\t" \
  "v_mfma_f32_16x16x16_bf16 v[156:159], v[30:31], v[76:77], v[156:159]\n\t" \
  "v_cvt_pk_bf16_f32 v116, v152, v153\n\t" \
  "v_cvt_pk_bf16_f32 v118, v154, v155\n\t" \
  "v_mfma_f32_16x16x16_bf16 v[160:163], v[30:31], a[6:7], v[160:163]\n\t" \
  "buffer_store_dwordx4 v[116:119], v59, s[16:19], s22 offen offset:1024 sc1\n\t" \
  "v_mfma_f32_16x16x16_bf16 v[160:163], v[30:31], a[24:25], v[160:163]\n\t" \
  "v_mfma_f32_16x16x16_bf16 v[160:163], v[30:31], v[78:79], v[160:163]\n\t" \
  "v_cvt_pk_bf16_f32 v120, v156, v157\n\t" \
  "v_cvt_pk_bf16_f32 v122, v158, v159\n\t" \
  "v_mfma_f32_16x16x16_bf16 v[164:167], v[30:31], a[8:9], v[164:167]\n\t" \
  "buffer_store_dwordx4 v[120:123], v59, s[16:19], s22 offen offset:2048 sc1\n\t" \
  "v_mfma_f32_16x16x16_bf16 v[164:167], v[30:31], a[26:27], v[164:167]\n\t" \
  "v_mfma_f32_16x16x16_bf16 v[164:167], v[30:31], v[80:81], v[164:167]\n\t" \
  "v_cvt_pk_bf16_f32 v124, v160, v161\n\t" \
  "v_cvt_pk_bf16_f32 v126, v162, v163\n\t" \
  "v_mfma_f32_16x16x16_bf16 v[168:171], v[30:31], a[10:11], v[168:171]\n\t" \
  "buffer_store_dwordx4 v[124:127], v59, s[16:19], s22 offen offset:3072 sc1\n\t" \
  "v_mfma_f32_16x16x16_bf16 v[168:171], v[30:31], a[28:29], v[168:171]\n\t" \
  "v_mfma_f32_16x16x16_bf16 v[168:171], v[30:31], v[82:83], v[168:171]\n\t" \
  "v_cvt_pk_bf16_f32 v128, v164, v165\n\t" \
  "v_cvt_pk_bf16_f32 v130, v166, v167\n\t" \
  "v_mfma_f32_16x16x16_bf16 v[172:175], v[30:31], a[12:13], v[172:175]\n\t" \
  "buffer_store_dwordx4 v[128:131], v59, s[16:19], s23 offen offset:0 sc1\n\t" \
  "v_mfma_f32_16x16x16_bf16 v[172:175], v[30:31], a[30:31], v[172:175]\n\t" \
  "v_mfma_f32_16x16x16_bf16 v[172:175], v[30:31], v[84:85], v[172:175]\n\t" \
  "v_cvt_pk_bf16_f32 v132, v168, v169\n\t" \
  "v_cvt_pk_bf16_f32 v134, v170, v171\n\t" \
  "v_mfma_f32_16x16x16_bf16 v[176:179], v[30:31], a[14:15], v[176:179]\n\t" \
  "buffer_store_dwordx4 v[132:135], v59, s[16:19], s23 offen offset:1024 sc1\n\t" \
  "v_mfma_f32_16x16x16_bf16 v[176:179], v[30:31], a[32:33], v[176:179]\n\t" \
  "v_mfma_f32_16x16x16_bf16 v[176:179], v[30:31], v[86:87], v[176:179]\n\t" \
  "v_cvt_pk_bf16_f32 v136, v172, v173\n\t" \
  "v_cvt_pk_bf16_f32 v138, v174, v175\n\t" \
  "v_mfma_f32_16x16x16_bf16 v[180:183], v[30:31], a[16:17], v[180:183]\n\t" \
  "buffer_store_dwordx4 v[136:139], v59, s[16:19], s23 offen offset:2048 sc1\n\t" \
  "v_mfma_f32_16x16x16_bf16 v[180:183], v[30:31], a[34:35], v[180:183]\n\t" \
  "v_mfma_f32_16x16x16_bf16 v[180:183], v[30:31], v[88:89], v[180:183]\n\t" \
  "v_cvt_pk_bf16_f32 v140, v176, v177\n\t" \
  "v_cvt_pk_bf16_f32 v142, v178, v179\n\t" \
  "buffer_store_dwordx4 v[140:143], v59, s[16:19], s23 offen offset:3072 sc1\n\t" \
  "s_nop 4\n\t" \
  "v_cvt_pk_bf16_f32 v144, v180, v181\n\t" \
  "v_cvt_pk_bf16_f32 v146, v182, v183\n\t" \
  "buffer_store_dwordx4 v[144:147], v59, s[16:19], s26 offen offset:0 sc1\n\t" \
  "SA%=:\n\t" \
  "s_cmp_eq_u32 s47, 1\n\t" \
  "s_cbranch_scc0 KLA%=\n\t" \
  "v_mfma_f32_4x4x4_16b_bf16 v[112:115], a[164:165], a[144:145], 0\n\t" \
  "v_mfma_f32_4x4x4_16b_bf16 v[116:119], a[164:165], a[146:147], 0\n\t" \
  "v_mfma_f32_4x4x4_16b_bf16 v[120:123], a[202:203], a[182:183], 0\n\t" \
  "v_mfma_f32_4x4x4_16b_bf16 v[124:127], a[202:203], a[184:185], 0\n\t" \
  "v_mfma_f32_4x4x4_16b_bf16 v[128:131], a[240:241], a[220:221], 0\n\t" \
  "v_mfma_f32_4x4x4_16b_bf16 v[132:135], a[240:241], a[222:223], 0\n\t" \
  "v_mfma_f32_4x4x4_16b_bf16 v[112:115], a[166:167], a[146:147], v[112:115]\n\t" \
  "v_mfma_f32_4x4x4_16b_bf16 v[116:119], a[166:167], a[148:149], v[116:119]\n\t" \
  "v_mfma_f32_4x4x4_16b_bf16 v[120:123], a[204:205], a[184:185], v[120:123]\n\t" \
  "v_mfma_f32_4x4x4_16b_bf16 v[124:127], a[204:205], a[186:187], v[124:127]\n\t" \
  "v_mfma_f32_4x4x4_16b_bf16 v[128:131], a[242:243], a[222:223], v[128:131]\n\t" \
  "v_mfma_f32_4x4x4_16b_bf16 v[132:135], a[242:243], a[224:225], v[132:135]\n\t" \
  "v_mfma_f32_4x4x4_16b_bf16 v[112:115], a[168:169], a[148:149], v[112:115]\n\t" \
  "v_mfma_f32_4x4x4_16b_bf16 v[116:119], a[168:169], a[150:151], v[116:119]\n\t" \
  "v_mfma_f32_4x4x4_16b_bf16 v[120:123], a[206:207], a[186:187], v[120:123]\n\t" \
  "v_mfma_f32_4x4x4_16b_bf16 v[124:127], a[206:207], a[188:189], v[124:127]\n\t" \
  "v_mfma_f32_4x4x4_16b_bf16 v[128:131], a[244:245], a[224:225], v[128:131]\n\t" \
  "v_mfma_f32_4x4x4_16b_bf16 v[132:135], a[244:245], a[226:227], v[132:135]\n\t" \
  "v_mfma_f32_4x4x4_16b_bf16 v[112:115], a[170:171], a[150:151], v[112:115]\n\t" \
  "v_mfma_f32_4x4x4_16b_bf16 v[116:119], a[170:171], a[152:153], v[116:119]\n\t" \
  "v_mfma_f32_4x4x4_16b_bf16 v[120:123], a[208:209], a[188:189], v[120:123]\n\t" \
  "v_mfma_f32_4x4x4_16b_bf16 v[124:127], a[208:209], a[190:191], v[124:127]\n\t" \
  "v_mfma_f32_4x4x4_16b_bf16 v[128:131], a[246:247], a[226:227], v[128:131]\n\t" \
  "v_mfma_f32_4x4x4_16b_bf16 v[132:135], a[246:247], a[228:229], v[132:135]\n\t" \
  "v_mfma_f32_4x4x4_16b_bf16 v[112:115], a[172:173], a[152:153], v[112:115]\n\t" \
  "v_mfma_f32_4x4x4_16b_bf16 v[116:119], a[172:173], a[154:155], v[116:119]\n\t" \
  "v_mfma_f32_4x4x4_16b_bf16 v[120:123], a[210:211], a[190:191], v[120:123]\n\t" \
  "v_mfma_f32_4x4x4_16b_bf16 v[124:127], a[210:211], a[192:193], v[124:127]\n\t" \
  "v_mfma_f32_4x4x4_16b_bf16 v[128:131], a[248:249], a[228:229], v[128:131]\n\t" \
  "v_mfma_f32_4x4x4_16b_bf16 v[132:135], a[248:249], a[230:231], v[132:135]\n\t" \
  "v_mfma_f32_4x4x4_16b_bf16 v[112:115], a[174:175], a[154:155], v[112:115]\n\t" \
  "v_mfma_f32_4x4x4_16b_bf16 v[116:119], a[174:175], a[156:157], v[116:119]\n\t" \
  "v_mfma_f32_4x4x4_16b_bf16 v[120:123], a[212:213], a[192:193], v[120:123]\n\t" \
  "v_mfma_f32_4x4x4_16b_bf16 v[124:127], a[212:213], a[194:195], v[124:127]\n\t" \
  "v_mfma_f32_4x4x4_16b_bf16 v[128:131], a[250:251], a[230:231], v[128:131]\n\t" \
  "v_mfma_f32_4x4x4_16b_bf16 v[132:135], a[250:251], a[232:233], v[132:135]\n\t" \
  "v_mfma_f32_4x4x4_16b_bf16 v[112:115], a[176:177], a[156:157], v[112:115]\n\t" \
  "v_mfma_f32_4x4x4_16b_bf16 v[116:119], a[176:177], a[158:159], v[116:119]\n\t" \
  "v_mfma_f32_4x4x4_16b_bf16 v[120:123], a[214:215], a[194:195], v[120:123]\n\t" \
  "v_mfma_f32_4x4x4_16b_bf16 v[124:127], a[214:215], a[196:197], v[124:127]\n\t" \
  "v_mfma_f32_4x4x4_16b_bf16 v[128:131], a[252:253], a[232:233], v[128:131]\n\t" \
  "v_mfma_f32_4x4x4_16b_bf16 v[132:135], a[252:253], a[234:235], v[132:135]\n\t" \
  "v_mfma_f32_4x4x4_16b_bf16 v[112:115], a[178:179], a[158:159], v[112:115]\n\t" \
  "v_mfma_f32_4x4x4_16b_bf16 v[116:119], a[178:179], a[160:161], v[116:119]\n\t" \
  "v_mfma_f32_4x4x4_16b_bf16 v[120:123], a[216:217], a[196:197], v[120:123]\n\t" \
  "v_mfma_f32_4x4x4_16b_bf16 v[124:127], a[216:217], a[198:199], v[124:127]\n\t" \
  "v_mfma_f32_4x4x4_16b_bf16 v[128:131], a[254:255], a[234:235], v[128:131]\n\t" \
  "v_mfma_f32_4x4x4_16b_bf16 v[132:135], a[254:255], a[236:237], v[132:135]\n\t" \
  "v_mfma_f32_4x4x4_16b_bf16 v[112:115], a[180:181], a[160:161], v[112:115]\n\t" \
  "v_mfma_f32_4x4x4_16b_bf16 v[116:119], a[180:181], a[162:163], v[116:119]\n\t" \
  "v_mfma_f32_4x4x4_16b_bf16 v[120:123], a[218:219], a[198:199], v[120:123]\n\t" \
  "v_mfma_f32_4x4x4_16b_bf16 v[124:127], a[218:219], a[200:201], v[124:127]\n\t" \
  "v_mfma_f32_4x4x4_16b_bf16 v[128:131], v[46:47], a[236:237], v[128:131]\n\t" \
  "v_mfma_f32_4x4x4_16b_bf16 v[132:135], v[46:47], a[238:239], v[132:135]\n\t" \
  "v_cvt_pk_bf16_f32 v136, v112, v113\n\t" \
  "v_cvt_pk_bf16_f32 v137, v114, v115\n\t" \
  "v_cvt_pk_bf16_f32 v138, v116, v117\n\t" \
  "v_cvt_pk_bf16_f32 v139, v118, v119\n\t" \
  "v_cvt_pk_bf16_f32 v140, v120, v121\n\t" \
  "v_cvt_pk_bf16_f32 v141, v122, v123\n\t" \
  "v_cvt_pk_bf16_f32 v142, v124, v125\n\t" \
  "v_cvt_pk_bf16_f32 v143, v126, v127\n\t" \
  "v_cvt_pk_bf16_f32 v144, v128, v129\n\t" \
  "v_cvt_pk_bf16_f32 v145, v130, v131\n\t" \
  "v_cvt_pk_bf16_f32 v146, v132, v133\n\t" \
  "v_cvt_pk_bf16_f32 v147, v134, v135\n\t" \
  "KLA%=:\n\t" \
  "s_cmp_eq_u32 s47, 2\n\t" \
  "s_cbranch_scc0 KXA%=\n\t" \
  "v_mfma_f32_16x16x32_bf16 v[96:99], a[72:75], a[36:39], 0\n\t" \
  "ds_read_b64_tr_b16 a[120:121], v53 offset:2560\n\t" \
  "v_mfma_f32_16x16x32_bf16 v[100:103], a[88:91], a[36:39], 0\n\t" \
  "ds_read_b64_tr_b16 a[122:123], v53 offset:3584\n\t" \
  "v_mfma_f32_16x16x32_bf16 v[96:99], a[76:79], a[40:43], v[96:99]\n\t" \
  "ds_read_b64_tr_b16 a[124:125], v53 offset:4096\n\t" \
  "v_mfma_f32_16x16x32_bf16 v[100:103], a[92:95], a[40:43], v[100:103]\n\t" \
  "ds_read_b64_tr_b16 a[126:127], v53 offset:5120\n\t" \
  "v_mfma_f32_16x16x32_bf16 v[96:99], a[80:83], a[44:47], v[96:99]\n\t" \
  "ds_read_b64_tr_b16 a[128:129], v53 offset:4608\n\t" \
  "v_mfma_f32_16x16x32_bf16 v[100:103], a[96:99], a[44:47], v[100:103]\n\t" \
  "ds_read_b64_tr_b16 a[130:131], v53 offset:5632\n\t" \
  "v_mfma_f32_16x16x32_bf16 v[96:99], a[84:87], a[48:51], v[96:99]\n\t" \
  "ds_read_b64_tr_b16 a[132:133], v53 offset:6144\n\t" \
  "v_mfma_f32_16x16x32_bf16 v[100:103], a[100:103], a[48:51], v[100:103]\n\t" \
  "ds_read_b64_tr_b16 a[134:135], v53 offset:7168\n\t" \
  "v_mfma_f32_16x16x16_bf16 v[96:99], a[104:105], a[52:53], v[96:99]\n\t" \
  "ds_read_b64_tr_b16 a[136:137], v53 offset:6656\n\t" \
  "v_mfma_f32_16x16x16_bf16 v[100:103], a[106:107], a[52:53], v[100:103]\n\t" \
  "ds_read_b64_tr_b16 a[138:139], v53 offset:7680\n\t" \
  "v_mfma_f32_16x16x32_bf16 v[104:107], a[72:75], a[54:57], 0\n\t" \
  "ds_read_b64_tr_b16 a[140:141], v53 offset:8192\n\t" \
  "v_mfma_f32_16x16x32_bf16 v[108:111], a[88:91], a[54:57], 0\n\t" \
  "ds_read_b64_tr_b16 a[142:143], v53 offset:8704\n\t" \
  "v_mfma_f32_16x16x32_bf16 v[104:107], a[76:79], a[58:61], v[104:107]\n\t" \
  "v_exp_f32_e32 v96, v96\n\t" \
  "v_exp_f32_e32 v97, v97\n\t" \
  "v_mfma_f32_16x16x32_bf16 v[108:111], a[92:95], a[58:61], v[108:111]\n\t" \
  "v_exp_f32_e32 v98, v98\n\t" \
  "v_exp_f32_e32 v99, v99\n\t" \
  "v_cvt_pk_bf16_f32 v68, v96, v97\n\t" \
  "v_mfma_f32_16x16x32_bf16 v[104:107], a[80:83], a[62:65], v[104:107]\n\t" \
  "v_exp_f32_e32 v100, v100\n\t" \
  "v_cvt_pk_bf16_f32 v69, v98, v99\n\t" \
  "v_mfma_f32_16x16x32_bf16 v[108:111], a[96:99], a[62:65], v[108:111]\n\t" \
  "v_exp_f32_e32 v101, v101\n\t" \
  "v_exp_f32_e32 v102, v102\n\t" \
  "v_mfma_f32_16x16x32_bf16 v[104:107], a[84:87], a[66:69], v[104:107]\n\t" \
  "v_exp_f32_e32 v103, v103\n\t" \
  "v_cvt_pk_bf16_f32 v70, v100, v101\n\t" \
  "v_mfma_f32_16x16x32_bf16 v[108:111], a[100:103], a[66:69], v[108:111]\n\t" \
  "v_cvt_pk_bf16_f32 v71, v102, v103\n\t" \
  "v_mfma_f32_16x16x16_bf16 v[104:107], a[104:105], a[70:71], v[104:107]\n\t" \
  "v_mov_b32_e32 v53, v51\n\t" \
  "v_mov_b32_e32 v56, v54\n\t" \
  "v_mfma_f32_16x16x16_bf16 v[108:111], a[106:107], a[70:71], v[108:111]\n\t" \
  "v_mfma_f32_16x16x32_bf16 v[184:187], a[108:111], v[68:71], v[184:187]\n\t" \
  "ds_read_b128 a[72:75], v48 offset:0\n\t" \
  "v_mfma_f32_16x16x32_bf16 v[188:191], a[112:115], v[68:71], v[188:191]\n\t" \
  "ds_read_b128 a[88:91], v48 offset:1024\n\t" \
  "v_mfma_f32_16x16x32_bf16 v[192:195], a[116:119], v[68:71], v[192:195]\n\t" \
  "v_exp_f32_e32 v104, v104\n\t" \
  "v_exp_f32_e32 v105, v105\n\t" \
  "s_waitcnt lgkmcnt(2)\n\t" \
  "v_mfma_f32_16x16x32_bf16 v[196:199], a[120:123], v[68:71], v[196:199]\n\t" \
  "v_exp_f32_e32 v106, v106\n\t" \
  "v_exp_f32_e32 v107, v107\n\t" \
  "v_cvt_pk_bf16_f32 v64, v104, v105\n\t" \
  "v_mfma_f32_16x16x32_bf16 v[200:203], a[124:127], v[68:71], v[200:203]\n\t" \
  "v_exp_f32_e32 v108, v108\n\t" \
  "v_cvt_pk_bf16_f32 v65, v106, v107\n\t" \
  "v_mfma_f32_16x16x32_bf16 v[204:207], a[128:131], v[68:71], v[204:207]\n\t" \
  "v_exp_f32_e32 v109, v109\n\t" \
  "v_exp_f32_e32 v110, v110\n\t" \
  "v_mfma_f32_16x16x32_bf16 v[208:211], a[132:135], v[68:71], v[208:211]\n\t" \
  "v_exp_f32_e32 v111, v111\n\t" \
  "v_cvt_pk_bf16_f32 v66, v108, v109\n\t" \
  "v_mfma_f32_16x16x32_bf16 v[212:215], a[136:139], v[68:71], v[212:215]\n\t" \
  "v_cvt_pk_bf16_f32 v67, v110, v111\n\t" \
  "v_mfma_f32_16x16x32_bf16 v[216:219], a[140:143], v[68:71], v[216:219]\n\t" \
  "ds_read_b128 a[76:79], v48 offset:2048\n\t" \
  "v_mfma_f32_16x16x32_bf16 v[220:223], a[108:111], v[64:67], v[220:223]\n\t" \
  "ds_read_b128 a[92:95], v48 offset:3072\n\t" \
  "v_mfma_f32_16x16x32_bf16 v[224:227], a[112:115], v[64:67], v[224:227]\n\t" \
  "ds_read_b128 a[80:83], v48 offset:4096\n\t" \
  "v_mfma_f32_16x16x32_bf16 v[228:231], a[116:119], v[64:67], v[228:231]\n\t" \
  "ds_read_b128 a[96:99], v48 offset:5120\n\t" \
  "v_mfma_f32_16x16x32_bf16 v[232:235], a[120:123], v[64:67], v[232:235]\n\t" \
  "ds_read_b128 a[84:87], v48 offset:6144\n\t" \
  "ds_read_b64_tr_b16 a[108:109], v51 offset:0\n\t" \
  "v_mfma_f32_16x16x32_bf16 v[236:239], a[124:127], v[64:67], v[236:239]\n\t" \
  "ds_read_b128 a[100:103], v48 offset:7168\n\t" \
  "ds_read_b64_tr_b16 a[110:111], v51 offset:1024\n\t" \
  "v_mfma_f32_16x16x32_bf16 v[240:243], a[128:131], v[64:67], v[240:243]\n\t" \
  "ds_read2st64_b64 a[104:107], v54 offset0:16 offset1:17\n\t" \
  "ds_read_b64_tr_b16 a[112:113], v51 offset:512\n\t" \
  "v_mfma_f32_16x16x32_bf16 v[244:247], a[132:135], v[64:67], v[244:247]\n\t" \
  "ds_read_b64_tr_b16 a[114:115], v51 offset:1536\n\t" \
  "v_mfma_f32_16x16x32_bf16 v[248:251], a[136:139], v[64:67], v[248:251]\n\t" \
  "ds_read_b64_tr_b16 a[116:117], v51 offset:2048\n\t" \
  "v_mfma_f32_16x16x32_bf16 v[252:255], a[140:143], v[64:67], v[252:255]\n\t" \
  "ds_read_b64_tr_b16 a[118:119], v51 offset:3072\n\t" \
  "KXA%=:\n\t" \
  "s_waitcnt lgkmcnt(0)\n\t" \
  "v_mfma_f32_16x16x32_bf16 v[96:99], a[72:75], a[36:39], 0\n\t" \
  "ds_read_b64_tr_b16 a[120:121], v53 offset:2560\n\t" \
  "v_and_b32_e32 v174, 960, v59\n\t" \
  "v_and_or_b32 v174, v63, 24, v174\n\t" \
  "v_mfma_f32_16x16x32_bf16 v[100:103], a[88:91], a[36:39], 0\n\t" \
  "ds_read_b64_tr_b16 a[122:123], v53 offset:3584\n\t" \
  "s_sub_u32 s94, 1024, s90\n\t" \
  "v_mfma_f32_16x16x32_bf16 v[96:99], a[76:79], a[40:43], v[96:99]\n\t" \
  "ds_read_b64_tr_b16 a[124:125], v53 offset:4096\n\t" \
  "v_add_u32_e32 v166, s80, v174\n\t" \
  "v_mfma_f32_16x16x32_bf16 v[100:103], a[92:95], a[40:43], v[100:103]\n\t" \
  "ds_read_b64_tr_b16 a[126:127], v53 offset:5120\n\t" \
  "v_add_u32_e32 v167, s81, v174\n\t" \
  "v_mfma_f32_16x16x32_bf16 v[96:99], a[80:83], a[44:47], v[96:99]\n\t" \
  "ds_read_b64_tr_b16 a[128:129], v53 offset:4608\n\t" \
  "v_add_u32_e32 v168, s82, v174\n\t" \
  "v_mfma_f32_16x16x32_bf16 v[100:103], a[96:99], a[44:47], v[100:103]\n\t" \
  "ds_read_b64_tr_b16 a[130:131], v53 offset:5632\n\t" \
  "v_add_u32_e32 v169, s83, v174\n\t" \
  "v_mfma_f32_16x16x32_bf16 v[96:99], a[84:87], a[48:51], v[96:99]\n\t" \
  "ds_read_b64_tr_b16 a[132:133], v53 offset:6144\n\t" \
  "v_add_u32_e32 v174, s84, v174\n\t" \
  "s_waitcnt lgkmcnt(5)\n\t" \
  "v_mfma_f32_16x16x32_bf16 v[100:103], a[100:103], a[48:51], v[100:103]\n\t" \
  "ds_read_b64_tr_b16 a[134:135], v53 offset:7168\n\t" \
  "v_add_u32_e32 v170, s94, v166\n\t" \
  "v_add_u32_e32 v171, s94, v167\n\t" \
  "v_mfma_f32_16x16x16_bf16 v[96:99], a[104:105], a[52:53], v[96:99]\n\t" \
  "ds_read_b64_tr_b16 a[136:137], v53 offset:6656\n\t" \
  "v_add_u32_e32 v172, s94, v168\n\t" \
  "v_mfma_f32_16x16x16_bf16 v[100:103], a[106:107], a[52:53], v[100:103]\n\t" \
  "ds_read_b64_tr_b16 a[138:139], v53 offset:7680\n\t" \
  "v_add_u32_e32 v173, s94, v169\n\t" \
  "v_mfma_f32_16x16x32_bf16 v[104:107], a[72:75], a[54:57], 0\n\t" \
  "ds_read_b64_tr_b16 a[140:141], v53 offset:8192\n\t" \
  "s_add_u32 s98, s90, 159232\n\t" \
  "v_mfma_f32_16x16x32_bf16 v[108:111], a[88:91], a[54:57], 0\n\t" \
  "ds_read_b64_tr_b16 a[142:143], v53 offset:8704\n\t" \
  "s_sub_u32 s99, 159232, s90\n\t" \
  "v_mfma_f32_16x16x32_bf16 v[104:107], a[76:79], a[58:61], v[104:107]\n\t" \
  "v_exp_f32_e32 v96, v96\n\t" \
  "v_exp_f32_e32 v97, v97\n\t" \
  "v_mfma_f32_16x16x32_bf16 v[108:111], a[92:95], a[58:61], v[108:111]\n\t" \
  "v_exp_f32_e32 v98, v98\n\t" \
  "v_exp_f32_e32 v99, v99\n\t" \
  "v_cvt_pk_bf16_f32 v68, v96, v97\n\t" \
  "v_mfma_f32_16x16x32_bf16 v[104:107], a[80:83], a[62:65], v[104:107]\n\t" \
  "v_exp_f32_e32 v100, v100\n\t" \
  "v_cvt_pk_bf16_f32 v69, v98, v99\n\t" \
  "v_mfma_f32_16x16x32_bf16 v[108:111], a[96:99], a[62:65], v[108:111]\n\t" \
  "v_exp_f32_e32 v101, v101\n\t" \
  "v_exp_f32_e32 v102, v102\n\t" \
  "v_mfma_f32_16x16x32_bf16 v[104:107], a[84:87], a[66:69], v[104:107]\n\t" \
  "v_exp_f32_e32 v103, v103\n\t" \
  "v_cvt_pk_bf16_f32 v70, v100, v101\n\t" \
  "v_mfma_f32_16x16x32_bf16 v[108:111], a[100:103], a[66:69], v[108:111]\n\t" \
  "v_cvt_pk_bf16_f32 v71, v102, v103\n\t" \
  "v_mfma_f32_16x16x16_bf16 v[104:107], a[104:105], a[70:71], v[104:107]\n\t" \
  "v_add_u32_e32 v181, s98, v59\n\t" \
  "v_add_u32_e32 v182, s99, v59\n\t" \
  "v_mfma_f32_16x16x16_bf16 v[108:111], a[106:107], a[70:71], v[108:111]\n\t" \
  "v_add_u32_e32 v183, 159232, v63\n\t" \
  "s_sub_u32 s98, 36864, s85\n\t" \
  "s_add_u32 s99, s85, 18432\n\t" \
  "v_mfma_f32_16x16x32_bf16 v[184:187], a[108:111], v[68:71], v[184:187]\n\t" \
  "v_subrev_u32_e32 v178, s98, v181\n\t" \
  "v_subrev_u32_e32 v175, s99, v181\n\t" \
  "v_mfma_f32_16x16x32_bf16 v[188:191], a[112:115], v[68:71], v[188:191]\n\t" \
  "v_subrev_u32_e32 v179, s98, v182\n\t" \
  "v_subrev_u32_e32 v176, s99, v182\n\t" \
  "v_mfma_f32_16x16x32_bf16 v[192:195], a[116:119], v[68:71], v[192:195]\n\t" \
  "v_exp_f32_e32 v104, v104\n\t" \
  "v_exp_f32_e32 v105, v105\n\t" \
  "v_mfma_f32_16x16x32_bf16 v[196:199], a[120:123], v[68:71], v[196:199]\n\t" \
  "v_exp_f32_e32 v106, v106\n\t" \
  "v_exp_f32_e32 v107, v107\n\t" \
  "v_cvt_pk_bf16_f32 v64, v104, v105\n\t" \
  "s_waitcnt lgkmcnt(0)\n\t" \
  "v_mfma_f32_16x16x32_bf16 v[200:203], a[124:127], v[68:71], v[200:203]\n\t" \
  "v_exp_f32_e32 v108, v108\n\t" \
  "v_cvt_pk_bf16_f32 v65, v106, v107\n\t" \
  "v_mfma_f32_16x16x32_bf16 v[204:207], a[128:131], v[68:71], v[204:207]\n\t" \
  "v_exp_f32_e32 v109, v109\n\t" \
  "v_exp_f32_e32 v110, v110\n\t" \
  "v_mfma_f32_16x16x32_bf16 v[208:211], a[132:135], v[68:71], v[208:211]\n\t" \
  "v_exp_f32_e32 v111, v111\n\t" \
  "v_cvt_pk_bf16_f32 v66, v108, v109\n\t" \
  "v_mfma_f32_16x16x32_bf16 v[212:215], a[136:139], v[68:71], v[212:215]\n\t" \
  "v_cvt_pk_bf16_f32 v67, v110, v111\n\t" \
  "v_mfma_f32_16x16x32_bf16 v[216:219], a[140:143], v[68:71], v[216:219]\n\t" \
  "v_subrev_u32_e32 v180, s98, v183\n\t" \
  "v_subrev_u32_e32 v177, s99, v183\n\t" \
  "v_mfma_f32_16x16x32_bf16 v[220:223], a[108:111], v[64:67], v[220:223]\n\t" \
  "v_cvt_pk_bf16_f32 v88, v184, v185\n\t" \
  "v_cvt_pk_bf16_f32 v89, v186, v187\n\t" \
  "v_cvt_pk_bf16_f32 v90, v188, v189\n\t" \
  "v_mfma_f32_16x16x32_bf16 v[224:227], a[112:115], v[64:67], v[224:227]\n\t" \
  "v_cvt_pk_bf16_f32 v91, v190, v191\n\t" \
  "ds_write_b128 v34, v[88:91] offset:36864\n\t" \
  "v_cvt_pk_bf16_f32 v92, v192, v193\n\t" \
  "v_mfma_f32_16x16x32_bf16 v[228:231], a[116:119], v[64:67], v[228:231]\n\t" \
  "v_cvt_pk_bf16_f32 v93, v194, v195\n\t" \
  "v_cvt_pk_bf16_f32 v94, v196, v197\n\t" \
  "v_mfma_f32_16x16x32_bf16 v[232:235], a[120:123], v[64:67], v[232:235]\n\t" \
  "v_cvt_pk_bf16_f32 v95, v198, v199\n\t" \
  "ds_write_b128 v34, v[92:95] offset:37888\n\t" \
  "v_cvt_pk_bf16_f32 v80, v200, v201\n\t" \
  "v_mfma_f32_16x16x32_bf16 v[236:239], a[124:127], v[64:67], v[236:239]\n\t" \
  "v_cvt_pk_bf16_f32 v81, v202, v203\n\t" \
  "v_cvt_pk_bf16_f32 v82, v204, v205\n\t" \
  "v_mfma_f32_16x16x32_bf16 v[240:243], a[128:131], v[64:67], v[240:243]\n\t" \
  "v_cvt_pk_bf16_f32 v83, v206, v207\n\t" \
  "ds_write_b128 v34, v[80:83] offset:38912\n\t" \
  "v_cvt_pk_bf16_f32 v84, v208, v209\n\t" \
  "v_mfma_f32_16x16x32_bf16 v[244:247], a[132:135], v[64:67], v[244:247]\n\t" \
  "v_cvt_pk_bf16_f32 v85, v210, v211\n\t" \
  "v_cvt_pk_bf16_f32 v86, v212, v213\n\t" \
  "v_mfma_f32_16x16x32_bf16 v[248:251], a[136:139], v[64:67], v[248:251]\n\t" \
  "v_cvt_pk_bf16_f32 v87, v214, v215\n\t" \
  "ds_write_b128 v34, v[84:87] offset:39936\n\t" \
  "v_cvt_pk_bf16_f32 v88, v216, v217\n\t" \
  "v_mfma_f32_16x16x32_bf16 v[252:255], a[140:143], v[64:67], v[252:255]\n\t" \
  "v_cvt_pk_bf16_f32 v89, v218, v219\n\t" \
  "ds_write_b64 v34, v[88:89] offset:45056\n\t" \
  "s_cmp_eq_u32 s47, 0\n\t" \
  "s_cbranch_scc0 SB%=\n\t" \
  "v_cvt_pk_bf16_f32 v92, v220, v221\n\t" \
  "v_cvt_pk_bf16_f32 v93, v222, v223\n\t" \
  "v_cvt_pk_bf16_f32 v94, v224, v225\n\t" \
  "v_cvt_pk_bf16_f32 v95, v226, v227\n\t" \
  "ds_write_b128 v34, v[92:95] offset:40960\n\t" \
  "v_cvt_pk_bf16_f32 v80, v228, v229\n\t" \
  "v_cvt_pk_bf16_f32 v81, v230, v231\n\t" \
  "v_cvt_pk_bf16_f32 v82, v232, v233\n\t" \
  "v_cvt_pk_bf16_f32 v83, v234, v235\n\t" \
  "ds_write_b128 v34, v[80:83] offset:41984\n\t" \
  "v_cvt_pk_bf16_f32 v84, v236, v237\n\t" \
  "v_cvt_pk_bf16_f32 v85, v238, v239\n\t" \
  "v_cvt_pk_bf16_f32 v86, v240, v241\n\t" \
  "v_cvt_pk_bf16_f32 v87, v242, v243\n\t" \
  "ds_write_b128 v34, v[84:87] offset:43008\n\t" \
  "v_cvt_pk_bf16_f32 v88, v244, v245\n\t" \
  "v_cvt_pk_bf16_f32 v89, v246, v247\n\t" \
  "v_cvt_pk_bf16_f32 v90, v248, v249\n\t" \
  "v_cvt_pk_bf16_f32 v91, v250, v251\n\t" \
  "ds_write_b128 v34, v[88:91] offset:44032\n\t" \
  "v_cvt_pk_bf16_f32 v92, v252, v253\n\t" \
  "v_cvt_pk_bf16_f32 v93, v254, v255\n\t" \
  "ds_write_b64 v34, v[92:93] offset:45064\n\t" \
  "SB%=:\n\t" \
  "v_add_u32_e32 v204, s85, v59\n\t" \
  "v_add_u32_e32 v205, s85, v63\n\t" \
  "v_add_u32_e32 v206, s86, v59\n\t" \
  "s_cmp_eq_u32 s47, 1\n\t" \
  "s_cbranch_scc1 NKLa%=\n\t" \
  "v_mfma_f32_4x4x4_16b_bf16 v[112:115], a[164:165], a[144:145], 0\n\t" \
  "v_mfma_f32_4x4x4_16b_bf16 v[116:119], a[164:165], a[146:147], 0\n\t" \
  "v_mfma_f32_4x4x4_16b_bf16 v[120:123], a[202:203], a[182:183], 0\n\t" \
  "v_mfma_f32_4x4x4_16b_bf16 v[124:127], a[202:203], a[184:185], 0\n\t" \
  "v_mfma_f32_4x4x4_16b_bf16 v[112:115], a[166:167], a[146:147], v[112:115]\n\t" \
  "v_mfma_f32_4x4x4_16b_bf16 v[116:119], a[166:167], a[148:149], v[116:119]\n\t" \
  "v_mfma_f32_4x4x4_16b_bf16 v[120:123], a[204:205], a[184:185], v[120:123]\n\t" \
  "v_mfma_f32_4x4x4_16b_bf16 v[124:127], a[204:205], a[186:187], v[124:127]\n\t" \
  "v_mfma_f32_4x4x4_16b_bf16 v[112:115], a[168:169], a[148:149], v[112:115]\n\t" \
  "v_mfma_f32_4x4x4_16b_bf16 v[116:119], a[168:169], a[150:151], v[116:119]\n\t" \
  "v_mfma_f32_4x4x4_16b_bf16 v[120:123], a[206:207], a[186:187], v[120:123]\n\t" \
  "v_mfma_f32_4x4x4_16b_bf16 v[124:127], a[206:207], a[188:189], v[124:127]\n\t" \
  "v_mfma_f32_4x4x4_16b_bf16 v[112:115], a[170:171], a[150:151], v[112:115]\n\t" \
  "v_mfma_f32_4x4x4_16b_bf16 v[116:119], a[170:171], a[152:153], v[116:119]\n\t" \
  "v_mfma_f32_4x4x4_16b_bf16 v[120:123], a[208:209], a[188:189], v[120:123]\n\t" \
  "v_mfma_f32_4x4x4_16b_bf16 v[124:127], a[208:209], a[190:191], v[124:127]\n\t" \
  "v_mfma_f32_4x4x4_16b_bf16 v[112:115], a[172:173], a[152:153], v[112:115]\n\t" \
  "v_mfma_f32_4x4x4_16b_bf16 v[116:119], a[172:173], a[154:155], v[116:119]\n\t" \
  "v_mfma_f32_4x4x4_16b_bf16 v[120:123], a[210:211], a[190:191], v[120:123]\n\t" \
  "v_mfma_f32_4x4x4_16b_bf16 v[124:127], a[210:211], a[192:193], v[124:127]\n\t" \
  "v_mfma_f32_4x4x4_16b_bf16 v[112:115], a[174:175], a[154:155], v[112:115]\n\t" \
  "v_mfma_f32_4x4x4_16b_bf16 v[116:119], a[174:175], a[156:157], v[116:119]\n\t" \
  "v_mfma_f32_4x4x4_16b_bf16 v[120:123], a[212:213], a[192:193], v[120:123]\n\t" \
  "v_mfma_f32_4x4x4_16b_bf16 v[124:127], a[212:213], a[194:195], v[124:127]\n\t" \
  "v_mfma_f32_4x4x4_16b_bf16 v[112:115], a[176:177], a[156:157], v[112:115]\n\t" \
  "v_mfma_f32_4x4x4_16b_bf16 v[116:119], a[176:177], a[158:159], v[116:119]\n\t" \
  "v_mfma_f32_4x4x4_16b_bf16 v[120:123], a[214:215], a[194:195], v[120:123]\n\t" \
  "v_mfma_f32_4x4x4_16b_bf16 v[124:127], a[214:215], a[196:197], v[124:127]\n\t" \
  "v_mfma_f32_4x4x4_16b_bf16 v[112:115], a[178:179], a[158:159], v[112:115]\n\t" \
  "v_mfma_f32_4x4x4_16b_bf16 v[116:119], a[178:179], a[160:161], v[116:119]\n\t" \
  "v_mfma_f32_4x4x4_16b_bf16 v[120:123], a[216:217], a[196:197], v[120:123]\n\t" \
  "v_mfma_f32_4x4x4_16b_bf16 v[124:127], a[216:217], a[198:199], v[124:127]\n\t" \
  "v_mfma_f32_4x4x4_16b_bf16 v[112:115], a[180:181], a[160:161], v[112:115]\n\t" \
  "v_mfma_f32_4x4x4_16b_bf16 v[116:119], a[180:181], a[162:163], v[116:119]\n\t" \
  "v_mfma_f32_4x4x4_16b_bf16 v[120:123], a[218:219], a[198:199], v[120:123]\n\t" \
  "v_mfma_f32_4x4x4_16b_bf16 v[124:127], a[218:219], a[200:201], v[124:127]\n\t" \
  "NKLa%=:\n\t" \
  "s_cbranch_scc0 KLs%=\n\t" \
  "s_sleep 7\n\t" \
  "KLs%=:\n\t" \
  "s_cmp_eq_u32 s47, 0\n\t" \
  "s_cbranch_scc1 KB%=\n\t" \
  "buffer_load_dwordx4 v[166:169], v59, s[16:19], s22 offen offset:0 sc1\n\t" \
  "buffer_load_dwordx4 v[170:173], v59, s[16:19], s22 offen offset:1024 sc1\n\t" \
  "buffer_load_dwordx4 v[174:177], v59, s[16:19], s22 offen offset:2048 sc1\n\t" \
  "buffer_load_dwordx4 v[178:181], v59, s[16:19], s22 offen offset:3072 sc1\n\t" \
  "buffer_load_dwordx4 v[182:185], v59, s[16:19], s23 offen offset:0 sc1\n\t" \
  "buffer_load_dwordx4 v[186:189], v59, s[16:19], s23 offen offset:1024 sc1\n\t" \
  "buffer_load_dwordx4 v[190:193], v59, s[16:19], s23 offen offset:2048 sc1\n\t" \
  "buffer_load_dwordx4 v[194:197], v59, s[16:19], s23 offen offset:3072 sc1\n\t" \
  "buffer_load_dwordx4 v[198:201], v59, s[16:19], s26 offen offset:0 sc1\n\t" \
  "KB%=:\n\t" \
  "s_cmp_eq_u32 s47, 1\n\t" \
  "s_cbranch_scc1 NKL%=\n\t" \
  "v_cvt_pk_bf16_f32 v136, v112, v113\n\t" \
  "v_cvt_pk_bf16_f32 v137, v114, v115\n\t" \
  "v_cvt_pk_bf16_f32 v138, v116, v117\n\t" \
  "v_cvt_pk_bf16_f32 v139, v118, v119\n\t" \
  "v_cvt_pk_bf16_f32 v140, v120, v121\n\t" \
  "v_cvt_pk_bf16_f32 v141, v122, v123\n\t" \
  "v_cvt_pk_bf16_f32 v142, v124, v125\n\t" \
  "v_cvt_pk_bf16_f32 v143, v126, v127\n\t" \
  "NKL%=:\n\t" \
  "s_cbranch_scc0 KL3%=\n\t" \
  "ds_write_b128 v15, v[144:147] offset:2048\n\t" \
  "KL3%=:\n\t" \
  "ds_write_b128 v15, v[136:139] offset:0\n\t" \
  "ds_write_b128 v15, v[140:143] offset:1024\n\t" \
  "s_waitcnt lgkmcnt(0)\n\t" \
  "s_barrier\n\t" \
  "s_cmp_eq_u32 s47, 0\n\t" \
  "s_cbranch_scc0 SC%=\n\t" \
  "ds_read_b64_tr_b16 v[130:131], v166\n\t" \
  "ds_read_b64_tr_b16 v[132:133], v167\n\t" \
  "ds_read_b64_tr_b16 v[134:135], v168\n\t" \
  "ds_read_b64_tr_b16 v[136:137], v169\n\t" \
  "ds_read_b64_tr_b16 v[138:139], v170\n\t" \
  "ds_read_b64_tr_b16 v[140:141], v171\n\t" \
  "ds_read_b64_tr_b16 v[142:143], v172\n\t" \
  "ds_read_b64_tr_b16 v[144:145], v173\n\t" \
  "ds_read_b64_tr_b16 v[146:147], v174\n\t" \
  "v_mfma_f32_16x16x16_bf16 v[80:83], v[30:31], v[22:23], 0\n\t" \
  "v_mfma_f32_16x16x16_bf16 v[84:87], v[30:31], v[24:25], 0\n\t" \
  "v_mfma_f32_16x16x16_bf16 v[88:91], v[30:31], v[26:27], 0\n\t" \
  "v_mfma_f32_16x16x16_bf16 v[92:95], v[30:31], v[28:29], 0\n\t" \
  "v_mfma_f32_16x16x16_bf16 v[96:99], v[30:31], v[60:61], 0\n\t" \
  "ds_read_b128 a[0:3], v175 offset:0\n\t" \
  "ds_read_b128 a[18:21], v175 offset:4608\n\t" \
  "ds_read_b128 a[36:39], v175 offset:9216\n\t" \
  "ds_read_b128 a[54:57], v175 offset:13824\n\t" \
  "ds_read_b128 a[72:75], v178 offset:0\n\t" \
  "ds_read_b128 a[90:93], v178 offset:4608\n\t" \
  "ds_read_b128 a[108:111], v178 offset:9216\n\t" \
  "ds_read_b128 a[126:129], v178 offset:13824\n\t" \
  "ds_read_b128 a[144:147], v181 offset:0\n\t" \
  "v_lshlrev_b32_e32 v184, 16, v36\n\t" \
  "v_and_b32_e32 v185, 0xffff0000, v36\n\t" \
  "s_waitcnt lgkmcnt(9)\n\t" \
  "v_lshlrev_b32_e32 v186, 16, v130\n\t" \
  "v_and_b32_e32 v187, 0xffff0000, v130\n\t" \
  "v_pk_mul_f32 v[184:185], v[184:185], v[186:187]\n\t" \
  "v_cvt_pk_bf16_f32 v148, v184, v185\n\t" \
  "v_lshlrev_b32_e32 v188, 16, v37\n\t" \
  "v_and_b32_e32 v189, 0xffff0000, v37\n\t" \
  "v_lshlrev_b32_e32 v190, 16, v131\n\t" \
  "v_and_b32_e32 v191, 0xffff0000, v131\n\t" \
  "v_pk_mul_f32 v[188:189], v[188:189], v[190:191]\n\t" \
  "v_cvt_pk_bf16_f32 v149, v188, v189\n\t" \
  "v_lshlrev_b32_e32 v184, 16, v38\n\t" \
  "v_and_b32_e32 v185, 0xffff0000, v38\n\t" \
  "v_lshlrev_b32_e32 v186, 16, v132\n\t" \
  "v_and_b32_e32 v187, 0xffff0000, v132\n\t" \
  "v_pk_mul_f32 v[184:185], v[184:185], v[186:187]\n\t" \
  "v_cvt_pk_bf16_f32 v150, v184, v185\n\t" \
  "v_lshlrev_b32_e32 v188, 16, v39\n\t" \
  "v_and_b32_e32 v189, 0xffff0000, v39\n\t" \
  "v_lshlrev_b32_e32 v190, 16, v133\n\t" \
  "v_and_b32_e32 v191, 0xffff0000, v133\n\t" \
  "v_pk_mul_f32 v[188:189], v[188:189], v[190:191]\n\t" \
  "v_cvt_pk_bf16_f32 v151, v188, v189\n\t" \
  "v_lshlrev_b32_e32 v184, 16, v40\n\t" \
  "v_and_b32_e32 v185, 0xffff0000, v40\n\t" \
  "v_lshlrev_b32_e32 v186, 16, v134\n\t" \
  "v_and_b32_e32 v187, 0xffff0000, v134\n\t" \
  "v_pk_mul_f32 v[184:185], v[184:185], v[186:187]\n\t" \
  "v_cvt_pk_bf16_f32 v152, v184, v185\n\t" \
  "v_lshlrev_b32_e32 v188, 16, v41\n\t" \
  "v_and_b32_e32 v189, 0xffff0000, v41\n\t" \
  "v_lshlrev_b32_e32 v190, 16, v135\n\t" \
  "v_and_b32_e32 v191, 0xffff0000, v135\n\t" \
  "v_pk_mul_f32 v[188:189], v[188:189], v[190:191]\n\t" \
  "v_cvt_pk_bf16_f32 v153, v188, v189\n\t" \
  "v_lshlrev_b32_e32 v184, 16, v42\n\t" \
  "v_and_b32_e32 v185, 0xffff0000, v42\n\t" \
  "v_lshlrev_b32_e32 v186, 16, v136\n\t" \
  "v_and_b32_e32 v187, 0xffff0000, v136\n\t" \
  "v_pk_mul_f32 v[184:185], v[184:185], v[186:187]\n\t" \
  "v_cvt_pk_bf16_f32 v154, v184, v185\n\t" \
  "v_lshlrev_b32_e32 v188, 16, v43\n\t" \
  "v_and_b32_e32 v189, 0xffff0000, v43\n\t" \
  "v_lshlrev_b32_e32 v190, 16, v137\n\t" \
  "v_and_b32_e32 v191, 0xffff0000, v137\n\t" \
  "v_pk_mul_f32 v[188:189], v[188:189], v[190:191]\n\t" \
  "v_cvt_pk_bf16_f32 v155, v188, v189\n\t" \
  "ds_read_b128 a[4:7], v175 offset:1024\n\t" \
  "ds_read_b128 a[22:25], v175 offset:5632\n\t" \
  "ds_read_b128 a[40:43], v175 offset:10240\n\t" \
  "ds_read_b128 a[58:61], v175 offset:14848\n\t" \
  "ds_read_b128 a[76:79], v178 offset:1024\n\t" \
  "ds_read_b128 a[94:97], v178 offset:5632\n\t" \
  "ds_read_b128 a[112:115], v178 offset:10240\n\t" \
  "ds_read_b128 a[130:133], v178 offset:14848\n\t" \
  "ds_read_b128 a[148:151], v181 offset:1024\n\t" \
  "s_waitcnt lgkmcnt(9)\n\t" \
  "v_mfma_f32_16x16x32_bf16 v[64:67], a[0:3], v[148:151], 0\n\t" \
  "ds_read_b128 a[8:11], v176 offset:2048\n\t" \
  "v_mfma_f32_16x16x32_bf16 v[68:71], a[18:21], v[148:151], 0\n\t" \
  "ds_read_b128 a[26:29], v176 offset:6656\n\t" \
  "v_mfma_f32_16x16x32_bf16 v[72:75], a[36:39], v[148:151], 0\n\t" \
  "ds_read_b128 a[44:47], v176 offset:11264\n\t" \
  "v_mfma_f32_16x16x32_bf16 v[76:79], a[54:57], v[148:151], 0\n\t" \
  "ds_read_b128 a[62:65], v176 offset:15872\n\t" \
  "v_mfma_f32_16x16x32_bf16 v[80:83], a[72:75], v[148:151], v[80:83]\n\t" \
  "ds_read_b128 a[80:83], v179 offset:2048\n\t" \
  "v_mfma_f32_16x16x32_bf16 v[84:87], a[90:93], v[148:151], v[84:87]\n\t" \
  "ds_read_b128 a[98:101], v179 offset:6656\n\t" \
  "v_mfma_f32_16x16x32_bf16 v[88:91], a[108:111], v[148:151], v[88:91]\n\t" \
  "ds_read_b128 a[116:119], v179 offset:11264\n\t" \
  "v_mfma_f32_16x16x32_bf16 v[92:95], a[126:129], v[148:151], v[92:95]\n\t" \
  "ds_read_b128 a[134:137], v179 offset:15872\n\t" \
  "v_mfma_f32_16x16x32_bf16 v[96:99], a[144:147], v[148:151], v[96:99]\n\t" \
  "ds_read_b128 a[152:155], v182 offset:2048\n\t" \
  "s_waitcnt lgkmcnt(7)\n\t" \
  "v_mfma_f32_16x16x32_bf16 v[64:67], a[4:7], v[152:155], v[64:67]\n\t" \
  "ds_read_b128 a[12:15], v176 offset:3072\n\t" \
  "v_lshlrev_b32_e32 v184, 16, v4\n\t" \
  "v_and_b32_e32 v185, 0xffff0000, v4\n\t" \
  "v_lshlrev_b32_e32 v186, 16, v138\n\t" \
  "v_and_b32_e32 v187, 0xffff0000, v138\n\t" \
  "v_mfma_f32_16x16x32_bf16 v[68:71], a[22:25], v[152:155], v[68:71]\n\t" \
  "ds_read_b128 a[30:33], v176 offset:7680\n\t" \
  "v_mul_f32_e32 v184, v184, v186\n\t" \
  "v_mul_f32_e32 v185, v185, v187\n\t" \
  "v_cvt_pk_bf16_f32 v156, v184, v185\n\t" \
  "v_lshlrev_b32_e32 v188, 16, v5\n\t" \
  "v_mfma_f32_16x16x32_bf16 v[72:75], a[40:43], v[152:155], v[72:75]\n\t" \
  "ds_read_b128 a[48:51], v176 offset:12288\n\t" \
  "v_and_b32_e32 v189, 0xffff0000, v5\n\t" \
  "v_lshlrev_b32_e32 v190, 16, v139\n\t" \
  "v_and_b32_e32 v191, 0xffff0000, v139\n\t" \
  "v_mul_f32_e32 v188, v188, v190\n\t" \
  "v_mfma_f32_16x16x32_bf16 v[76:79], a[58:61], v[152:155], v[76:79]\n\t" \
  "ds_read_b128 a[66:69], v176 offset:16896\n\t" \
  "v_mul_f32_e32 v189, v189, v191\n\t" \
  "v_cvt_pk_bf16_f32 v157, v188, v189\n\t" \
  "v_lshlrev_b32_e32 v184, 16, v6\n\t" \
  "v_and_b32_e32 v185, 0xffff0000, v6\n\t" \
  "v_mfma_f32_16x16x32_bf16 v[80:83], a[76:79], v[152:155], v[80:83]\n\t" \
  "ds_read_b128 a[84:87], v179 offset:3072\n\t" \
  "v_lshlrev_b32_e32 v186, 16, v140\n\t" \
  "v_and_b32_e32 v187, 0xffff0000, v140\n\t" \
  "v_mul_f32_e32 v184, v184, v186\n\t" \
  "v_mul_f32_e32 v185, v185, v187\n\t" \
  "v_mfma_f32_16x16x32_bf16 v[84:87], a[94:97], v[152:155], v[84:87]\n\t" \
  "ds_read_b128 a[102:105], v179 offset:7680\n\t" \
  "v_cvt_pk_bf16_f32 v158, v184, v185\n\t" \
  "v_lshlrev_b32_e32 v188, 16, v7\n\t" \
  "v_and_b32_e32 v189, 0xffff0000, v7\n\t" \
  "v_lshlrev_b32_e32 v190, 16, v141\n\t" \
  "v_mfma_f32_16x16x32_bf16 v[88:91], a[112:115], v[152:155], v[88:91]\n\t" \
  "ds_read_b128 a[120:123], v179 offset:12288\n\t" \
  "v_and_b32_e32 v191, 0xffff0000, v141\n\t" \
  "v_mul_f32_e32 v188, v188, v190\n\t" \
  "v_mul_f32_e32 v189, v189, v191\n\t" \
  "v_cvt_pk_bf16_f32 v159, v188, v189\n\t" \
  "v_mfma_f32_16x16x32_bf16 v[92:95], a[130:133], v[152:155], v[92:95]\n\t" \
  "ds_read_b128 a[138:141], v179 offset:16896\n\t" \
  "v_mfma_f32_16x16x32_bf16 v[96:99], a[148:151], v[152:155], v[96:99]\n\t" \
  "ds_read_b128 a[156:159], v182 offset:3072\n\t" \
  "v_mfma_f32_16x16x32_bf16 v[64:67], a[8:11], v[156:159], v[64:67]\n\t" \
  "ds_read_b64 a[16:17], v177 offset:4096\n\t" \
  "v_lshlrev_b32_e32 v184, 16, v8\n\t" \
  "v_and_b32_e32 v185, 0xffff0000, v8\n\t" \
  "v_lshlrev_b32_e32 v186, 16, v142\n\t" \
  "v_and_b32_e32 v187, 0xffff0000, v142\n\t" \
  "v_mfma_f32_16x16x32_bf16 v[68:71], a[26:29], v[156:159], v[68:71]\n\t" \
  "ds_read_b64 a[34:35], v177 offset:8704\n\t" \
  "v_mul_f32_e32 v184, v184, v186\n\t" \
  "v_mul_f32_e32 v185, v185, v187\n\t" \
  "v_cvt_pk_bf16_f32 v160, v184, v185\n\t" \
  "v_lshlrev_b32_e32 v188, 16, v9\n\t" \
  "s_waitcnt lgkmcnt(3)\n\t" \
  "v_mfma_f32_16x16x32_bf16 v[72:75], a[44:47], v[156:159], v[72:75]\n\t" \
  "ds_read_b64 a[52:53], v177 offset:13312\n\t" \
  "v_and_b32_e32 v189, 0xffff0000, v9\n\t" \
  "v_lshlrev_b32_e32 v190, 16, v143\n\t" \
  "v_and_b32_e32 v191, 0xffff0000, v143\n\t" \
  "v_mul_f32_e32 v188, v188, v190\n\t" \
  "v_mfma_f32_16x16x32_bf16 v[76:79], a[62:65], v[156:159], v[76:79]\n\t" \
  "ds_read_b64 a[70:71], v177 offset:17920\n\t" \
  "v_mul_f32_e32 v189, v189, v191\n\t" \
  "v_cvt_pk_bf16_f32 v161, v188, v189\n\t" \
  "v_lshlrev_b32_e32 v184, 16, v10\n\t" \
  "v_and_b32_e32 v185, 0xffff0000, v10\n\t" \
  "v_mfma_f32_16x16x32_bf16 v[80:83], a[80:83], v[156:159], v[80:83]\n\t" \
  "ds_read_b64 a[88:89], v180 offset:4096\n\t" \
  "v_lshlrev_b32_e32 v186, 16, v144\n\t" \
  "v_and_b32_e32 v187, 0xffff0000, v144\n\t" \
  "v_mul_f32_e32 v184, v184, v186\n\t" \
  "v_mul_f32_e32 v185, v185, v187\n\t" \
  "v_mfma_f32_16x16x32_bf16 v[84:87], a[98:101], v[156:159], v[84:87]\n\t" \
  "ds_read_b64 a[106:107], v180 offset:8704\n\t" \
  "v_cvt_pk_bf16_f32 v162, v184, v185\n\t" \
  "v_lshlrev_b32_e32 v188, 16, v11\n\t" \
  "v_and_b32_e32 v189, 0xffff0000, v11\n\t" \
  "v_lshlrev_b32_e32 v190, 16, v145\n\t" \
  "v_mfma_f32_16x16x32_bf16 v[88:91], a[116:119], v[156:159], v[88:91]\n\t" \
  "ds_read_b64 a[124:125], v180 offset:13312\n\t" \
  "v_and_b32_e32 v191, 0xffff0000, v145\n\t" \
  "v_mul_f32_e32 v188, v188, v190\n\t" \
  "v_mul_f32_e32 v189, v189, v191\n\t" \
  "v_cvt_pk_bf16_f32 v163, v188, v189\n\t" \
  "v_mfma_f32_16x16x32_bf16 v[92:95], a[134:137], v[156:159], v[92:95]\n\t" \
  "ds_read_b64 a[142:143], v180 offset:17920\n\t" \
  "v_mfma_f32_16x16x32_bf16 v[96:99], a[152:155], v[156:159], v[96:99]\n\t" \
  "ds_read_b64 a[160:161], v183 offset:4096\n\t" \
  "v_mfma_f32_16x16x32_bf16 v[64:67], a[12:15], v[160:163], v[64:67]\n\t" \
  "ds_read_b128 a[162:165], v204 offset:0\n\t" \
  "v_lshlrev_b32_e32 v184, 16, v44\n\t" \
  "v_and_b32_e32 v185, 0xffff0000, v44\n\t" \
  "v_mfma_f32_16x16x32_bf16 v[68:71], a[30:33], v[160:163], v[68:71]\n\t" \
  "ds_read_b128 a[180:183], v204 offset:4608\n\t" \
  "v_lshlrev_b32_e32 v186, 16, v146\n\t" \
  "v_and_b32_e32 v187, 0xffff0000, v146\n\t" \
  "v_mfma_f32_16x16x32_bf16 v[72:75], a[48:51], v[160:163], v[72:75]\n\t" \
  "ds_read_b128 a[166:169], v204 offset:1024\n\t" \
  "v_mul_f32_e32 v184, v184, v186\n\t" \
  "v_mul_f32_e32 v185, v185, v187\n\t" \
  "v_mfma_f32_16x16x32_bf16 v[76:79], a[66:69], v[160:163], v[76:79]\n\t" \
  "ds_read_b128 a[184:187], v204 offset:5632\n\t" \
  "v_cvt_pk_bf16_f32 v164, v184, v185\n\t" \
  "v_lshlrev_b32_e32 v188, 16, v45\n\t" \
  "v_mfma_f32_16x16x32_bf16 v[80:83], a[84:87], v[160:163], v[80:83]\n\t" \
  "ds_read_b128 a[170:173], v204 offset:2048\n\t" \
  "v_and_b32_e32 v189, 0xffff0000, v45\n\t" \
  "v_lshlrev_b32_e32 v190, 16, v147\n\t" \
  "v_mfma_f32_16x16x32_bf16 v[84:87], a[102:105], v[160:163], v[84:87]\n\t" \
  "ds_read_b128 a[188:191], v204 offset:6656\n\t" \
  "v_and_b32_e32 v191, 0xffff0000, v147\n\t" \
  "v_mul_f32_e32 v188, v188, v190\n\t" \
  "v_mfma_f32_16x16x32_bf16 v[88:91], a[120:123], v[160:163], v[88:91]\n\t" \
  "ds_read_b128 a[174:177], v204 offset:3072\n\t" \
  "v_mul_f32_e32 v189, v189, v191\n\t" \
  "v_cvt_pk_bf16_f32 v165, v188, v189\n\t" \
  "v_mfma_f32_16x16x32_bf16 v[92:95], a[138:141], v[160:163], v[92:95]\n\t" \
  "ds_read_b128 a[192:195], v204 offset:7680\n\t" \
  "s_waitcnt lgkmcnt(4)\n\t" \
  "v_mfma_f32_16x16x32_bf16 v[96:99], a[156:159], v[160:163], v[96:99]\n\t" \
  "ds_read_b64 a[178:179], v205 offset:4096\n\t" \
  "ds_read_b64 a[196:197], v205 offset:8704\n\t" \
  "v_mfma_f32_16x16x16_bf16 v[64:67], a[16:17], v[164:165], v[64:67]\n\t" \
  "ds_read_b128 a[198:201], v204 offset:9216\n\t" \
  "v_mfma_f32_16x16x16_bf16 v[68:71], a[34:35], v[164:165], v[68:71]\n\t" \
  "ds_read_b128 a[216:219], v204 offset:13824\n\t" \
  "v_mfma_f32_16x16x16_bf16 v[72:75], a[52:53], v[164:165], v[72:75]\n\t" \
  "ds_read_b128 a[202:205], v204 offset:10240\n\t" \
  "v_mfma_f32_16x16x16_bf16 v[76:79], a[70:71], v[164:165], v[76:79]\n\t" \
  "ds_read_b128 a[220:223], v204 offset:14848\n\t" \
  "v_mfma_f32_16x16x16_bf16 v[80:83], a[88:89], v[164:165], v[80:83]\n\t" \
  "ds_read_b128 a[206:209], v204 offset:11264\n\t" \
  "v_mfma_f32_16x16x16_bf16 v[84:87], a[106:107], v[164:165], v[84:87]\n\t" \
  "ds_read_b128 a[224:227], v204 offset:15872\n\t" \
  "ds_write_b128 v34, v[64:67] offset:0\n\t" \
  "v_mfma_f32_16x16x16_bf16 v[88:91], a[124:125], v[164:165], v[88:91]\n\t" \
  "ds_read_b128 a[210:213], v204 offset:12288\n\t" \
  "ds_write_b128 v34, v[68:71] offset:1024\n\t" \
  "v_mfma_f32_16x16x16_bf16 v[92:95], a[142:143], v[164:165], v[92:95]\n\t" \
  "ds_read_b128 a[228:231], v204 offset:16896\n\t" \
  "ds_write_b128 v34, v[72:75] offset:2048\n\t" \
  "v_mfma_f32_16x16x16_bf16 v[96:99], a[160:161], v[164:165], v[96:99]\n\t" \
  "ds_read_b64 a[214:215], v205 offset:13312\n\t" \
  "ds_read_b64 a[232:233], v205 offset:17920\n\t" \
  "ds_write_b128 v34, v[76:79] offset:3072\n\t" \
  "v_add_u32_e32 v207, s86, v63\n\t" \
  "s_cmp_eq_u32 s47, 0\n\t" \
  "SC%=:\n\t" \
  "s_cbranch_scc1 KC%=\n\t" \
  "ds_read_b128 a[0:3], v12 offset:0\n\t" \
  "ds_read_b128 a[18:21], v13 offset:0\n\t" \
  "ds_read_b128 a[36:39], v14 offset:4096\n\t" \
  "ds_read_b128 a[4:7], v12 offset:1024\n\t" \
  "ds_read_b128 a[22:25], v13 offset:1024\n\t" \
  "ds_read_b128 a[40:43], v14 offset:5120\n\t" \
  "ds_read_b128 a[8:11], v12 offset:2048\n\t" \
  "ds_read_b128 a[26:29], v13 offset:2048\n\t" \
  "ds_read_b128 a[44:47], v14 offset:6144\n\t" \
  "ds_read_b128 a[12:15], v12 offset:3072\n\t" \
  "ds_read_b128 a[30:33], v13 offset:3072\n\t" \
  "ds_read_b128 a[48:51], v14 offset:7168\n\t" \
  "ds_read_b64 a[16:17], v12 offset:8192\n\t" \
  "ds_read_b64 a[34:35], v13 offset:8192\n\t" \
  "ds_read_b64 a[52:53], v14 offset:8200\n\t" \
  "v_mfma_f32_16x16x16_bf16 v[80:83], v[30:31], v[22:23], 0\n\t" \
  "ds_read_b128 a[162:165], v204 offset:0\n\t" \
  "v_mfma_f32_16x16x16_bf16 v[84:87], v[30:31], v[24:25], 0\n\t" \
  "ds_read_b128 a[180:183], v204 offset:4608\n\t" \
  "v_mfma_f32_16x16x16_bf16 v[88:91], v[30:31], v[26:27], 0\n\t" \
  "ds_read_b128 a[166:169], v204 offset:1024\n\t" \
  "v_mfma_f32_16x16x16_bf16 v[92:95], v[30:31], v[28:29], 0\n\t" \
  "ds_read_b128 a[184:187], v204 offset:5632\n\t" \
  "s_waitcnt lgkmcnt(10)\n\t" \
  "v_mfma_f32_16x16x16_bf16 v[220:223], v[30:31], a[0:1], v[220:223]\n\t" \
  "ds_read_b128 a[170:173], v204 offset:2048\n\t" \
  "v_mfma_f32_16x16x16_bf16 v[220:223], v[30:31], a[18:19], v[220:223]\n\t" \
  "ds_read_b128 a[188:191], v204 offset:6656\n\t" \
  "v_mfma_f32_16x16x16_bf16 v[220:223], v[30:31], a[36:37], v[220:223]\n\t" \
  "ds_read_b128 a[174:177], v204 offset:3072\n\t" \
  "v_mfma_f32_16x16x16_bf16 v[224:227], v[30:31], a[2:3], v[224:227]\n\t" \
  "ds_read_b128 a[192:195], v204 offset:7680\n\t" \
  "v_mfma_f32_16x16x16_bf16 v[224:227], v[30:31], a[20:21], v[224:227]\n\t" \
  "ds_read_b64 a[178:179], v205 offset:4096\n\t" \
  "v_mfma_f32_16x16x16_bf16 v[224:227], v[30:31], a[38:39], v[224:227]\n\t" \
  "ds_read_b64 a[196:197], v205 offset:8704\n\t" \
  "v_mfma_f32_16x16x16_bf16 v[228:231], v[30:31], a[4:5], v[228:231]\n\t" \
  "ds_read_b128 a[198:201], v204 offset:9216\n\t" \
  "v_mfma_f32_16x16x16_bf16 v[228:231], v[30:31], a[22:23], v[228:231]\n\t" \
  "ds_read_b128 a[216:219], v204 offset:13824\n\t" \
  "v_mfma_f32_16x16x16_bf16 v[228:231], v[30:31], a[40:41], v[228:231]\n\t" \
  "ds_read_b128 a[202:205], v204 offset:10240\n\t" \
  "v_mfma_f32_16x16x16_bf16 v[232:235], v[30:31], a[6:7], v[232:235]\n\t" \
  "ds_read_b128 a[220:223], v204 offset:14848\n\t" \
  "v_mfma_f32_16x16x16_bf16 v[232:235], v[30:31], a[24:25], v[232:235]\n\t" \
  "ds_read_b128 a[206:209], v204 offset:11264\n\t" \
  "v_mfma_f32_16x16x16_bf16 v[232:235], v[30:31], a[42:43], v[232:235]\n\t" \
  "ds_read_b128 a[224:227], v204 offset:15872\n\t" \
  "v_mfma_f32_16x16x16_bf16 v[236:239], v[30:31], a[8:9], v[236:239]\n\t" \
  "ds_read_b128 a[210:213], v204 offset:12288\n\t" \
  "v_mfma_f32_16x16x16_bf16 v[236:239], v[30:31], a[26:27], v[236:239]\n\t" \
  "ds_read_b128 a[228:231], v204 offset:16896\n\t" \
  "v_mfma_f32_16x16x16_bf16 v[236:239], v[30:31], a[44:45], v[236:239]\n\t" \
  "ds_read_b64 a[214:215], v205 offset:13312\n\t" \
  "v_mfma_f32_16x16x16_bf16 v[240:243], v[30:31], a[10:11], v[240:243]\n\t" \
  "ds_read_b64 a[232:233], v205 offset:17920\n\t" \
  "v_mfma_f32_16x16x16_bf16 v[240:243], v[30:31], a[28:29], v[240:243]\n\t" \
  "v_add_u32_e32 v207, s55, v63\n\t" \
  "v_mfma_f32_16x16x16_bf16 v[240:243], v[30:31], a[46:47], v[240:243]\n\t" \
  "s_waitcnt lgkmcnt(6)\n\t" \
  "v_mfma_f32_16x16x16_bf16 v[244:247], v[30:31], a[12:13], v[244:247]\n\t" \
  "v_mfma_f32_16x16x16_bf16 v[244:247], v[30:31], a[30:31], v[244:247]\n\t" \
  "v_mfma_f32_16x16x16_bf16 v[244:247], v[30:31], a[48:49], v[244:247]\n\t" \
  "v_mfma_f32_16x16x16_bf16 v[248:251], v[30:31], a[14:15], v[248:251]\n\t" \
  "v_mfma_f32_16x16x16_bf16 v[248:251], v[30:31], a[32:33], v[248:251]\n\t" \
  "v_mfma_f32_16x16x16_bf16 v[248:251], v[30:31], a[50:51], v[248:251]\n\t" \
  "v_mfma_f32_16x16x16_bf16 v[252:255], v[30:31], a[16:17], v[252:255]\n\t" \
  "v_mfma_f32_16x16x16_bf16 v[252:255], v[30:31], a[34:35], v[252:255]\n\t" \
  "v_mfma_f32_16x16x16_bf16 v[252:255], v[30:31], a[52:53], v[252:255]\n\t" \
  "s_waitcnt vmcnt(0)\n\t" \
  "v_and_b32_e32 v202, v167, v169\n\t" \
  "v_or_b32_e32 v203, v167, v169\n\t" \
  "v_bitop3_b32 v202, v202, v171, v173 bitop3:0x80\n\t" \
  "v_or3_b32 v203, v203, v171, v173\n\t" \
  "v_bitop3_b32 v202, v202, v175, v177 bitop3:0x80\n\t" \
  "v_or3_b32 v203, v203, v175, v177\n\t" \
  "v_bitop3_b32 v202, v202, v179, v181 bitop3:0x80\n\t" \
  "v_or3_b32 v203, v203, v179, v181\n\t" \
  "v_bitop3_b32 v202, v202, v183, v185 bitop3:0x80\n\t" \
  "v_or3_b32 v203, v203, v183, v185\n\t" \
  "v_bitop3_b32 v202, v202, v187, v189 bitop3:0x80\n\t" \
  "v_or3_b32 v203, v203, v187, v189\n\t" \
  "v_bitop3_b32 v202, v202, v191, v193 bitop3:0x80\n\t" \
  "v_or3_b32 v203, v203, v191, v193\n\t" \
  "v_bitop3_b32 v202, v202, v195, v197 bitop3:0x80\n\t" \
  "v_or3_b32 v203, v203, v195, v197\n\t" \
  "v_bitop3_b32 v202, v202, v199, v201 bitop3:0x80\n\t" \
  "v_or3_b32 v203, v203, v199, v201\n\t" \
  "v_xor_b32_e32 v202, s92, v202\n\t" \
  "v_xor_b32_e32 v203, s92, v203\n\t" \
  "v_or_b32_e32 v202, v202, v203\n\t" \
  "v_cmp_ne_u32_e32 vcc, 0, v202\n\t" \
  "s_nop 0\n\t" \
  "s_cbranch_vccnz POLLC%=\n\t" \
  "BACKC%=:\n\t" \
  "v_dot2_f32_bf16 v220, v166, s40, v220\n\t" \
  "v_dot2_f32_bf16 v221, v166, s41, v221\n\t" \
  "v_dot2_f32_bf16 v222, v168, s40, v222\n\t" \
  "v_dot2_f32_bf16 v223, v168, s41, v223\n\t" \
  "v_dot2_f32_bf16 v224, v170, s40, v224\n\t" \
  "v_dot2_f32_bf16 v225, v170, s41, v225\n\t" \
  "v_dot2_f32_bf16 v226, v172, s40, v226\n\t" \
  "v_dot2_f32_bf16 v227, v172, s41, v227\n\t" \
  "v_dot2_f32_bf16 v228, v174, s40, v228\n\t" \
  "v_dot2_f32_bf16 v229, v174, s41, v229\n\t" \
  "v_dot2_f32_bf16 v230, v176, s40, v230\n\t" \
  "v_dot2_f32_bf16 v231, v176, s41, v231\n\t" \
  "v_dot2_f32_bf16 v232, v178, s40, v232\n\t" \
  "v_dot2_f32_bf16 v233, v178, s41, v233\n\t" \
  "v_dot2_f32_bf16 v234, v180, s40, v234\n\t" \
  "v_dot2_f32_bf16 v235, v180, s41, v235\n\t" \
  "v_dot2_f32_bf16 v236, v182, s40, v236\n\t" \
  "v_dot2_f32_bf16 v237, v182, s41, v237\n\t" \
  "v_dot2_f32_bf16 v238, v184, s40, v238\n\t" \
  "v_dot2_f32_bf16 v239, v184, s41, v239\n\t" \
  "v_dot2_f32_bf16 v240, v186, s40, v240\n\t" \
  "v_dot2_f32_bf16 v241, v186, s41, v241\n\t" \
  "v_dot2_f32_bf16 v242, v188, s40, v242\n\t" \
  "v_dot2_f32_bf16 v243, v188, s41, v243\n\t" \
  "v_dot2_f32_bf16 v244, v190, s40, v244\n\t" \
  "v_dot2_f32_bf16 v245, v190, s41, v245\n\t" \
  "v_dot2_f32_bf16 v246, v192, s40, v246\n\t" \
  "v_dot2_f32_bf16 v247, v192, s41, v247\n\t" \
  "v_dot2_f32_bf16 v248, v194, s40, v248\n\t" \
  "v_dot2_f32_bf16 v249, v194, s41, v249\n\t" \
  "v_dot2_f32_bf16 v250, v196, s40, v250\n\t" \
  "v_dot2_f32_bf16 v251, v196, s41, v251\n\t" \
  "v_dot2_f32_bf16 v252, v198, s40, v252\n\t" \
  "v_dot2_f32_bf16 v253, v198, s41, v253\n\t" \
  "v_dot2_f32_bf16 v254, v200, s40, v254\n\t" \
  "v_dot2_f32_bf16 v255, v200, s41, v255\n\t" \
  "NORM%=:\n\t" \
  "v_pk_add_f32 v[160:161], v[220:221], v[222:223]\n\t" \
  "v_pk_add_f32 v[162:163], v[224:225], v[226:227]\n\t" \
  "v_pk_add_f32 v[160:161], v[160:161], v[228:229]\n\t" \
  "v_pk_add_f32 v[162:163], v[162:163], v[230:231]\n\t" \
  "v_pk_add_f32 v[160:161], v[160:161], v[232:233]\n\t" \
  "v_pk_add_f32 v[162:163], v[162:163], v[234:235]\n\t" \
  "v_pk_add_f32 v[160:161], v[160:161], v[236:237]\n\t" \
  "v_pk_add_f32 v[162:163], v[162:163], v[238:239]\n\t" \
  "v_pk_add_f32 v[160:161], v[160:161], v[240:241]\n\t" \
  "v_pk_add_f32 v[162:163], v[162:163], v[242:243]\n\t" \
  "v_pk_add_f32 v[160:161], v[160:161], v[244:245]\n\t" \
  "v_pk_add_f32 v[162:163], v[162:163], v[246:247]\n\t" \
  "v_pk_add_f32 v[160:161], v[160:161], v[248:249]\n\t" \
  "v_pk_add_f32 v[162:163], v[162:163], v[250:251]\n\t" \
  "v_pk_add_f32 v[160:161], v[160:161], v[252:253]\n\t" \
  "v_pk_add_f32 v[162:163], v[162:163], v[254:255]\n\t" \
  "v_pk_add_f32 v[160:161], v[160:161], v[162:163]\n\t" \
  "v_add_f32_e32 v164, v160, v161\n\t" \
  "v_mov_b32_e32 v165, v164\n\t" \
  "s_nop 1\n\t" \
  "v_permlane32_swap_b32_e32 v164, v165\n\t" \
  "v_add_f32_e32 v164, v164, v165\n\t" \
  "v_mov_b32_e32 v165, v164\n\t" \
  "s_nop 1\n\t" \
  "v_permlane16_swap_b32_e32 v164, v165\n\t" \
  "v_add_f32_e32 v164, v164, v165\n\t" \
  "v_mul_f32_e32 v164, 0x3c638e39, v164\n\t" \
  "v_mov_b32_e32 v162, 0x71800000\n\t" \
  "v_cmp_nlt_f32_e32 vcc, 0x21800000, v164\n\t" \
  "v_cmp_nlt_f32_e64 s[94:95], v164, v162\n\t" \
  "v_rcp_f32_e32 v164, v164\n\t" \
  "s_or_b64 vcc, vcc, s[94:95]\n\t" \
  "s_and_b64 vcc, vcc, s[24:25]\n\t" \
  "s_cbranch_vccnz SLOW%=\n\t" \
  "v_pk_fma_f32 v[220:221], v[220:221], v[164:165], s[36:37] op_sel_hi:[1,0,1]\n\t" \
  "v_pk_fma_f32 v[222:223], v[222:223], v[164:165], s[36:37] op_sel_hi:[1,0,1]\n\t" \
  "v_cvt_pk_bf16_f32 v112, v220, v221\n\t" \
  "v_pk_fma_f32 v[224:225], v[224:225], v[164:165], s[36:37] op_sel_hi:[1,0,1]\n\t" \
  "v_cvt_pk_bf16_f32 v113, v222, v223\n\t" \
  "v_pk_fma_f32 v[226:227], v[226:227], v[164:165], s[36:37] op_sel_hi:[1,0,1]\n\t" \
  "v_cvt_pk_bf16_f32 v114, v224, v225\n\t" \
  "v_pk_fma_f32 v[228:229], v[228:229], v[164:165], s[36:37] op_sel_hi:[1,0,1]\n\t" \
  "v_cvt_pk_bf16_f32 v115, v226, v227\n\t" \
  "ds_write_b128 v34, v[112:115] offset:0\n\t" \
  "v_pk_fma_f32 v[230:231], v[230:231], v[164:165], s[36:37] op_sel_hi:[1,0,1]\n\t" \
  "v_cvt_pk_bf16_f32 v116, v228, v229\n\t" \
  "v_pk_fma_f32 v[232:233], v[232:233], v[164:165], s[36:37] op_sel_hi:[1,0,1]\n\t" \
  "v_cvt_pk_bf16_f32 v117, v230, v231\n\t" \
  "v_pk_fma_f32 v[234:235], v[234:235], v[164:165], s[36:37] op_sel_hi:[1,0,1]\n\t" \
  "v_cvt_pk_bf16_f32 v118, v232, v233\n\t" \
  "v_pk_fma_f32 v[236:237], v[236:237], v[164:165], s[36:37] op_sel_hi:[1,0,1]\n\t" \
  "v_cvt_pk_bf16_f32 v119, v234, v235\n\t" \
  "ds_write_b128 v34, v[116:119] offset:1024\n\t" \
  "v_pk_fma_f32 v[238:239], v[238:239], v[164:165], s[36:37] op_sel_hi:[1,0,1]\n\t" \
  "v_cvt_pk_bf16_f32 v120, v236, v237\n\t" \
  "v_pk_fma_f32 v[240:241], v[240:241], v[164:165], s[36:37] op_sel_hi:[1,0,1]\n\t" \
  "v_cvt_pk_bf16_f32 v121, v238, v239\n\t" \
  "v_pk_fma_f32 v[242:243], v[242:243], v[164:165], s[36:37] op_sel_hi:[1,0,1]\n\t" \
  "v_cvt_pk_bf16_f32 v122, v240, v241\n\t" \
  "v_pk_fma_f32 v[244:245], v[244:245], v[164:165], s[36:37] op_sel_hi:[1,0,1]\n\t" \
  "v_cvt_pk_bf16_f32 v123, v242, v243\n\t" \
  "ds_write_b128 v34, v[120:123] offset:2048\n\t" \
  "v_pk_fma_f32 v[246:247], v[246:247], v[164:165], s[36:37] op_sel_hi:[1,0,1]\n\t" \
  "v_cvt_pk_bf16_f32 v124, v244, v245\n\t" \
  "v_pk_fma_f32 v[248:249], v[248:249], v[164:165], s[36:37] op_sel_hi:[1,0,1]\n\t" \
  "v_cvt_pk_bf16_f32 v125, v246, v247\n\t" \
  "v_pk_fma_f32 v[250:251], v[250:251], v[164:165], s[36:37] op_sel_hi:[1,0,1]\n\t" \
  "v_cvt_pk_bf16_f32 v126, v248, v249\n\t" \
  "v_pk_fma_f32 v[252:253], v[252:253], v[164:165], s[36:37] op_sel_hi:[1,0,1]\n\t" \
  "v_cvt_pk_bf16_f32 v127, v250, v251\n\t" \
  "ds_write_b128 v34, v[124:127] offset:3072\n\t" \
  "v_pk_fma_f32 v[254:255], v[254:255], v[164:165], s[36:37] op_sel_hi:[1,0,1]\n\t" \
  "v_cvt_pk_bf16_f32 v128, v252, v253\n\t" \
  "v_cvt_pk_bf16_f32 v129, v254, v255\n\t" \
  "ds_write_b64 v207, v[128:129] offset:4096\n\t" \
  "KC%=:\n\t" \
  "s_waitcnt lgkmcnt(0)\n\t" \
  "s_barrier\n\t" \
  "s_sub_u32 s94, s20, s67\n\t" \
  "s_sub_u32 s95, s21, s67\n\t" \
  "v_add_u32_e32 v140, s94, v2\n\t" \
  "v_add_u32_e32 v141, s95, v3\n\t" \
  "v_add_u32_e32 v142, s87, v59\n\t" \
  "s_cmp_eq_u32 s47, 0\n\t" \
  "s_cbranch_scc0 SD%=\n\t" \
  "ds_read_b128 v[112:115], v206 offset:0\n\t" \
  "ds_read_b128 v[116:119], v206 offset:1024\n\t" \
  "ds_read_b128 v[120:123], v206 offset:2048\n\t" \
  "ds_read_b128 v[124:127], v206 offset:3072\n\t" \
  "ds_read_b64 v[128:129], v207 offset:4096\n\t" \
  "s_waitcnt lgkmcnt(4)\n\t" \
  "v_mfma_f32_16x16x32_bf16 v[80:83], a[162:165], v[112:115], v[80:83]\n\t" \
  "v_mfma_f32_16x16x32_bf16 v[84:87], a[180:183], v[112:115], v[84:87]\n\t" \
  "s_waitcnt lgkmcnt(3)\n\t" \
  "v_mfma_f32_16x16x32_bf16 v[80:83], a[166:169], v[116:119], v[80:83]\n\t" \
  "v_mfma_f32_16x16x32_bf16 v[84:87], a[184:187], v[116:119], v[84:87]\n\t" \
  "s_waitcnt lgkmcnt(2)\n\t" \
  "v_mfma_f32_16x16x32_bf16 v[80:83], a[170:173], v[120:123], v[80:83]\n\t" \
  "v_mfma_f32_16x16x32_bf16 v[84:87], a[188:191], v[120:123], v[84:87]\n\t" \
  "s_waitcnt lgkmcnt(1)\n\t" \
  "v_mfma_f32_16x16x32_bf16 v[80:83], a[174:177], v[124:127], v[80:83]\n\t" \
  "v_mfma_f32_16x16x32_bf16 v[84:87], a[192:195], v[124:127], v[84:87]\n\t" \
  "s_waitcnt lgkmcnt(0)\n\t" \
  "v_mfma_f32_16x16x16_bf16 v[80:83], a[178:179], v[128:129], v[80:83]\n\t" \
  "v_mfma_f32_16x16x16_bf16 v[84:87], a[196:197], v[128:129], v[84:87]\n\t" \
  "v_mfma_f32_16x16x32_bf16 v[88:91], a[198:201], v[112:115], v[88:91]\n\t" \
  "v_mfma_f32_16x16x32_bf16 v[92:95], a[216:219], v[112:115], v[92:95]\n\t" \
  "v_mfma_f32_16x16x32_bf16 v[96:99], a[240:243], v[112:115], v[96:99]\n\t" \
  "v_mfma_f32_16x16x32_bf16 v[88:91], a[202:205], v[116:119], v[88:91]\n\t" \
  "v_cvt_pk_bf16_f32 v130, v80, v81\n\t" \
  "v_cvt_pk_bf16_f32 v131, v82, v83\n\t" \
  "v_cvt_pk_bf16_f32 v132, v84, v85\n\t" \
  "v_cvt_pk_bf16_f32 v133, v86, v87\n\t" \
  "v_mfma_f32_16x16x32_bf16 v[92:95], a[220:223], v[116:119], v[92:95]\n\t" \
  "ds_write_b128 v140, v[130:133] offset:0\n\t" \
  "v_mfma_f32_16x16x32_bf16 v[96:99], a[244:247], v[116:119], v[96:99]\n\t" \
  "v_mfma_f32_16x16x32_bf16 v[88:91], a[206:209], v[120:123], v[88:91]\n\t" \
  "v_mfma_f32_16x16x32_bf16 v[92:95], a[224:227], v[120:123], v[92:95]\n\t" \
  "v_mfma_f32_16x16x32_bf16 v[96:99], a[248:251], v[120:123], v[96:99]\n\t" \
  "v_mfma_f32_16x16x32_bf16 v[88:91], a[210:213], v[124:127], v[88:91]\n\t" \
  "v_mfma_f32_16x16x32_bf16 v[92:95], a[228:231], v[124:127], v[92:95]\n\t" \
  "v_mfma_f32_16x16x32_bf16 v[96:99], a[252:255], v[124:127], v[96:99]\n\t" \
  "v_mfma_f32_16x16x16_bf16 v[88:91], a[214:215], v[128:129], v[88:91]\n\t" \
  "v_mfma_f32_16x16x16_bf16 v[92:95], a[232:233], v[128:129], v[92:95]\n\t" \
  "v_mfma_f32_16x16x16_bf16 v[96:99], v[46:47], v[128:129], v[96:99]\n\t" \
  "v_cvt_pk_bf16_f32 v134, v88, v89\n\t" \
  "v_cvt_pk_bf16_f32 v135, v90, v91\n\t" \
  "s_nop 1\n\t" \
  "v_cvt_pk_bf16_f32 v136, v92, v93\n\t" \
  "v_cvt_pk_bf16_f32 v137, v94, v95\n\t" \
  "ds_write_b128 v140, v[134:137] offset:64\n\t" \
  "s_nop 0\n\t" \
  "v_cvt_pk_bf16_f32 v138, v96, v97\n\t" \
  "v_cvt_pk_bf16_f32 v139, v98, v99\n\t" \
  "ds_write_b64 v141, v[138:139]\n\t" \
  "SD%=:\n\t" \
  "s_cbranch_scc1 KD%=\n\t" \
  "ds_read_b128 v[64:67], v206 offset:0\n\t" \
  "ds_read_b128 v[68:71], v206 offset:1024\n\t" \
  "ds_read_b128 v[72:75], v206 offset:2048\n\t" \
  "ds_read_b128 v[76:79], v206 offset:3072\n\t" \
  "v_mfma_f32_16x16x32_bf16 v[80:83], a[162:165], v[112:115], v[80:83]\n\t" \
  "v_mfma_f32_16x16x32_bf16 v[84:87], a[180:183], v[112:115], v[84:87]\n\t" \
  "v_mfma_f32_16x16x32_bf16 v[80:83], a[166:169], v[116:119], v[80:83]\n\t" \
  "v_mfma_f32_16x16x32_bf16 v[84:87], a[184:187], v[116:119], v[84:87]\n\t" \
  "v_mfma_f32_16x16x32_bf16 v[80:83], a[170:173], v[120:123], v[80:83]\n\t" \
  "v_mfma_f32_16x16x32_bf16 v[84:87], a[188:191], v[120:123], v[84:87]\n\t" \
  "v_mfma_f32_16x16x32_bf16 v[80:83], a[174:177], v[124:127], v[80:83]\n\t" \
  "v_mfma_f32_16x16x32_bf16 v[84:87], a[192:195], v[124:127], v[84:87]\n\t" \
  "v_mfma_f32_16x16x16_bf16 v[80:83], a[178:179], v[128:129], v[80:83]\n\t" \
  "v_mfma_f32_16x16x16_bf16 v[84:87], a[196:197], v[128:129], v[84:87]\n\t" \
  "v_mfma_f32_16x16x32_bf16 v[88:91], a[198:201], v[112:115], v[88:91]\n\t" \
  "v_mfma_f32_16x16x32_bf16 v[92:95], a[216:219], v[112:115], v[92:95]\n\t" \
  "v_mfma_f32_16x16x32_bf16 v[88:91], a[202:205], v[116:119], v[88:91]\n\t" \
  "v_mfma_f32_16x16x32_bf16 v[92:95], a[220:223], v[116:119], v[92:95]\n\t" \
  "v_mfma_f32_16x16x32_bf16 v[88:91], a[206:209], v[120:123], v[88:91]\n\t" \
  "v_mfma_f32_16x16x32_bf16 v[92:95], a[224:227], v[120:123], v[92:95]\n\t" \
  "v_mfma_f32_16x16x32_bf16 v[88:91], a[210:213], v[124:127], v[88:91]\n\t" \
  "v_mfma_f32_16x16x32_bf16 v[92:95], a[228:231], v[124:127], v[92:95]\n\t" \
  "v_mfma_f32_16x16x16_bf16 v[88:91], a[214:215], v[128:129], v[88:91]\n\t" \
  "v_mfma_f32_16x16x16_bf16 v[92:95], a[232:233], v[128:129], v[92:95]\n\t" \
  "s_waitcnt lgkmcnt(0)\n\t" \
  "v_pk_add_f32 v[80:81], v[80:81], v[64:65]\n\t" \
  "v_pk_add_f32 v[82:83], v[82:83], v[66:67]\n\t" \
  "v_pk_add_f32 v[84:85], v[84:85], v[68:69]\n\t" \
  "v_pk_add_f32 v[86:87], v[86:87], v[70:71]\n\t" \
  "v_pk_add_f32 v[88:89], v[88:89], v[72:73]\n\t" \
  "v_pk_add_f32 v[90:91], v[90:91], v[74:75]\n\t" \
  "s_nop 0\n\t" \
  "v_pk_add_f32 v[92:93], v[92:93], v[76:77]\n\t" \
  "v_pk_add_f32 v[94:95], v[94:95], v[78:79]\n\t" \
  "v_cvt_pk_bf16_f32 v130, v80, v81\n\t" \
  "v_cvt_pk_bf16_f32 v131, v82, v83\n\t" \
  "v_cvt_pk_bf16_f32 v132, v84, v85\n\t" \
  "v_cvt_pk_bf16_f32 v133, v86, v87\n\t" \
  "ds_write_b128 v140, v[130:133] offset:0\n\t" \
  "v_cvt_pk_bf16_f32 v134, v88, v89\n\t" \
  "v_cvt_pk_bf16_f32 v135, v90, v91\n\t" \
  "v_cvt_pk_bf16_f32 v136, v92, v93\n\t" \
  "v_cvt_pk_bf16_f32 v137, v94, v95\n\t" \
  "ds_write_b128 v140, v[134:137] offset:64\n\t" \
  "KD%=:\n\t" \
  "s_waitcnt lgkmcnt(0)\n\t" \
  "s_barrier\n\t" \
  "ds_read_b128 v[144:147], v142 offset:0\n\t" \
  "ds_read_b128 v[148:151], v142 offset:1024\n\t" \
  "ds_read_b128 v[152:155], v142 offset:2048\n\t" \
  "s_cmp_eq_u32 s89, 3\n\t" \
  "s_cselect_b32 s94, s67, 1879048192\n\t" \
  "s_waitcnt lgkmcnt(2)\n\t" \
  "buffer_store_dwordx4 v[144:147], v142, s[8:11], s67 offen offset:0 sc0 sc1 nt\n\t" \
  "s_waitcnt lgkmcnt(1)\n\t" \
  "buffer_store_dwordx4 v[148:151], v142, s[8:11], s67 offen offset:1024 sc0 sc1 nt\n\t" \
  "s_waitcnt lgkmcnt(0)\n\t" \
  "buffer_store_dwordx4 v[152:155], v142, s[8:11], s94 offen offset:2048 sc0 sc1 nt\n\t" \
  "s_endpgm\n\t" \
  "POLLM%=:\n\t" \
  "s_mov_b32 s98, 65536\n\t" \
  "POLLM2%=:\n\t" \
  "s_sleep 2\n\t" \
  "buffer_load_dwordx2 v[60:61], v255, s[16:19], 0 offen sc1\n\t" \
  "s_waitcnt vmcnt(0)\n\t" \
  "v_cmp_ne_u64_e32 vcc, s[92:93], v[60:61]\n\t" \
  "s_add_i32 s98, s98, -1\n\t" \
  "s_cmp_eq_u32 s98, 0\n\t" \
  "s_cbranch_scc1 BACKM%=\n\t" \
  "s_cbranch_vccnz POLLM2%=\n\t" \
  "s_branch BACKM%=\n\t" \
  "POLLE%=:\n\t" \
  "s_mov_b32 s98, 65536\n\t" \
  "POLLE2%=:\n\t" \
  "s_sleep 2\n\t" \
  "buffer_load_dwordx2 v[60:61], v252, s[16:19], 0 offen sc1\n\t" \
  "s_waitcnt vmcnt(0)\n\t" \
  "v_cmp_ne_u64_e32 vcc, s[92:93], v[60:61]\n\t" \
  "s_add_i32 s98, s98, -1\n\t" \
  "s_cmp_eq_u32 s98, 0\n\t" \
  "s_cbranch_scc1 BACKE%=\n\t" \
  "s_cbranch_vccnz POLLE2%=\n\t" \
  "s_branch BACKE%=\n\t" \
  "POLLA%=:\n\t" \
  "s_mov_b32 s98, 65536\n\t" \
  "POLLA2%=:\n\t" \
  "s_sleep 2\n\t" \
  "buffer_load_dwordx2 v[60:61], v253, s[16:19], 0 offen sc1\n\t" \
  "s_waitcnt vmcnt(0)\n\t" \
  "v_cmp_ne_u64_e32 vcc, s[92:93], v[60:61]\n\t" \
  "s_add_i32 s98, s98, -1\n\t" \
  "s_cmp_eq_u32 s98, 0\n\t" \
  "s_cbranch_scc1 BACKA%=\n\t" \
  "s_cbranch_vccnz POLLA2%=\n\t" \
  "s_branch BACKA%=\n\t" \
  "POLLB%=:\n\t" \
  "s_mov_b32 s98, 65536\n\t" \
  "POLLB2%=:\n\t" \
  "s_sleep 2\n\t" \
  "buffer_load_dwordx2 v[60:61], v32, s[16:19], 0 offen sc1\n\t" \
  "s_waitcnt vmcnt(0)\n\t" \
  "v_cmp_ne_u64_e32 vcc, s[92:93], v[60:61]\n\t" \
  "s_add_i32 s98, s98, -1\n\t" \
  "s_cmp_eq_u32 s98, 0\n\t" \
  "s_cbranch_scc1 BACKB%=\n\t" \
  "s_cbranch_vccnz POLLB2%=\n\t" \
  "s_branch BACKB%=\n\t" \
  "POLLC%=:\n\t" \
  "s_mov_b32 s98, 65536\n\t" \
  "POLLC2%=:\n\t" \
  "s_sleep 2\n\t" \
  "buffer_load_dwordx4 v[166:169], v59, s[16:19], s22 offen offset:0 sc1\n\t" \
  "buffer_load_dwordx4 v[170:173], v59, s[16:19], s22 offen offset:1024 sc1\n\t" \
  "buffer_load_dwordx4 v[174:177], v59, s[16:19], s22 offen offset:2048 sc1\n\t" \
  "buffer_load_dwordx4 v[178:181], v59, s[16:19], s22 offen offset:3072 sc1\n\t" \
  "buffer_load_dwordx4 v[182:185], v59, s[16:19], s23 offen offset:0 sc1\n\t" \
  "buffer_load_dwordx4 v[186:189], v59, s[16:19], s23 offen offset:1024 sc1\n\t" \
  "buffer_load_dwordx4 v[190:193], v59, s[16:19], s23 offen offset:2048 sc1\n\t" \
  "buffer_load_dwordx4 v[194:197], v59, s[16:19], s23 offen offset:3072 sc1\n\t" \
  "buffer_load_dwordx4 v[198:201], v59, s[16:19], s26 offen offset:0 sc1\n\t" \
  "s_waitcnt vmcnt(0)\n\t" \
  "v_and_b32_e32 v202, v167, v169\n\t" \
  "v_or_b32_e32 v203, v167, v169\n\t" \
  "v_bitop3_b32 v202, v202, v171, v173 bitop3:0x80\n\t" \
  "v_or3_b32 v203, v203, v171, v173\n\t" \
  "v_bitop3_b32 v202, v202, v175, v177 bitop3:0x80\n\t" \
  "v_or3_b32 v203, v203, v175, v177\n\t" \
  "v_bitop3_b32 v202, v202, v179, v181 bitop3:0x80\n\t" \
  "v_or3_b32 v203, v203, v179, v181\n\t" \
  "v_bitop3_b32 v202, v202, v183, v185 bitop3:0x80\n\t" \
  "v_or3_b32 v203, v203, v183, v185\n\t" \
  "v_bitop3_b32 v202, v202, v187, v189 bitop3:0x80\n\t" \
  "v_or3_b32 v203, v203, v187, v189\n\t" \
  "v_bitop3_b32 v202, v202, v191, v193 bitop3:0x80\n\t" \
  "v_or3_b32 v203, v203, v191, v193\n\t" \
  "v_bitop3_b32 v202, v202, v195, v197 bitop3:0x80\n\t" \
  "v_or3_b32 v203, v203, v195, v197\n\t" \
  "v_bitop3_b32 v202, v202, v199, v201 bitop3:0x80\n\t" \
  "v_or3_b32 v203, v203, v199, v201\n\t" \
  "v_xor_b32_e32 v202, s92, v202\n\t" \
  "v_xor_b32_e32 v203, s92, v203\n\t" \
  "v_or_b32_e32 v202, v202, v203\n\t" \
  "v_cmp_ne_u32_e32 vcc, 0, v202\n\t" \
  "s_add_i32 s98, s98, -1\n\t" \
  "s_cmp_eq_u32 s98, 0\n\t" \
  "s_cbranch_scc1 BACKC%=\n\t" \
  "s_cbranch_vccnz POLLC2%=\n\t" \
  "s_branch BACKC%=\n\t" \
  "CODE_END%=:\n\t" \
  "POLLD%=:\n\t" \
  "s_mov_b32 s98, 65536\n\t" \
  "POLLD2%=:\n\t" \
  "s_sleep 2\n\t" \
  "buffer_load_dwordx2 v[134:135], v132, s[16:19], 0 offen sc1\n\t" \
  "s_waitcnt vmcnt(0)\n\t" \
  "v_cmp_ne_u64_e32 vcc, s[92:93], v[134:135]\n\t" \
  "s_add_i32 s98, s98, -1\n\t" \
  "s_cmp_eq_u32 s98, 0\n\t" \
  "s_cbranch_scc1 BACKD%=\n\t" \
  "s_cbranch_vccnz POLLD2%=\n\t" \
  "s_branch BACKD%=\n\t" \
  "SLOW%=:\n\t" \
  "s_mov_b64 s[24:25], 0\n\t" \
  "s_lshl_b32 s94, s3, 15\n\t" \
  "s_add_u32 s94, s94, 4096\n\t" \
  "s_mov_b32 s97, 0\n\t" \
  "SLOWP%=:\n\t" \
  "v_add_u32_e32 v125, s97, v0\n\t" \
  "v_min_u32_e32 v125, 249, v125\n\t" \
  "v_lshl_add_u32 v132, v125, 7, s94\n\t" \
  "s_branch POLLD%=\n\t" \
  "BACKD%=:\n\t" \
  "s_add_u32 s97, s97, 64\n\t" \
  "s_cmp_lt_u32 s97, 256\n\t" \
  "s_cbranch_scc1 SLOWP%=\n\t" \
  "s_mul_i32 s96, s3, 1161216\n\t" \
  "s_add_u32 s96, s96, 131072\n\t" \
  "s_mov_b32 s95, s55\n\t" \
  "s_mov_b32 s97, 125\n\t" \
  "v_add_u32_e32 v129, s95, v17\n\t" \
  "v_add_u32_e32 v130, s95, v18\n\t" \
  "v_add_u32_e32 v131, s95, v19\n\t" \
  "v_mov_b32_e32 v124, 0xff61b1e6\n\t" \
  "v_mov_b32_e32 v220, 0\n\t" \
  "v_mov_b32_e32 v221, 0\n\t" \
  "v_mov_b32_e32 v222, 0\n\t" \
  "v_mov_b32_e32 v223, 0\n\t" \
  "v_mov_b32_e32 v224, 0\n\t" \
  "v_mov_b32_e32 v225, 0\n\t" \
  "v_mov_b32_e32 v226, 0\n\t" \
  "v_mov_b32_e32 v227, 0\n\t" \
  "v_mov_b32_e32 v228, 0\n\t" \
  "v_mov_b32_e32 v229, 0\n\t" \
  "v_mov_b32_e32 v230, 0\n\t" \
  "v_mov_b32_e32 v231, 0\n\t" \
  "v_mov_b32_e32 v232, 0\n\t" \
  "v_mov_b32_e32 v233, 0\n\t" \
  "v_mov_b32_e32 v234, 0\n\t" \
  "v_mov_b32_e32 v235, 0\n\t" \
  "v_mov_b32_e32 v236, 0\n\t" \
  "v_mov_b32_e32 v237, 0\n\t" \
  "v_mov_b32_e32 v238, 0\n\t" \
  "v_mov_b32_e32 v239, 0\n\t" \
  "v_mov_b32_e32 v240, 0\n\t" \
  "v_mov_b32_e32 v241, 0\n\t" \
  "v_mov_b32_e32 v242, 0\n\t" \
  "v_mov_b32_e32 v243, 0\n\t" \
  "v_mov_b32_e32 v244, 0\n\t" \
  "v_mov_b32_e32 v245, 0\n\t" \
  "v_mov_b32_e32 v246, 0\n\t" \
  "v_mov_b32_e32 v247, 0\n\t" \
  "v_mov_b32_e32 v248, 0\n\t" \
  "v_mov_b32_e32 v249, 0\n\t" \
  "v_mov_b32_e32 v250, 0\n\t" \
  "v_mov_b32_e32 v251, 0\n\t" \
  "v_mov_b32_e32 v252, 0\n\t" \
  "v_mov_b32_e32 v253, 0\n\t" \
  "v_mov_b32_e32 v254, 0\n\t" \
  "v_mov_b32_e32 v255, 0\n\t" \
  "SLOWL%=:\n\t" \
  "s_add_u32 m0, s95, 0\n\t" \
  "s_nop 0\n\t" \
  "buffer_load_dwordx4 v59, s[16:19], s96 offen offset:0 sc1 lds\n\t" \
  "buffer_load_dwordx4 v59, s[16:19], s96 offen offset:1024 sc1 lds\n\t" \
  "buffer_load_dwordx4 v59, s[16:19], s96 offen offset:2048 sc1 lds\n\t" \
  "buffer_load_dwordx4 v59, s[16:19], s96 offen offset:3072 sc1 lds\n\t" \
  "s_add_u32 m0, s95, 4096\n\t" \
  "s_nop 0\n\t" \
  "buffer_load_dwordx4 v57, s[16:19], s96 offen offset:0 sc1 lds\n\t" \
  "buffer_load_dwordx4 v57, s[16:19], s96 offen offset:1024 sc1 lds\n\t" \
  "buffer_load_dwordx4 v57, s[16:19], s96 offen offset:2048 sc1 lds\n\t" \
  "buffer_load_dwordx4 v57, s[16:19], s96 offen offset:3072 sc1 lds\n\t" \
  "s_add_u32 m0, s95, 8192\n\t" \
  "s_nop 0\n\t" \
  "buffer_load_dwordx4 v58, s[16:19], s96 offen offset:0 sc1 lds\n\t" \
  "s_waitcnt vmcnt(0)\n\t" \
  "ds_read_b128 a[72:75], v129 offset:0\n\t" \
  "ds_read_b128 a[76:79], v129 offset:2048\n\t" \
  "ds_read_b128 a[80:83], v129 offset:4096\n\t" \
  "ds_read_b128 a[84:87], v129 offset:6144\n\t" \
  "ds_read_b128 a[88:91], v129 offset:1024\n\t" \
  "ds_read_b128 a[92:95], v129 offset:3072\n\t" \
  "ds_read_b128 a[96:99], v129 offset:5120\n\t" \
  "ds_read_b128 a[100:103], v129 offset:7168\n\t" \
  "ds_read2st64_b64 a[104:107], v131 offset0:16 offset1:17\n\t" \
  "ds_read_b64_tr_b16 a[108:109], v130 offset:0\n\t" \
  "ds_read_b64_tr_b16 a[110:111], v130 offset:1024\n\t" \
  "ds_read_b64_tr_b16 a[112:113], v130 offset:512\n\t" \
  "ds_read_b64_tr_b16 a[114:115], v130 offset:1536\n\t" \
  "ds_read_b64_tr_b16 a[116:117], v130 offset:2048\n\t" \
  "ds_read_b64_tr_b16 a[118:119], v130 offset:3072\n\t" \
  "ds_read_b64_tr_b16 a[120:121], v130 offset:2560\n\t" \
  "ds_read_b64_tr_b16 a[122:123], v130 offset:3584\n\t" \
  "ds_read_b64_tr_b16 a[124:125], v130 offset:4096\n\t" \
  "ds_read_b64_tr_b16 a[126:127], v130 offset:5120\n\t" \
  "ds_read_b64_tr_b16 a[128:129], v130 offset:4608\n\t" \
  "ds_read_b64_tr_b16 a[130:131], v130 offset:5632\n\t" \
  "ds_read_b64_tr_b16 a[132:133], v130 offset:6144\n\t" \
  "ds_read_b64_tr_b16 a[134:135], v130 offset:7168\n\t" \
  "ds_read_b64_tr_b16 a[136:137], v130 offset:6656\n\t" \
  "ds_read_b64_tr_b16 a[138:139], v130 offset:7680\n\t" \
  "ds_read_b64_tr_b16 a[140:141], v130 offset:8192\n\t" \
  "ds_read_b64_tr_b16 a[142:143], v130 offset:8704\n\t" \
  "s_waitcnt lgkmcnt(14)\n\t" \
  "v_mfma_f32_16x16x32_bf16 v[112:115], a[72:75], a[54:57], 0\n\t" \
  "v_mfma_f32_16x16x32_bf16 v[116:119], a[88:91], a[54:57], 0\n\t" \
  "v_mfma_f32_16x16x32_bf16 v[112:115], a[76:79], a[58:61], v[112:115]\n\t" \
  "v_mfma_f32_16x16x32_bf16 v[116:119], a[92:95], a[58:61], v[116:119]\n\t" \
  "v_mfma_f32_16x16x32_bf16 v[112:115], a[80:83], a[62:65], v[112:115]\n\t" \
  "v_mfma_f32_16x16x32_bf16 v[116:119], a[96:99], a[62:65], v[116:119]\n\t" \
  "v_mfma_f32_16x16x32_bf16 v[112:115], a[84:87], a[66:69], v[112:115]\n\t" \
  "v_mfma_f32_16x16x32_bf16 v[116:119], a[100:103], a[66:69], v[116:119]\n\t" \
  "v_mfma_f32_16x16x16_bf16 v[112:115], a[104:105], a[70:71], v[112:115]\n\t" \
  "v_mfma_f32_16x16x16_bf16 v[116:119], a[106:107], a[70:71], v[116:119]\n\t" \
  "s_nop 3\n\t" \
  "v_max3_f32 v125, v112, v113, v114\n\t" \
  "s_nop 2\n\t" \
  "v_max3_f32 v125, v125, v115, v116\n\t" \
  "v_max3_f32 v125, v125, v117, v118\n\t" \
  "v_max3_f32 v125, v125, v119, v124\n\t" \
  "v_mov_b32_e32 v128, v125\n\t" \
  "s_nop 1\n\t" \
  "v_permlane32_swap_b32_e32 v125, v128\n\t" \
  "v_max_f32_e32 v125, v125, v128\n\t" \
  "v_mov_b32_e32 v128, v125\n\t" \
  "s_nop 1\n\t" \
  "v_permlane16_swap_b32_e32 v125, v128\n\t" \
  "v_max_f32_e32 v125, v125, v128\n\t" \
  "v_sub_f32_e32 v126, v124, v125\n\t" \
  "v_exp_f32_e32 v126, v126\n\t" \
  "v_mov_b32_e32 v124, v125\n\t" \
  "v_sub_f32_e32 v112, v112, v125\n\t" \
  "v_sub_f32_e32 v113, v113, v125\n\t" \
  "v_sub_f32_e32 v114, v114, v125\n\t" \
  "v_sub_f32_e32 v115, v115, v125\n\t" \
  "v_sub_f32_e32 v116, v116, v125\n\t" \
  "v_sub_f32_e32 v117, v117, v125\n\t" \
  "v_sub_f32_e32 v118, v118, v125\n\t" \
  "v_sub_f32_e32 v119, v119, v125\n\t" \
  "v_exp_f32_e32 v112, v112\n\t" \
  "v_exp_f32_e32 v113, v113\n\t" \
  "v_exp_f32_e32 v114, v114\n\t" \
  "v_exp_f32_e32 v115, v115\n\t" \
  "v_exp_f32_e32 v116, v116\n\t" \
  "v_exp_f32_e32 v117, v117\n\t" \
  "v_exp_f32_e32 v118, v118\n\t" \
  "v_exp_f32_e32 v119, v119\n\t" \
  "v_cvt_pk_bf16_f32 v120, v112, v113\n\t" \
  "v_cvt_pk_bf16_f32 v121, v114, v115\n\t" \
  "v_cvt_pk_bf16_f32 v122, v116, v117\n\t" \
  "v_cvt_pk_bf16_f32 v123, v118, v119\n\t" \
  "v_pk_mul_f32 v[220:221], v[220:221], v[126:127] op_sel_hi:[1,0]\n\t" \
  "v_pk_mul_f32 v[222:223], v[222:223], v[126:127] op_sel_hi:[1,0]\n\t" \
  "v_pk_mul_f32 v[224:225], v[224:225], v[126:127] op_sel_hi:[1,0]\n\t" \
  "v_pk_mul_f32 v[226:227], v[226:227], v[126:127] op_sel_hi:[1,0]\n\t" \
  "v_pk_mul_f32 v[228:229], v[228:229], v[126:127] op_sel_hi:[1,0]\n\t" \
  "v_pk_mul_f32 v[230:231], v[230:231], v[126:127] op_sel_hi:[1,0]\n\t" \
  "v_pk_mul_f32 v[232:233], v[232:233], v[126:127] op_sel_hi:[1,0]\n\t" \
  "v_pk_mul_f32 v[234:235], v[234:235], v[126:127] op_sel_hi:[1,0]\n\t" \
  "v_pk_mul_f32 v[236:237], v[236:237], v[126:127] op_sel_hi:[1,0]\n\t" \
  "v_pk_mul_f32 v[238:239], v[238:239], v[126:127] op_sel_hi:[1,0]\n\t" \
  "v_pk_mul_f32 v[240:241], v[240:241], v[126:127] op_sel_hi:[1,0]\n\t" \
  "v_pk_mul_f32 v[242:243], v[242:243], v[126:127] op_sel_hi:[1,0]\n\t" \
  "v_pk_mul_f32 v[244:245], v[244:245], v[126:127] op_sel_hi:[1,0]\n\t" \
  "v_pk_mul_f32 v[246:247], v[246:247], v[126:127] op_sel_hi:[1,0]\n\t" \
  "v_pk_mul_f32 v[248:249], v[248:249], v[126:127] op_sel_hi:[1,0]\n\t" \
  "v_pk_mul_f32 v[250:251], v[250:251], v[126:127] op_sel_hi:[1,0]\n\t" \
  "v_pk_mul_f32 v[252:253], v[252:253], v[126:127] op_sel_hi:[1,0]\n\t" \
  "v_pk_mul_f32 v[254:255], v[254:255], v[126:127] op_sel_hi:[1,0]\n\t" \
  "v_mfma_f32_16x16x32_bf16 v[220:223], a[108:111], v[120:123], v[220:223]\n\t" \
  "v_mfma_f32_16x16x32_bf16 v[224:227], a[112:115], v[120:123], v[224:227]\n\t" \
  "s_waitcnt lgkmcnt(0)\n\t" \
  "v_mfma_f32_16x16x32_bf16 v[228:231], a[116:119], v[120:123], v[228:231]\n\t" \
  "v_mfma_f32_16x16x32_bf16 v[232:235], a[120:123], v[120:123], v[232:235]\n\t" \
  "v_mfma_f32_16x16x32_bf16 v[236:239], a[124:127], v[120:123], v[236:239]\n\t" \
  "v_mfma_f32_16x16x32_bf16 v[240:243], a[128:131], v[120:123], v[240:243]\n\t" \
  "v_mfma_f32_16x16x32_bf16 v[244:247], a[132:135], v[120:123], v[244:247]\n\t" \
  "v_mfma_f32_16x16x32_bf16 v[248:251], a[136:139], v[120:123], v[248:251]\n\t" \
  "v_mfma_f32_16x16x32_bf16 v[252:255], a[140:143], v[120:123], v[252:255]\n\t" \
  "s_add_u32 s96, s96, 9216\n\t" \
  "s_add_i32 s97, s97, -1\n\t" \
  "s_cmp_eq_u32 s97, 0\n\t" \
  "s_nop 7\n\t" \
  "s_cbranch_scc0 SLOWL%=\n\t" \
  "s_branch NORM%=\n\t" \
  "LEAVE%=:\n\t"
#define MAIN_CLOBBERS "v64","v65","v66","v67","v68","v69","v70","v71","v72","v73","v74","v75","v76","v77","v78","v79","v80","v81","v82","v83","v84","v85","v86","v87","v88","v89","v90","v91","v92","v93","v94","v95","v96","v97","v98","v99","v100","v101","v102","v103","v104","v105","v106","v107","v108","v109","v110","v111","v112","v113","v114","v115","v116","v117","v118","v119","v120","v121","v122","v123","v124","v125","v126","v127","v128","v129","v130","v131","v132","v133","v134","v135","v136","v137","v138","v139","v140","v141","v142","v143","v144","v145","v146","v147","v148","v149","v150","v151","v152","v153","v154","v155","v156","v157","v158","v159","v160","v161","v162","v163","v164","v165","v166","v167","v168","v169","v170","v171","v172","v173","v174","v175","v176","v177","v178","v179","v180","v181","v182","v183","v184","v185","v186","v187","v188","v189","v190","v191","v192","v193","v194","v195","v196","v197","v198","v199","v200","v201","v202","v203","v204","v205","v206","v207","v208","v209","v210","v211","v212","v213","v214","v215","v216","v217","v218","v219","v220","v221","v222","v223","v224","v225","v226","v227","v228","v229","v230","v231","v232","v233","v234","v235","v236","v237","v238","v239","v240","v241","v242","v243","v244","v245","v246","v247","v248","v249","v250","v251","v252","v253","v254","v255","a0","a1","a2","a3","a4","a5","a6","a7","a8","a9","a10","a11","a12","a13","a14","a15","a16","a17","a18","a19","a20","a21","a22","a23","a24","a25","a26","a27","a28","a29","a30","a31","a32","a33","a34","a35","a36","a37","a38","a39","a40","a41","a42","a43","a44","a45","a46","a47","a48","a49","a50","a51","a52","a53","a54","a55","a56","a57","a58","a59","a60","a61","a62","a63","a64","a65","a66","a67","a68","a69","a70","a71","a72","a73","a74","a75","a76","a77","a78","a79","a80","a81","a82","a83","a84","a85","a86","a87","a88","a89","a90","a91","a92","a93","a94","a95","a96","a97","a98","a99","a100","a101","a102","a103","a104","a105","a106","a107","a108","a109","a110","a111","a112","a113","a114","a115","a116","a117","a118","a119","a120","a121","a122","a123","a124","a125","a126","a127","a128","a129","a130","a131","a132","a133","a134","a135","a136","a137","a138","a139","a140","a141","a142","a143","a144","a145","a146","a147","a148","a149","a150","a151","a152","a153","a154","a155","a156","a157","a158","a159","a160","a161","a162","a163","a164","a165","a166","a167","a168","a169","a170","a171","a172","a173","a174","a175","a176","a177","a178","a179","a180","a181","a182","a183","a184","a185","a186","a187","a188","a189","a190","a191","a192","a193","a194","a195","a196","a197","a198","a199","a200","a201","a202","a203","a204","a205","a206","a207","a208","a209","a210","a211","a212","a213","a214","a215","a216","a217","a218","a219","a220","a221","a222","a223","a224","a225","a226","a227","a228","a229","a230","a231","a232","a233","a234","a235","a236","a237","a238","a239","a240","a241","a242","a243","a244","a245","a246","a247","a248","a249","a250","a251","a252","a253","a254","a255","s36","s37","s38","s39","s40","s41","s42","s43","s44","s45","s46","s47","s48","s49","s50","s51","s52","s53","s54","s55","s56","s57","s58","s59","s60","s61","s62","s63","s64","s65","s66","s67","s68","s69","s70","s71","s72","s73","s74","s75","s76","s77","s78","s79","s80","s81","s82","s83","s84","s85","s86","s87","s88","s89","s90","s91","s92","s93","s94","s95","s96","s97","s98","s99","vcc","scc","memory"
constexpr unsigned WS_WF = 1024, WS_ZF = 4096, WS_WD = 9476096, WS_WP = 9517568, WS_TW = 9559040;
constexpr unsigned WS_G = 2453504, WS_END = 9641984;
#define ARG_CONSTS {0xbdd2d3e8u, 0xbdd2d3e8u, 0x3f800000u, 0x3f800000u, 0xbf000000u, 0xbf000000u, 0x3df6384fu, 0x3df6384fu, 0x3f80u, 0x3f800000u, 0x3f803f80u, 0xc0135761u}
#define ARG_TABLE {0u, 0u, 1u, 1u, 0u, 0u, 0u, 0u, 0u, 4096u, 8192u, 36864u, 40960u, 45056u, 73728u, 77824u, 81920u, 64512u, 55296u, 46080u, 36864u, 110592u, 118784u, 0u, 9559040u, 9577472u, 9595904u, 8192u, 12288u, 0u, 161280u, 0u, 82944u, 92160u, 101376u, 77824u, 77824u, 87040u, 59392u, 68608u, 79872u, 0u, 55296u, 0u, 4096u, 0u, 0u, 0u, 8u, 0u, 1u, 2u, 9216u, 10240u, 18432u, 1024u, 9216u, 13312u, 17408u, 46080u, 50176u, 54272u, 82944u, 87040u, 91136u, 55296u, 64512u, 36864u, 46080u, 110624u, 118816u, 15u, 9563648u, 9582080u, 1888644096u, 1879056384u, 16896u, 5120u, 161408u, 0u, 73728u, 101376u, 92160u, 87040u, 77856u, 87072u, 59424u, 68640u, 79904u, 0u, 64512u, 2048u, 5120u, 1u, 0u, 1u, 0u, 1u, 0u, 0u, 18432u, 20480u, 36864u, 2048u, 18432u, 22528u, 26624u, 55296u, 59392u, 63488u, 92160u, 96256u, 100352u, 64512u, 55296u, 46080u, 36864u, 114688u, 118784u, 30u, 9568256u, 9586688u, 9554432u, 1879056384u, 21504u, 0u, 161536u, 128u, 64512u, 36864u, 46080u, 59392u, 78848u, 88064u, 60416u, 69632u, 79872u, 18432u, 36864u, 4096u, 6144u, 2u, 2048u, 0u, 8u, 1u, 0u, 0u, 27648u, 30208u, 55296u, 3072u, 27648u, 31744u, 35840u, 64512u, 68608u, 72704u, 101376u, 105472u, 109568u, 55296u, 64512u, 36864u, 46080u, 114720u, 118816u, 45u, 9572864u, 9591296u, 9554432u, 1879056384u, 26112u, 5120u, 161664u, 128u, 55296u, 46080u, 36864u, 68608u, 78880u, 88096u, 60448u, 69664u, 79904u, 18432u, 46080u, 6144u, 7168u, 3u, 2048u, 1u, 0u, 0u, 0u, 0u, 0u, 0u, 0u, 0u, 0u, 4096u, 8192u, 36864u, 40960u, 45056u, 73728u, 77824u, 81920u, 46080u, 36864u, 64512u, 55296u, 110592u, 118784u, 60u, 9559040u, 9577472u, 9554432u, 1879056384u, 12288u, 0u, 161280u, 0u, 46080u, 55296u, 64512u, 40960u, 40960u, 50176u, 96256u, 105472u, 98304u, 0u, 55296u, 0u, 4096u, 0u, 0u, 0u, 8u, 0u, 0u, 0u, 9216u, 10240u, 18432u, 1024u, 9216u, 13312u, 17408u, 46080u, 50176u, 54272u, 82944u, 87040u, 91136u, 36864u, 46080u, 55296u, 64512u, 110624u, 118816u, 75u, 9563648u, 9582080u, 9554432u, 1879056384u, 16896u, 5120u, 161408u, 0u, 36864u, 64512u, 55296u, 50176u, 40992u, 50208u, 96288u, 105504u, 98336u, 0u, 64512u, 2048u, 5120u, 1u, 0u, 1u, 0u, 1u, 1u, 1u, 18432u, 20480u, 36864u, 2048u, 18432u, 22528u, 26624u, 55296u, 59392u, 63488u, 92160u, 96256u, 100352u, 46080u, 36864u, 64512u, 55296u, 114688u, 118784u, 90u, 9568256u, 9586688u, 9595904u, 8192u, 21504u, 0u, 161536u, 128u, 101376u, 73728u, 82944u, 96256u, 41984u, 51200u, 97280u, 106496u, 98304u, 18432u, 36864u, 4096u, 6144u, 2u, 2048u, 0u, 8u, 1u, 1u, 2u, 27648u, 30208u, 55296u, 3072u, 27648u, 31744u, 35840u, 64512u, 68608u, 72704u, 101376u, 105472u, 109568u, 36864u, 46080u, 55296u, 64512u, 114720u, 118816u, 105u, 9572864u, 9591296u, 1888644096u, 1879056384u, 26112u, 5120u, 161664u, 128u, 92160u, 82944u, 73728u, 105472u, 42016u, 51232u, 97312u, 106528u, 98336u, 18432u, 46080u, 6144u, 7168u, 3u, 2048u, 1u}
#define HELPER_SHIFT 2


#define DI __device__ __forceinline__
#define LAS __attribute__((address_space(3)))
#define GAS __attribute__((address_space(1)))
typedef unsigned u32;
typedef unsigned long long u64;
typedef u32 u32x2 __attribute__((ext_vector_type(2)));
typedef u32 u32x4 __attribute__((ext_vector_type(4)));
typedef float f32x2 __attribute__((ext_vector_type(2)));
typedef float f32x4 __attribute__((ext_vector_type(4)));
typedef short s16x4 __attribute__((ext_vector_type(4)));
typedef __bf16 bf16x2 __attribute__((ext_vector_type(2)));
typedef __bf16 bf16x8 __attribute__((ext_vector_type(8)));
typedef LAS char* lptr;
typedef __amdgpu_buffer_rsrc_t rsrc_t;

constexpr int TT = 4000, ROWB = 288, WB = 41472, FRB = 4608;
constexpr u32 WS_EPOCH = 0;
constexpr u32 SPIN_MAX = 1u << 16;

DI u32 rfl(u32 x) { return __builtin_amdgcn_readfirstlane(x); }
DI rsrc_t mk_rsrc(const void* p, u32 bytes) { return __builtin_amdgcn_make_buffer_rsrc((void*)p, (short)0, (int)bytes, 0x00020000); }
DI u32 pk(float lo, float hi) { f32x2 v = {lo, hi}; return __builtin_bit_cast(u32, __builtin_convertvector(v, bf16x2)); }
DI f32x4 mfma(u32x4 a, u32x4 b, f32x4 c) { return __builtin_amdgcn_mfma_f32_16x16x32_bf16(__builtin_bit_cast(bf16x8, a), __builtin_bit_cast(bf16x8, b), c, 0, 0, 0); }
DI u32x2 trd(lptr p) { return __builtin_bit_cast(u32x2, __builtin_amdgcn_ds_read_tr16_b64_v4i16((LAS s16x4*)p)); }
DI u32x2 ld64(lptr p) { return *(LAS u32x2*)p; }
#define WAITV(n) asm volatile("s_waitcnt vmcnt(" #n ")" ::: "memory")
#define BAR() asm volatile("s_waitcnt lgkmcnt(0)\n\ts_barrier" ::: "memory")
#define DMA1(ldsaddr, voff, rs, soff, AUX) asm volatile("s_mov_b32 m0, %0\n\ts_nop 0\n\tbuffer_load_dwordx4 %1, %2, %3 offen " AUX " lds" :: "s"(ldsaddr), "v"(voff), "s"(rs), "s"(soff) : "memory")
#define DMA4(ldsaddr, voff, rs, soff, AUX) asm volatile("s_mov_b32 m0, %0\n\ts_nop 0\n\tbuffer_load_dwordx4 %1, %2, %3 offen " AUX " lds\n\tbuffer_load_dwordx4 %1, %2, %3 offen offset:1024 " AUX " lds\n\t" \
    "buffer_load_dwordx4 %1, %2, %3 offen offset:2048 " AUX " lds\n\tbuffer_load_dwordx4 %1, %2, %3 offen offset:3072 " AUX " lds" :: "s"(ldsaddr), "v"(voff), "s"(rs), "s"(soff) : "memory")
#define DMA_QUARTER(ldsbase, rs, w, l16, AUX) do { const u32 o_ = (w) == 3 ? (u32)(WB - 11 * 1024) : (w) * 10240u; \
    DMA4((ldsbase) + o_, l16, rs, o_, AUX); DMA4((ldsbase) + o_ + 4096, l16, rs, o_ + 4096, AUX); \
    _Pragma("unroll") for (int k_ = 8; k_ < 11; ++k_) DMA1((ldsbase) + o_ + k_ * 1024, l16, rs, o_ + k_ * 1024, AUX); } while (0)
#define SB() __builtin_amdgcn_sched_barrier(0)
DI void flag_set(char* ws, u32 off, u64 tag) { __hip_atomic_store((GAS u64*)(ws + off), tag, __ATOMIC_RELAXED, __HIP_MEMORY_SCOPE_AGENT); }
DI u64 flag_get(char* ws, u32 off) { return __hip_atomic_load((GAS u64*)(ws + off), __ATOMIC_RELAXED, __HIP_MEMORY_SCOPE_AGENT); }
DI void poll(char* ws, u32 off, u64 tag) {
  for (u32 s = 0; s < SPIN_MAX; ++s) {
    if (__all(flag_get(ws, off) == tag)) break;
    __builtin_amdgcn_s_sleep(4);
  }
  asm volatile("" ::: "memory");
}
struct WFrag { u32x2 r[9]; };
DI WFrag frag_tr(lptr wp) {
  WFrag f;
#pragma unroll
  for (int i = 0; i < 9; ++i) f.r[i] = trd(wp + i * FRB);
  return f;
}
template <bool SWAP> DI f32x4 tile_mm(const WFrag& f, const u32x4 (&xf)[5]) {
  f32x4 acc = {0.f, 0.f, 0.f, 0.f};
#pragma unroll
  for (int i = 0; i < 5; ++i) {
    const u32x4 wf = i < 4 ? u32x4{f.r[2 * i][0], f.r[2 * i][1], f.r[2 * i + 1][0], f.r[2 * i + 1][1]} : u32x4{f.r[8][0], f.r[8][1], 0u, 0u};
    acc = SWAP ? mfma(xf[i], wf, acc) : mfma(wf, xf[i], acc);
  }
  return acc;
}
DI void pack_tiles(const f32x4 (&t)[9], u32x4 (&f)[5]) {
#pragma unroll
  for (int i = 0; i < 4; ++i) f[i] = u32x4{pk(t[2 * i][0], t[2 * i][1]), pk(t[2 * i][2], t[2 * i][3]), pk(t[2 * i + 1][0], t[2 * i + 1][1]), pk(t[2 * i + 1][2], t[2 * i + 1][3])};
  f[4] = u32x4{pk(t[8][0], t[8][1]), pk(t[8][2], t[8][3]), 0u, 0u};
}

DI u32 lane_id() { u32 l; asm volatile("v_mbcnt_lo_u32_b32 %0, -1, 0\n\tv_mbcnt_hi_u32_b32 %0, -1, %0" : "=v"(l)); return l; }
DI void helper(int h, u32 w, lptr lds, const char* wv, const char* wao, const char* dw, const char* wdn, const char* wm, char* ws) {
  const u32 lane = lane_id(), n = lane & 15, g = lane >> 4, q4 = (lane >> 2) & 3, p4 = lane & 3, l16 = lane * 16;
  const u32 lb = (u32)(size_t)lds;
  const bool post = h < 2;
  DMA_QUARTER(lb + WB, mk_rsrc(wm + (post ? 0 : WB), WB), w, l16, "");
  DMA_QUARTER(lb + 2 * WB, mk_rsrc(post ? wv : wdn, WB), w, l16, "");
  if (post) DMA_QUARTER(lb, mk_rsrc(wao, WB), w, l16, "");
  if (h & 1) DMA4(lb + 3 * WB + w * 4096, l16, mk_rsrc(dw, 31 * ROWB), w * 4096, "");
  const u64 tag = flag_get(ws, WS_EPOCH) + 1;
  WAITV(0); BAR();
  if ((h & 1) && w > 0) {
    const rsrc_t rt = mk_rsrc(ws + WS_TW, WB);
#pragma unroll 1
    for (u32 grp = (h >> 1) * 3 + w - 1; grp < 9; grp += 6) {
      u32 v[36];
#pragma unroll
      for (int k = 0; k < 36; ++k) { const u32 tau = (u32)(k - (int)p4 - 1); const u32 x = *(LAS unsigned short*)(lds + 3 * WB + min(tau, 31u) * ROWB + grp * 32 + (lane >> 2) * 2); v[k] = x; }
#pragma unroll
      for (int k = 0; k < 4; ++k)
        __builtin_amdgcn_raw_buffer_store_b128(u32x4{v[8 * k] | (v[8 * k + 1] << 16), v[8 * k + 2] | (v[8 * k + 3] << 16), v[8 * k + 4] | (v[8 * k + 5] << 16), v[8 * k + 6] | (v[8 * k + 7] << 16)}, rt, grp * FRB + k * 1024 + l16, 0, 16);
      __builtin_amdgcn_raw_buffer_store_b64(u32x2{v[32] | (v[33] << 16), v[34] | (v[35] << 16)}, rt, grp * FRB + 4096 + lane * 8, 0, 16);
    }
  } else {
    const rsrc_t ro = mk_rsrc(ws + (post ? WS_WP : WS_WD), WB);
    u32x4 xs[2][5];
#pragma unroll
    for (int u = 0; u < 2; ++u) {
      const u32 row = (h & 1) ? 128 + n : post ? 32 * w + 16 * ((n >> 2) & 1) + 8 * u + 4 * (n >> 3) + (n & 3) : 32 * w + 16 * u + n;
      const lptr src = lds + 2 * WB + row * ROWB + g * 8;
#pragma unroll
      for (int k = 0; k < 4; ++k) { const u32x2 a = ld64(src + k * 64), c = ld64(src + k * 64 + 32); xs[u][k] = u32x4{a[0], a[1], c[0], c[1]}; }
      { const u32x2 a = ld64(src + 256); xs[u][4] = u32x4{a[0], a[1], 0u, 0u}; }
    }
    const lptr wl = lds + (4 * g + q4) * ROWB;
    if (post) {
      f32x4 t0[9], t1[9];
      WFrag f = frag_tr(wl + p4 * 8);
#pragma unroll
      for (int E = 0; E < 9; ++E) { const WFrag c = f; if (E < 8) f = frag_tr(wl + p4 * 8 + (E + 1) * 32); SB(); t0[E] = tile_mm<false>(c, xs[0]); t1[E] = tile_mm<false>(c, xs[1]); SB(); }
      pack_tiles(t0, xs[0]); pack_tiles(t1, xs[1]);
    }
    WFrag f = frag_tr(wl + WB + p4 * 16);
    f32x4 ta[9], tb[9];
#pragma unroll
    for (int E = 0; E < 9; ++E) {
      const WFrag c = f;
      if (E < 8) f = frag_tr(wl + WB + (E < 7 ? ((E + 1) >> 1) * 64 + p4 * 16 + ((E + 1) & 1) * 8 : 256 + p4 * 8));
      SB(); ta[E] = tile_mm<true>(c, xs[0]); tb[E] = tile_mm<true>(c, xs[1]); SB();
    }
    if (h & 1) {
#pragma unroll
      for (int E = 0; E < 9; ++E) __builtin_amdgcn_raw_buffer_store_b64(u32x2{pk(ta[E][0], ta[E][1]), pk(ta[E][2], ta[E][3])}, ro, lane * 8, E * FRB + 4096, 16);
    } else {
#pragma unroll
      for (int E = 0; E < 9; ++E) __builtin_amdgcn_raw_buffer_store_b128(u32x4{pk(ta[E][0], ta[E][1]), pk(ta[E][2], ta[E][3]), pk(tb[E][0], tb[E][1]), pk(tb[E][2], tb[E][3])}, ro, w * 1024 + l16, E * FRB, 16);
    }
  }
  asm volatile("BF_HELPER_END:");
  WAITV(0);
  if (lane == 0) flag_set(ws, WS_WF + (h * 4 + w) * 8, tag);
  if (h == 0 && w == 0) {
    poll(ws, WS_WF + n * 8, tag);
#pragma unroll 1
    for (int k = 0; k < 4; ++k) { const u32 t = (k & 1) * 64 + lane; poll(ws, WS_ZF + ((k >> 1) * 128 + (t < 126 ? t : 0)) * 256, tag); }
    if (lane == 0) flag_set(ws, WS_EPOCH, tag);
  }
}

struct Srd { const void* p; u32 bytes, fmt; };
struct Args {
  Srd per_batch[2][4];
  Srd wq, wk, wup; u32 consts[12];
  u32 table[8 * 48];
  const char *wv, *wao, *dw, *wdn, *wm; char* ws;
};
static_assert(sizeof(Args) == 128 + 96 + 1536 + 48, "no padding");

__global__ __launch_bounds__(256, 1) void branchformer_fused(Args a) {
  extern __shared__ __attribute__((aligned(16))) char smem[];
  u32 bid = blockIdx.x, tid = threadIdx.x;
  u64 ka = (u64)__builtin_amdgcn_kernarg_segment_ptr();
  asm volatile(MAIN_ASM : "+{s[0:1]}"(ka) : "{s2}"(bid), "{v0}"(tid) : MAIN_CLOBBERS);
  const __attribute__((address_space(4))) Args* ap = (const __attribute__((address_space(4))) Args*)ka;
  helper((bid >> HELPER_SHIFT) & 3, rfl(tid >> 6), (lptr)smem, ap->wv, ap->wao, ap->dw, ap->wdn, ap->wm, ap->ws);
}

extern "C" void kernel_launch(void* const* d_in, const int* in_sizes, int n_in, void* d_out, int out_size, void* d_ws, size_t ws_size, hipStream_t stream) {
  (void)in_sizes; (void)n_in; (void)out_size; (void)ws_size;
  (void)hipFuncSetAttribute((const void*)branchformer_fused, hipFuncAttributeMaxDynamicSharedMemorySize, 163840);
  const u32 F = 0x00020000u, NB = TT * ROWB;
  char* ws = (char*)d_ws;
  Args a = {{{{(char*)d_in[0], NB, F}, {(char*)d_out, NB, F}, {ws + WS_G, NB, F}, {ws, WS_END, F}},
             {{(char*)d_in[0] + NB, NB, F}, {(char*)d_out + NB, NB, F}, {ws + WS_G + NB, NB, F}, {ws, WS_END, F}}},
            {d_in[1], (u32)WB, F}, {d_in[2], (u32)WB, F}, {d_in[5], 2u * WB, F}, ARG_CONSTS, ARG_TABLE,
            (const char*)d_in[3], (const char*)d_in[4], (const char*)d_in[6], (const char*)d_in[7], (const char*)d_in[8], ws};
  branchformer_fused<<<256, 256, 163840, stream>>>(a);
}
```
